```python
import torch

def _softmax(x, axis=-1):
    return torch.softmax(x, dim=axis)
def _gelu(x):
    return torch.nn.functional.gelu(x, approximate="tanh")
def _silu(x):
    return torch.nn.functional.silu(x)
def _sigmoid(x):
    return torch.sigmoid(x)
def _layernorm(x, axis=-1, eps=1e-5):
    m = x.mean(dim=axis, keepdim=True)
    v = x.var(dim=axis, keepdim=True, unbiased=False)
    return (x - m) / torch.sqrt(v + eps)
def _scatter_add(tgt, idx, src, axis=0):
    return tgt.clone().index_add_(axis, idx, src)
def _segment_sum(src, seg, n):
    out = torch.zeros((n,) + src.shape[1:], dtype=src.dtype)
    return out.index_add_(0, seg, src)
def _pad(x, pw):
    flat = tuple(v for pair in reversed(pw) for v in pair)
    return torch.nn.functional.pad(x, flat)

kernel_name = 'sequential_preLN__compressed_kv_cla__mixer_token_mlp__prompt'

def setup_inputs():

    _w = (lambda a: (lambda t: t.to(torch.bfloat16) if t.is_floating_point() else t)(torch.as_tensor(a)))
    def _A_setup():
        B, H, Sq, Sk, D, M = 1, 32, 2048, 2048, 128, 640
        Dc = 4 * D
        return {
            "x": _w((lambda *s: torch.randn(*s, dtype=torch.bfloat16) * 0.05)(B, Sq, M)),
            "c_kv": _w((lambda *s: torch.randn(*s, dtype=torch.bfloat16) * 0.2)(B, Sk, Dc)),
            "w_q": _w((lambda *s: torch.randn(*s, dtype=torch.bfloat16) * 3.2)(M, H, D) * (M ** -0.5)),
            "w_uk": _w((lambda *s: torch.randn(*s, dtype=torch.bfloat16) * 6.4)(Dc, H, D) * (Dc ** -0.5)),
            "w_uv": _w((lambda *s: torch.randn(*s, dtype=torch.bfloat16) * 0.2)(Dc, H, D) * (Dc ** -0.5)),
            "w_o": _w((lambda *s: torch.randn(*s, dtype=torch.bfloat16) * 0.2)(H, D, M) * ((H * D) ** -0.5)),
        }
    _A_in = _A_setup()

    def _B_setup():
        B, S, D, F = 1, 2048, 640, 1728
        return {
            "x": _w((lambda *s: torch.randn(*s, dtype=torch.bfloat16) * 0.05)(B, S, D)),
            "w1": _w((lambda *s: torch.randn(*s, dtype=torch.bfloat16) * 0.4)(S, F) * (S ** -0.5)),
            "b1": _w((lambda *s: torch.randn(*s, dtype=torch.bfloat16) * 3.2)(F) * 0.02),
            "w2": _w((lambda *s: torch.randn(*s, dtype=torch.bfloat16) * 0.2)(F, S) * (F ** -0.5)),
            "b2": _w((lambda *s: torch.randn(*s, dtype=torch.bfloat16) * 0.8)(S) * 0.02),
        }
    _B_in = _B_setup()

    return {
        "A_x": _A_in["x"],
        "A_c_kv": _A_in["c_kv"],
        "A_w_q": _A_in["w_q"],
        "A_w_uk": _A_in["w_uk"],
        "A_w_uv": _A_in["w_uv"],
        "A_w_o": _A_in["w_o"],
        "B_x": _B_in["x"],
        "B_w1": _B_in["w1"],
        "B_b1": _B_in["b1"],
        "B_w2": _B_in["w2"],
        "B_b2": _B_in["b2"],
    }

def reference(A_x, A_c_kv, A_w_q, A_w_uk, A_w_uv, A_w_o, B_x, B_w1, B_b1, B_w2, B_b2):
    def _A_ref(x, c_kv, w_q, w_uk, w_uv, w_o):
        B, Sq, M = x.shape[0], x.shape[1], x.shape[2]
        Sk = c_kv.shape[1]
        H, D = w_q.shape[1], w_q.shape[2]
        q = torch.einsum("bqm,mhd->bhqd", x, w_q)
        k = torch.einsum("bkc,chd->bhkd", c_kv, w_uk)
        v = torch.einsum("bkc,chd->bhkd", c_kv, w_uv)
        scores = torch.einsum("bhqd,bhkd->bhqk", q, k) * (D ** -0.5)
        off = Sk - Sq
        qi = torch.arange(Sq)[:, None]
        kj = torch.arange(Sk)[None, :]
        cmask = kj <= (qi + off)
        scores = torch.where(cmask[None, None], scores, -1e9)
        p = _softmax(scores, axis=-1)
        ctx = torch.einsum("bhqk,bhkd->bhqd", p, v)
        return torch.einsum("bhqd,hdm->bqm", ctx, w_o)

    def _B_ref(x, w1, b1, w2, b2):
        B, S, D = x.shape
        h = _layernorm(x, axis=-1)
        u = torch.einsum("bsd,sf->bdf", h, w1) + b1
        u = _gelu(u)
        y = torch.einsum("bdf,fs->bds", u, w2) + b2
        return (lambda x, *d: x.permute(*d))(y, 0, 2, 1)

    x = A_x
    h = x + _A_ref(_layernorm(x, axis=-1), A_c_kv, A_w_q, A_w_uk, A_w_uv, A_w_o)
    h2 = h + _B_ref(_layernorm(h, axis=-1), B_w1, B_b1, B_w2, B_b2)
    return h2
```

```cpp
#include <hip/hip_runtime.h>
#include <stdint.h>

namespace {

typedef unsigned short u16;
typedef __bf16 bf16x8 __attribute__((ext_vector_type(8)));
typedef __bf16 bf16x2 __attribute__((ext_vector_type(2)));
typedef short s16x4 __attribute__((ext_vector_type(4)));
typedef short s16x8 __attribute__((ext_vector_type(8)));
typedef float f32x2 __attribute__((ext_vector_type(2)));
typedef float f32x4 __attribute__((ext_vector_type(4)));
typedef float f32x16 __attribute__((ext_vector_type(16)));
typedef unsigned u32x2 __attribute__((ext_vector_type(2)));
typedef unsigned u32x4 __attribute__((ext_vector_type(4)));
#define LAS __attribute__((address_space(3)))
#define DI __device__ __forceinline__
#define SBAR() __builtin_amdgcn_sched_barrier(0)
#define RFL(x) __builtin_amdgcn_readfirstlane(x)

constexpr int SEQ = 2048, DM = 640, DC = 512, NH = 32, HD = 128, FF = 1728;
constexpr float LN_EPS = 1e-5f;
constexpr float QSCALE = 0.08838834764831845f * 1.4426950408889634f;
constexpr float THR = 8.0f;
constexpr float NEG_BIG = -1e30f;

constexpr size_t WS_XN = 0;
constexpr size_t WS_KF = WS_XN + (size_t)SEQ * DM * 2;
constexpr size_t WS_VF = WS_KF + (size_t)NH * SEQ * HD * 2;
constexpr size_t WS_CF = WS_VF + (size_t)NH * SEQ * HD * 2;
constexpr size_t WS_PT = WS_CF + (size_t)NH * SEQ * HD * 2;
constexpr size_t WS_H = WS_PT + (size_t)4 * SEQ * DM * 4;
constexpr size_t WS_HN = WS_H + (size_t)SEQ * DM * 4;
constexpr size_t WS_UF = WS_HN + (size_t)SEQ * DM * 2;
constexpr size_t WS_END = WS_UF + (size_t)DM * FF * 2;

DI __amdgpu_buffer_rsrc_t mk_rsrc(const void* p, unsigned bytes) {
  return __builtin_amdgcn_make_buffer_rsrc((void*)p, 0, bytes, 0x00020000);
}
DI void dma1(__amdgpu_buffer_rsrc_t rsrc, unsigned voff, unsigned soff, unsigned ldsaddr) {
  asm volatile("s_mov_b32 m0, %0\n\ts_nop 0\n\tbuffer_load_dwordx4 %1, %2, %3 offen lds"
               ::"s"(ldsaddr), "v"(voff), "s"(rsrc), "s"(soff) : "memory", "m0");
}
DI void dma4(__amdgpu_buffer_rsrc_t rsrc, unsigned voff, unsigned soff, unsigned ldsaddr) {
  asm volatile("s_mov_b32 m0, %0\n\ts_nop 0\n\t"
               "buffer_load_dwordx4 %1, %2, %3 offen lds\n\t"
               "buffer_load_dwordx4 %1, %2, %3 offen offset:1024 lds\n\t"
               "buffer_load_dwordx4 %1, %2, %3 offen offset:2048 lds\n\t"
               "buffer_load_dwordx4 %1, %2, %3 offen offset:3072 lds"
               ::"s"(ldsaddr), "v"(voff), "s"(rsrc), "s"(soff) : "memory", "m0");
}
#define WAIT_VL_BAR(N) asm volatile("s_waitcnt vmcnt(" #N ") lgkmcnt(0)\n\ts_barrier" ::: "memory")
#define WAIT_L_BAR() asm volatile("s_waitcnt lgkmcnt(0)\n\ts_barrier" ::: "memory")
#define WAIT_V(N) asm volatile("s_waitcnt vmcnt(" #N ")" ::: "memory")
#define WAIT_L0() asm volatile("s_waitcnt lgkmcnt(0)" ::: "memory")

DI bf16x8 lds_b128(LAS const char* p) { return *(LAS const bf16x8*)p; }
DI bf16x8 lds_tr2(LAS const char* p0, LAS const char* p1) {
  s16x4 lo = __builtin_amdgcn_ds_read_tr16_b64_v4i16((LAS s16x4*)p0);
  s16x4 hi = __builtin_amdgcn_ds_read_tr16_b64_v4i16((LAS s16x4*)p1);
  s16x8 w = __builtin_shufflevector(lo, hi, 0, 1, 2, 3, 4, 5, 6, 7);
  return __builtin_bit_cast(bf16x8, w);
}
DI unsigned cvtpk(float lo, float hi) {
  f32x2 v = {lo, hi};
  bf16x2 b = __builtin_convertvector(v, bf16x2);
  return __builtin_bit_cast(unsigned, b);
}
DI u32x4 pack8(const f32x16& a, int o) {
  u32x4 w = {cvtpk(a[o], a[o + 1]), cvtpk(a[o + 2], a[o + 3]), cvtpk(a[o + 4], a[o + 5]), cvtpk(a[o + 6], a[o + 7])};
  return w;
}
DI float bf2f(unsigned short b) { return __uint_as_float(((unsigned)b) << 16); }
DI float bflo(unsigned w) { return __uint_as_float(w << 16); }
DI float bfhi(unsigned w) { return __uint_as_float(w & 0xffff0000u); }
#define MFMA32(a, b, c) __builtin_amdgcn_mfma_f32_32x32x16_bf16((a), (b), (c), 0, 0, 0)
#define MFMA16(a, b, c) __builtin_amdgcn_mfma_f32_16x16x32_bf16((a), (b), (c), 0, 0, 0)
DI float swap_max(float v) {
  auto rr = __builtin_amdgcn_permlane32_swap(__float_as_uint(v), __float_as_uint(v), false, false);
  return fmaxf(__uint_as_float(rr[0]), __uint_as_float(rr[1]));
}
DI float swap_add(float v) {
  auto rr = __builtin_amdgcn_permlane32_swap(__float_as_uint(v), __float_as_uint(v), false, false);
  return __uint_as_float(rr[0]) + __uint_as_float(rr[1]);
}

constexpr int K1_LDS = 2 * 65536 + 1024;

template <bool ISV>
DI void k1_mma(f32x16 (&acc)[4][2], const bf16x8 (&a)[4], const bf16x8 (&w)[2]) {
#pragma unroll
  for (int i = 0; i < 4; ++i)
#pragma unroll
    for (int n = 0; n < 2; ++n) {
      if (ISV) acc[i][n] = MFMA32(a[i], w[n], acc[i][n]);
      else acc[i][n] = MFMA32(w[n], a[i], acc[i][n]);
    }
}

template <bool ISV>
DI void k1_body(LAS char* lds, unsigned lds0, int wid, int lane, int head, int tb, __amdgpu_buffer_rsrc_t rA,
                __amdgpu_buffer_rsrc_t rK, __amdgpu_buffer_rsrc_t rV, unsigned voffA0, unsigned voffA1, unsigned voffW,
                u16* KF, u16* VF) {
  const int wm = wid >> 2, wn = wid & 3;
  const int r = lane & 31, h = lane >> 5;
  const int g1 = (lane >> 4) & 1, q = (lane & 15) >> 2, p = lane & 3;
  unsigned abase[4];
#pragma unroll
  for (int s = 0; s < 4; ++s)
    abase[s] = wm * 16384 + (r >> 3) * 1024 + (r & 7) * 128 + (((2 * s + h) ^ ((r >> 1) & 7)) << 4);
  const unsigned wbase = 32768 + h * 4096 + wn * 1024 + q * 64 + g1 * 32 + p * 8;

  f32x16 acc[4][2];
#pragma unroll
  for (int i = 0; i < 4; ++i)
#pragma unroll
    for (int n = 0; n < 2; ++n) acc[i][n] = f32x16{};

  auto issue = [&](int kt, int slot) {
    const unsigned sb = lds0 + slot * 65536;
#pragma unroll
    for (int i = 0; i < 4; ++i) {
      const int P = wid * 4 + i;
      dma1(rA, (i & 1) ? voffA1 : voffA0, (unsigned)((tb * 256 + P * 8) * 1024 + kt * 128), sb + P * 1024);
    }
#pragma unroll
    for (int np = 0; np < 4; ++np)
      dma1(np < 2 ? rK : rV, voffW, (unsigned)((kt * 64 + wid * 8) * 8192 + head * 256 + (np & 1) * 128),
           sb + 32768 + (wid * 4 + np) * 1024);
  };
  auto rd = [&](bf16x8 (&a)[4], bf16x8 (&w)[2], int slot, int s) {
    LAS const char* sb = lds + slot * 65536;
#pragma unroll
    for (int n = 0; n < 2; ++n)
      w[n] = lds_tr2(sb + wbase + s * 8192 + n * 512, sb + wbase + s * 8192 + n * 512 + 256);
#pragma unroll
    for (int i = 0; i < 4; ++i) a[i] = lds_b128(sb + abase[s] + i * 4096);
  };

  bf16x8 aA[4], wA[2], aB[4], wB[2];
  for (int kt = 0; kt < 8; ++kt) {
    const int slot = kt & 1;
    if (kt == 0) { WAIT_VL_BAR(8); } else { WAIT_VL_BAR(0); }
    if (kt >= 1 && kt + 1 < 8) issue(kt + 1, slot ^ 1);
    SBAR();
    rd(aA, wA, slot, 0);
    SBAR();
    if (kt > 0) k1_mma<ISV>(acc, aB, wB);
    SBAR();
    rd(aB, wB, slot, 1);
    SBAR();
    k1_mma<ISV>(acc, aA, wA);
    SBAR();
    rd(aA, wA, slot, 2);
    SBAR();
    k1_mma<ISV>(acc, aB, wB);
    SBAR();
    rd(aB, wB, slot, 3);
    SBAR();
    k1_mma<ISV>(acc, aA, wA);
    SBAR();
  }
  k1_mma<ISV>(acc, aB, wB);

#pragma unroll
  for (int i = 0; i < 4; ++i) {
    const int kb32 = tb * 8 + wm * 4 + i;
#pragma unroll
    for (int n = 0; n < 2; ++n) {
#pragma unroll
      for (int sg = 0; sg < 2; ++sg) {
        const u32x4 w = pack8(acc[i][n], 8 * sg);
        if (ISV) {
          const int db = 2 * (wn - 2) + n;
          *(u32x4*)(VF + ((size_t)((((head * 64 + kb32) * 4 + db) * 2 + sg) * 64 + lane)) * 8) = w;
        } else {
          const int db = 2 * wn + n;
          *(u32x4*)(KF + ((size_t)(((head * 64 + kb32) * 8 + 2 * db + sg) * 64 + lane)) * 8) = w;
        }
      }
    }
  }
}

__global__ __launch_bounds__(512, 2) void k1_kvproj(const u16* __restrict__ x, const u16* __restrict__ ckv,
                                                    const u16* __restrict__ wuk, const u16* __restrict__ wuv,
                                                    u16* __restrict__ KF, u16* __restrict__ VF, u16* __restrict__ XN) {
  extern __shared__ __attribute__((aligned(16))) char smem[];
  LAS char* lds = (LAS char*)smem;
  const unsigned lds0 = (unsigned)(uintptr_t)lds;
  const int tid = threadIdx.x, lane = tid & 63;
  const int wid = RFL(tid >> 6);
  const int b = blockIdx.x, xcd = b & 7, idx = b >> 3;
  const int head = xcd * 4 + (idx & 3), tb = idx >> 2;
  const __amdgpu_buffer_rsrc_t rA = mk_rsrc(ckv, SEQ * DC * 2);
  const __amdgpu_buffer_rsrc_t rK = mk_rsrc(wuk, DC * NH * HD * 2);
  const __amdgpu_buffer_rsrc_t rV = mk_rsrc(wuv, DC * NH * HD * 2);
  const unsigned voffA0 = (lane >> 3) * 1024 + (((lane & 7) ^ (lane >> 4)) << 4);
  const unsigned voffA1 = (lane >> 3) * 1024 + (((lane & 7) ^ (4 + (lane >> 4))) << 4);
  const unsigned voffW = ((lane >> 2) & 7) * 8192 + (lane >> 5) * 64 + (lane & 3) * 16;
#pragma unroll
  for (int kt = 0; kt < 2; ++kt) {
    const unsigned sb = lds0 + kt * 65536;
#pragma unroll
    for (int i = 0; i < 4; ++i) {
      const int P = wid * 4 + i;
      dma1(rA, (i & 1) ? voffA1 : voffA0, (unsigned)((tb * 256 + P * 8) * 1024 + kt * 128), sb + P * 1024);
    }
#pragma unroll
    for (int np = 0; np < 4; ++np)
      dma1(np < 2 ? rK : rV, voffW, (unsigned)((kt * 64 + wid * 8) * 8192 + head * 256 + (np & 1) * 128),
           sb + 32768 + (wid * 4 + np) * 1024);
  }
  {
    LAS float* red = (LAS float*)(lds + 2 * 65536);
    const int t8 = lane & 7, cs = lane >> 3;
    const int token = b * 8 + t8;
    const int c0 = wid * 10 + cs, c1 = wid * 10 + 8 + cs;
    const bool has1 = cs < 2;
    const u32x4 v0 = *(const u32x4*)(x + (size_t)token * DM + c0 * 8);
    u32x4 v1 = {0u, 0u, 0u, 0u};
    if (has1) v1 = *(const u32x4*)(x + (size_t)token * DM + c1 * 8);
    float f0[8], f1[8];
#pragma unroll
    for (int j = 0; j < 4; ++j) {
      f0[2 * j] = bflo(v0[j]); f0[2 * j + 1] = bfhi(v0[j]);
      f1[2 * j] = bflo(v1[j]); f1[2 * j + 1] = bfhi(v1[j]);
    }
    float s = 0.f;
#pragma unroll
    for (int j = 0; j < 8; ++j) s += f0[j] + f1[j];
    s += __shfl_xor(s, 8); s += __shfl_xor(s, 16); s += __shfl_xor(s, 32);
    if (lane < 8) red[wid * 8 + lane] = s;
    WAIT_L_BAR();
    float tot = 0.f;
#pragma unroll
    for (int w = 0; w < 8; ++w) tot += red[w * 8 + t8];
    const float mean = tot * (1.0f / DM);
    float sq = 0.f;
#pragma unroll
    for (int j = 0; j < 8; ++j) {
      const float d0 = f0[j] - mean; sq += d0 * d0;
      const float d1 = has1 ? f1[j] - mean : 0.f; sq += d1 * d1;
    }
    sq += __shfl_xor(sq, 8); sq += __shfl_xor(sq, 16); sq += __shfl_xor(sq, 32);
    if (lane < 8) red[64 + wid * 8 + lane] = sq;
    WAIT_L_BAR();
    float tq = 0.f;
#pragma unroll
    for (int w = 0; w < 8; ++w) tq += red[64 + w * 8 + t8];
    const float rstd = 1.0f / sqrtf(tq * (1.0f / DM) + LN_EPS);
    const int tb32 = token >> 5, rr = token & 31;
    u32x4 o0, o1;
#pragma unroll
    for (int j = 0; j < 4; ++j) {
      o0[j] = cvtpk((f0[2 * j] - mean) * rstd, (f0[2 * j + 1] - mean) * rstd);
      o1[j] = cvtpk((f1[2 * j] - mean) * rstd, (f1[2 * j + 1] - mean) * rstd);
    }
    *(u32x4*)(XN + ((size_t)((tb32 * 40 + (c0 >> 1)) * 64 + (c0 & 1) * 32 + rr)) * 8) = o0;
    if (has1) *(u32x4*)(XN + ((size_t)((tb32 * 40 + (c1 >> 1)) * 64 + (c1 & 1) * 32 + rr)) * 8) = o1;
  }
  if ((wid & 3) >= 2) k1_body<true>(lds, lds0, wid, lane, head, tb, rA, rK, rV, voffA0, voffA1, voffW, KF, VF);
  else k1_body<false>(lds, lds0, wid, lane, head, tb, rA, rK, rV, voffA0, voffA1, voffW, KF, VF);
}

constexpr int K2_LDS = 163840;
constexpr unsigned K2_SLOT0 = 98304, K2_SLOT1 = 0, K2_QST = 65536;

DI void k2_send(LAS char* dst, const f32x16& a, const f32x16& b) {
#pragma unroll
  for (int gq = 0; gq < 4; ++gq) {
    *(LAS f32x4*)(dst + gq * 1024) = (f32x4){a[4 * gq], a[4 * gq + 1], a[4 * gq + 2], a[4 * gq + 3]};
    *(LAS f32x4*)(dst + (4 + gq) * 1024) = (f32x4){b[4 * gq], b[4 * gq + 1], b[4 * gq + 2], b[4 * gq + 3]};
  }
}
DI void k2_recv(LAS const char* src, const f32x16& a, const f32x16& b, float ca, float cb, u16* dstp) {
  f32x16 ra, rb;
#pragma unroll
  for (int gq = 0; gq < 4; ++gq) {
    const f32x4 va = *(LAS const f32x4*)(src + gq * 1024);
    const f32x4 vb = *(LAS const f32x4*)(src + (4 + gq) * 1024);
#pragma unroll
    for (int e = 0; e < 4; ++e) {
      ra[4 * gq + e] = a[4 * gq + e] * ca + va[e] * cb;
      rb[4 * gq + e] = b[4 * gq + e] * ca + vb[e] * cb;
    }
  }
  *(u32x4*)(dstp) = pack8(ra, 0);
  *(u32x4*)(dstp + 512) = pack8(ra, 8);
  *(u32x4*)(dstp + 1024) = pack8(rb, 0);
  *(u32x4*)(dstp + 1536) = pack8(rb, 8);
}

__global__ __launch_bounds__(512, 2) void k2_attn(const u16* __restrict__ XN, const u16* __restrict__ wq,
                                                  const u16* __restrict__ KF, const u16* __restrict__ VF,
                                                  u16* __restrict__ CF) {
  extern __shared__ __attribute__((aligned(16))) char smem[];
  LAS char* lds = (LAS char*)smem;
  const unsigned lds0 = (unsigned)(uintptr_t)lds;
  const int tid = threadIdx.x, lane = tid & 63;
  const int wid = RFL(tid >> 6);
  const int b = blockIdx.x, xcd = b & 7, idx = b >> 3;
  const int head = xcd * 4 + (idx >> 3), px = idx & 7;
  const int qbH = 15 - px, qbL = px;
  const int rr = wid & 3, c = wid >> 2;
  const int r = lane & 31, h = lane >> 5;
  const int g1 = (lane >> 4) & 1, q4 = (lane & 15) >> 2, p4 = lane & 3;
  const __amdgpu_buffer_rsrc_t rX = mk_rsrc(XN, SEQ * DM * 2);
  const __amdgpu_buffer_rsrc_t rW = mk_rsrc(wq, DM * NH * HD * 2);
  const __amdgpu_buffer_rsrc_t rK = mk_rsrc(KF, NH * SEQ * HD * 2);
  const __amdgpu_buffer_rsrc_t rV = mk_rsrc(VF, NH * SEQ * HD * 2);
  const unsigned vlin = lane * 16;
  const unsigned voffW = ((lane >> 2) & 7) * 8192 + (lane >> 5) * 64 + (lane & 3) * 16;

  auto issue_kv = [&](int t, unsigned slot) {
    const unsigned so = (unsigned)((head * 64 + 4 * t) * 8192 + wid * 4096);
    dma4(rK, vlin, so, lds0 + slot + wid * 4096);
    dma4(rV, vlin, so, lds0 + slot + 32768 + wid * 4096);
  };
  const int tbx = (wid < 4) ? (4 * qbH + wid) : (4 * qbL + wid - 4);
  auto issue_q = [&](int kt, int st) {
    const unsigned sb = lds0 + st * 49152;
#pragma unroll
    for (int np = 0; np < 2; ++np)
      dma1(rW, voffW, (unsigned)((kt * 64 + wid * 8) * 8192 + head * 256 + np * 128), sb + (wid * 2 + np) * 1024);
    dma4(rX, vlin, (unsigned)((tbx * 40 + 4 * kt) * 1024), sb + 16384 + wid * 4096);
  };

  issue_q(0, 0);
  issue_q(1, 1);
  issue_kv(0, K2_SLOT0);
  bf16x8 qf[8];
  {
    f32x16 qa[2][2];
#pragma unroll
    for (int i = 0; i < 2; ++i)
#pragma unroll
      for (int n = 0; n < 2; ++n) qa[i][n] = f32x16{};
    const unsigned wbase = h * 2048 + c * 1024 + q4 * 64 + g1 * 32 + p4 * 8;
    const unsigned xbase = 16384 + rr * 4096 + lane * 16;
    for (int kt = 0; kt < 10; ++kt) {
      const int st = kt & 1;
      if (kt == 0) { WAIT_VL_BAR(14); }
      else if (kt == 1) { WAIT_VL_BAR(8); }
      else { WAIT_VL_BAR(0); }
      if (kt >= 1 && kt + 1 < 10) issue_q(kt + 1, st ^ 1);
      LAS const char* sb = lds + st * 49152;
#pragma unroll
      for (int s = 0; s < 4; ++s) {
        bf16x8 w[2], xh, xl;
#pragma unroll
        for (int n = 0; n < 2; ++n) w[n] = lds_tr2(sb + wbase + s * 4096 + n * 512, sb + wbase + s * 4096 + n * 512 + 256);
        xh = lds_b128(sb + xbase + s * 1024);
        xl = lds_b128(sb + xbase + 16384 + s * 1024);
#pragma unroll
        for (int n = 0; n < 2; ++n) {
          qa[0][n] = MFMA32(w[n], xh, qa[0][n]);
          qa[1][n] = MFMA32(w[n], xl, qa[1][n]);
        }
      }
    }
    WAIT_L_BAR();
#pragma unroll
    for (int i = 0; i < 2; ++i) {
      LAS char* dst = lds + (i == 0 ? 0u : K2_QST) + rr * 8192 + lane * 16;
#pragma unroll
      for (int n = 0; n < 2; ++n) {
        f32x16 t = qa[i][n] * QSCALE;
#pragma unroll
        for (int sg = 0; sg < 2; ++sg) *(LAS u32x4*)(dst + (4 * c + 2 * n + sg) * 1024) = pack8(t, 8 * sg);
      }
    }
    WAIT_L_BAR();
#pragma unroll
    for (int s = 0; s < 8; ++s) qf[s] = lds_b128(lds + rr * 8192 + lane * 16 + s * 1024);
  }

  const int nH = 16 - px, nL = px + 1;
  int tau = 0;
  for (int seg = 0; seg < 2; ++seg) {
    const int n = seg == 0 ? nH : nL;
    const int qb = seg == 0 ? qbH : qbL;
    f32x16 o[4];
#pragma unroll
    for (int d = 0; d < 4; ++d) o[d] = f32x16{};
    float mref = 0.f, lsum = 0.f;
    f32x16 negm = f32x16{};
    for (int t = 0; t < n; ++t, ++tau) {
      const unsigned slot = (tau & 1) ? K2_SLOT1 : K2_SLOT0;
      const unsigned nslot = (tau & 1) ? K2_SLOT0 : K2_SLOT1;
      WAIT_VL_BAR(0);
      if (t + 1 < n) issue_kv(t + 1, nslot);
      else if (seg == 0) issue_kv(0, nslot);
      LAS const char* kp = lds + slot + (2 * c) * 8192 + lane * 16;
      LAS const char* vp = kp + 32768;
      f32x16 s0, s1;
      {
        bf16x8 k0 = lds_b128(kp), k1 = lds_b128(kp + 8192);
        s0 = MFMA32(k0, qf[0], negm);
        s1 = MFMA32(k1, qf[0], negm);
#pragma unroll
        for (int s = 1; s < 8; ++s) {
          k0 = lds_b128(kp + s * 1024); k1 = lds_b128(kp + 8192 + s * 1024);
          s0 = MFMA32(k0, qf[s], s0);
          s1 = MFMA32(k1, qf[s], s1);
        }
      }
      if (t == n - 1) {
        const int qrel = 32 * rr + r, kb = 64 * c + 4 * h;
#pragma unroll
        for (int e = 0; e < 16; ++e) {
          const int kv = kb + (e & 3) + 8 * (e >> 2);
          if (kv > qrel) s0[e] = -INFINITY;
          if (kv + 32 > qrel) s1[e] = -INFINITY;
        }
      }
      float rm = fmaxf(s0[0], s1[0]);
#pragma unroll
      for (int e = 1; e < 16; ++e) rm = fmaxf(rm, fmaxf(s0[e], s1[e]));
      rm = swap_max(rm);
      if (t == 0 || __any(rm > THR)) {
        const float dl = (t == 0) ? fmaxf(rm, NEG_BIG) : fmaxf(rm, 0.f);
        mref += dl;
#pragma unroll
        for (int e = 0; e < 16; ++e) { s0[e] -= dl; s1[e] -= dl; negm[e] = -mref; }
        if (t != 0) {
          const float f = __builtin_amdgcn_exp2f(-dl);
          lsum *= f;
#pragma unroll
          for (int d = 0; d < 4; ++d) o[d] *= f;
        }
      }
      float ps = 0.f;
#pragma unroll
      for (int e = 0; e < 16; ++e) {
        s0[e] = __builtin_amdgcn_exp2f(s0[e]); s1[e] = __builtin_amdgcn_exp2f(s1[e]);
        ps += s0[e] + s1[e];
      }
      lsum += ps;
      bf16x8 pf[4];
      pf[0] = __builtin_bit_cast(bf16x8, pack8(s0, 0)); pf[1] = __builtin_bit_cast(bf16x8, pack8(s0, 8));
      pf[2] = __builtin_bit_cast(bf16x8, pack8(s1, 0)); pf[3] = __builtin_bit_cast(bf16x8, pack8(s1, 8));
#pragma unroll
      for (int kb = 0; kb < 2; ++kb)
#pragma unroll
        for (int sg = 0; sg < 2; ++sg)
#pragma unroll
          for (int d = 0; d < 4; ++d) {
            const bf16x8 vfr = lds_b128(vp + kb * 8192 + (d * 2 + sg) * 1024);
            o[d] = MFMA32(vfr, pf[kb * 2 + sg], o[d]);
          }
    }
    const unsigned fslot = ((tau - 1) & 1) ? K2_SLOT1 : K2_SLOT0;
    if (seg == 0) {
#pragma unroll
      for (int s = 0; s < 8; ++s) qf[s] = lds_b128(lds + K2_QST + rr * 8192 + lane * 16 + s * 1024);
    }
    lsum = swap_add(lsum);
    WAIT_L_BAR();
    if (c == 0) k2_send(lds + fslot + wid * 8192 + lane * 16, o[2], o[3]);
    else k2_send(lds + fslot + wid * 8192 + lane * 16, o[0], o[1]);
    if (h == 0) *(LAS f32x2*)(lds + K2_QST + wid * 256 + r * 8) = (f32x2){mref, lsum};
    WAIT_L_BAR();
    {
      const int pw = wid ^ 4;
      const f32x2 ml = *(LAS const f32x2*)(lds + K2_QST + pw * 256 + r * 8);
      const float m = fmaxf(mref, ml[0]);
      const float fa = __builtin_amdgcn_exp2f(mref - m), fb = __builtin_amdgcn_exp2f(ml[0] - m);
      const float inv = 1.0f / (lsum * fa + ml[1] * fb);
      const float ca = fa * inv, cb = fb * inv;
      LAS const char* src = lds + fslot + pw * 8192 + lane * 16;
      const int tb32 = 4 * qb + rr;
      u16* dstp = CF + ((size_t)(((tb32 * 32 + head) * 4 + 2 * c) * 2) * 64 + lane) * 8;
      if (c == 0) k2_recv(src, o[0], o[1], ca, cb, dstp);
      else k2_recv(src, o[2], o[3], ca, cb, dstp);
    }
  }
}

constexpr int K3_STAGE = 36864, K3_NST = 4, K3_LDS = K3_STAGE * K3_NST;

__global__ __launch_bounds__(512, 2) void k3_oproj(const u16* __restrict__ CF, const u16* __restrict__ wo,
                                                   float* __restrict__ PT) {
  extern __shared__ __attribute__((aligned(16))) char smem[];
  LAS char* lds = (LAS char*)smem;
  const unsigned lds0 = (unsigned)(uintptr_t)lds;
  const int tid = threadIdx.x, lane = tid & 63;
  const int wid = RFL(tid >> 6);
  const int b = blockIdx.x, xcd = b & 7, idx = b >> 3;
  const int ks = xcd >> 1, panel = (xcd & 1) * 8 + (idx >> 2), nt = idx & 3;
  const int tbi = wid & 3, kp = wid >> 2;
  const int h = lane >> 5, g1 = (lane >> 4) & 1, q4 = (lane & 15) >> 2, p4 = lane & 3;
  const __amdgpu_buffer_rsrc_t rC = mk_rsrc(CF, NH * SEQ * HD * 2);
  const __amdgpu_buffer_rsrc_t rW = mk_rsrc(wo, NH * HD * DM * 2);
  const unsigned vlin = lane * 16;
  unsigned voffW[3];
#pragma unroll
  for (int i = 0; i < 3; ++i) {
    const int pc = wid + 8 * i;
    const int u = 2 * pc + (lane >> 5);
    const int kb = u / 5, nb = u % 5;
    voffW[i] = (unsigned)((kb * 8 + ((lane >> 2) & 7)) * 1280 + (nb * 32 + (lane & 3) * 8) * 2);
  }
  auto issue = [&](int j, int slot) {
    const unsigned sb = lds0 + slot * K3_STAGE;
    const unsigned sW = (unsigned)((ks * 1024 + j * 64) * 1280 + nt * 320);
    dma1(rW, voffW[0], sW, sb + wid * 1024);
    dma1(rW, voffW[1], sW, sb + (wid + 8) * 1024);
    const int kk = ks * 64 + j * 4;
    if (wid < 4) {
      dma1(rW, voffW[2], sW, sb + (wid + 16) * 1024);
      const int cp = wid * 4 + 3;
      dma1(rC, vlin, (unsigned)(((panel * 4 + wid) * 256 + kk + 3) * 1024), sb + 20480 + cp * 1024);
    } else {
      const int tb = wid - 4;
#pragma unroll
      for (int s = 0; s < 3; ++s)
        dma1(rC, vlin, (unsigned)(((panel * 4 + tb) * 256 + kk + s) * 1024), sb + 20480 + (tb * 4 + s) * 1024);
    }
  };
  f32x16 acc[5];
#pragma unroll
  for (int n = 0; n < 5; ++n) acc[n] = f32x16{};
  const unsigned wbase = h * 256 + q4 * 64 + g1 * 32 + p4 * 8;
  const unsigned cbase = 20480 + tbi * 4096 + lane * 16;
  issue(0, 0); issue(1, 1); issue(2, 2);
  for (int j = 0; j < 16; ++j) {
    const int slot = j & 3;
    if (j + 2 < 16) { if (wid < 4) { WAIT_VL_BAR(8); } else { WAIT_VL_BAR(10); } }
    else if (j + 1 < 16) { if (wid < 4) { WAIT_VL_BAR(4); } else { WAIT_VL_BAR(5); } }
    else { WAIT_VL_BAR(0); }
    if (j + 3 < 16) issue(j + 3, (j + 3) & 3);
    LAS const char* sb = lds + slot * K3_STAGE;
#pragma unroll
    for (int si = 0; si < 2; ++si) {
      const int s = 2 * kp + si;
      const bf16x8 cf = lds_b128(sb + cbase + s * 1024);
#pragma unroll
      for (int n = 0; n < 5; ++n) {
        const bf16x8 w = lds_tr2(sb + wbase + (2 * s) * 2560 + n * 512, sb + wbase + (2 * s + 1) * 2560 + n * 512);
        acc[n] = MFMA32(w, cf, acc[n]);
      }
    }
  }
  WAIT_L_BAR();
  if (kp == 1) {
    LAS char* dst = lds + tbi * 20480 + lane * 16;
#pragma unroll
    for (int n = 0; n < 5; ++n)
#pragma unroll
      for (int gq = 0; gq < 4; ++gq)
        *(LAS f32x4*)(dst + (n * 4 + gq) * 1024) =
            (f32x4){acc[n][4 * gq], acc[n][4 * gq + 1], acc[n][4 * gq + 2], acc[n][4 * gq + 3]};
  }
  WAIT_L_BAR();
  if (kp == 0) {
    LAS const char* src = lds + tbi * 20480 + lane * 16;
    const int tb32 = panel * 4 + tbi;
#pragma unroll
    for (int n = 0; n < 5; ++n)
#pragma unroll
      for (int gq = 0; gq < 4; ++gq) {
        const f32x4 v = *(LAS const f32x4*)(src + (n * 4 + gq) * 1024);
        const f32x4 o = {acc[n][4 * gq] + v[0], acc[n][4 * gq + 1] + v[1], acc[n][4 * gq + 2] + v[2],
                         acc[n][4 * gq + 3] + v[3]};
        *(f32x4*)(PT + ((size_t)((((ks * 64 + tb32) * 20 + nt * 5 + n) * 4 + gq) * 64 + lane)) * 4) = o;
      }
  }
}

__global__ __launch_bounds__(256) void k4_ln(const u16* __restrict__ x, const float* __restrict__ PT,
                                             float* __restrict__ H, u16* __restrict__ HN) {
  __shared__ __attribute__((aligned(16))) u16 tile[8 * 648];
  __shared__ float red[2][4][8];
  const int tid = threadIdx.x, lane = tid & 63, wid = tid >> 6;
  const int b = blockIdx.x;
  const int r8 = lane & 7, hh = (lane >> 3) & 1, gq = lane >> 4;
  const int token = b * 8 + r8, tb32 = token >> 5, rr = token & 31;
  float v[5][4];
  float s = 0.f;
#pragma unroll
  for (int i = 0; i < 5; ++i) {
    const int mb = wid * 5 + i;
    const int m0 = mb * 32 + gq * 8 + hh * 4;
    const u32x2 xv = *(const u32x2*)(x + (size_t)token * DM + m0);
    f32x4 a = {bflo(xv[0]), bfhi(xv[0]), bflo(xv[1]), bfhi(xv[1])};
#pragma unroll
    for (int ks = 0; ks < 4; ++ks)
      a += *(const f32x4*)(PT + ((size_t)((((ks * 64 + tb32) * 20 + mb) * 4 + gq) * 64 + hh * 32 + rr)) * 4);
    *(f32x4*)(H + (size_t)token * DM + m0) = a;
#pragma unroll
    for (int e = 0; e < 4; ++e) { v[i][e] = a[e]; s += a[e]; }
  }
  s += __shfl_xor(s, 8); s += __shfl_xor(s, 16); s += __shfl_xor(s, 32);
  if (lane < 8) red[0][wid][lane] = s;
  __syncthreads();
  const float mean = (red[0][0][r8] + red[0][1][r8] + red[0][2][r8] + red[0][3][r8]) * (1.0f / DM);
  float sq = 0.f;
#pragma unroll
  for (int i = 0; i < 5; ++i)
#pragma unroll
    for (int e = 0; e < 4; ++e) { v[i][e] -= mean; sq += v[i][e] * v[i][e]; }
  sq += __shfl_xor(sq, 8); sq += __shfl_xor(sq, 16); sq += __shfl_xor(sq, 32);
  if (lane < 8) red[1][wid][lane] = sq;
  __syncthreads();
  const float var = (red[1][0][r8] + red[1][1][r8] + red[1][2][r8] + red[1][3][r8]) * (1.0f / DM);
  const float r1 = 1.0f / sqrtf(var + LN_EPS);
  const float r2 = 1.0f / sqrtf(var * r1 * r1 + LN_EPS);
  const float sc = r1 * r2;
#pragma unroll
  for (int i = 0; i < 5; ++i) {
    const int m0 = (wid * 5 + i) * 32 + gq * 8 + hh * 4;
    u32x2 w = {cvtpk(v[i][0] * sc, v[i][1] * sc), cvtpk(v[i][2] * sc, v[i][3] * sc)};
    *(u32x2*)(tile + r8 * 648 + m0) = w;
  }
  __syncthreads();
  const int ks32 = b >> 2, g = b & 3;
  for (int m = tid; m < DM; m += 256) {
    u16 e[8];
#pragma unroll
    for (int j = 0; j < 8; ++j) e[j] = tile[j * 648 + m];
    u32x4 w = {(unsigned)e[0] | ((unsigned)e[1] << 16), (unsigned)e[2] | ((unsigned)e[3] << 16),
               (unsigned)e[4] | ((unsigned)e[5] << 16), (unsigned)e[6] | ((unsigned)e[7] << 16)};
    *(u32x4*)(HN + ((size_t)(((m >> 4) * 64 + ks32) * 64 + g * 16 + (m & 15))) * 8) = w;
  }
}

constexpr int K56_LDS = 163840;

DI float gelu_tanh(float x) {
  const float u = 0.7978845608028654f * (x + 0.044715f * x * x * x);
  const float e = __builtin_amdgcn_exp2f(u * 2.8853900817779268f);
  const float t = 1.0f - 2.0f * __builtin_amdgcn_rcpf(e + 1.0f);
  return 0.5f * x * (1.0f + t);
}

template <int MODE>
__global__ __launch_bounds__(512, 2) void k56_mix(const u16* __restrict__ W, const u16* __restrict__ AF,
                                                  const u16* __restrict__ bias, const float* __restrict__ H,
                                                  u16* __restrict__ outp) {
  constexpr int LDW = MODE == 0 ? FF : SEQ;
  constexpr int NKS = MODE == 0 ? 64 : 54;
  constexpr int KROWS = MODE == 0 ? SEQ : FF;
  extern __shared__ __attribute__((aligned(16))) char smem[];
  LAS char* lds = (LAS char*)smem;
  const unsigned lds0 = (unsigned)(uintptr_t)lds;
  const int tid = threadIdx.x, lane = tid & 63;
  const int wid = RFL(tid >> 6);
  const int b = blockIdx.x, xcd = b & 7, idx = b >> 3;
  int wt, dt;
  if (MODE == 0) {
    if (idx >= 27) return;
    const int t = xcd * 27 + idx;
    wt = t >> 3; dt = t & 7;
  } else {
    wt = xcd * 4 + (idx >> 3); dt = idx & 7;
  }
  const __amdgpu_buffer_rsrc_t rW = mk_rsrc(W, KROWS * LDW * 2);
  const __amdgpu_buffer_rsrc_t rA = mk_rsrc(AF, 40 * NKS * 1024);
  const unsigned vlin = lane * 16;
  const int prow = (lane >> 2) & 7;
  const unsigned voffW0 = (unsigned)(prow * LDW * 2 + (lane >> 5) * 64 +
                                     (((lane & 3) ^ (MODE == 1 ? (((prow >> 2) & 1) << 1) : 0)) << 4));
  const unsigned voffW1 = MODE == 1 ? voffW0 : (voffW0 ^ 32u);
  const unsigned myl = lds0 + wid * 20480;
  LAS const char* mylp = lds + wid * 20480;
  auto issue = [&](int kk, int slot) {
    const unsigned sb = myl + slot * 9216;
#pragma unroll
    for (int kb = 0; kb < 4; ++kb)
      dma1(rW, (kb & 1) ? voffW1 : voffW0, (unsigned)((kk * 32 + kb * 8) * LDW * 2 + wt * 128), sb + kb * 1024);
#pragma unroll
    for (int d = 0; d < 5; ++d) dma1(rA, vlin, (unsigned)(((dt * 5 + d) * NKS + kk) * 1024), sb + 4096 + d * 1024);
  };
  const int nk = (NKS - wid + 7) >> 3;
  f32x4 acc[4][5];
#pragma unroll
  for (int i = 0; i < 4; ++i)
#pragma unroll
    for (int d = 0; d < 5; ++d) acc[i][d] = (f32x4){0.f, 0.f, 0.f, 0.f};
  const int g = lane >> 4, q4 = (lane & 15) >> 2, p4 = lane & 3;
  unsigned wb[2][2];
#pragma unroll
  for (int t = 0; t < 2; ++t)
#pragma unroll
    for (int hf = 0; hf < 2; ++hf) {
      const int kb = MODE == 0 ? g : 2 * t + (g >> 1);
      const int row = MODE == 0 ? 4 * t + q4 : 4 * (g & 1) + q4;
      const int sw = MODE == 0 ? (kb & 1) : ((row >> 2) & 1);
      wb[t][hf] = kb * 1024 + row * 64 + ((hf ^ sw) * 32) + p4 * 8;
    }
  issue(wid, 0);
  if (nk > 1) issue(wid + 8, 1);
  for (int it = 0; it < nk; ++it) {
    const int slot = it & 1;
    if (it + 1 < nk) { WAIT_V(9); } else { WAIT_V(0); }
    LAS const char* sb = mylp + slot * 9216;
    bf16x8 wf[4], af[5];
#pragma unroll
    for (int i = 0; i < 4; ++i) wf[i] = lds_tr2(sb + wb[0][i & 1] + (i >> 1) * 512, sb + wb[1][i & 1] + (i >> 1) * 512);
#pragma unroll
    for (int d = 0; d < 5; ++d) af[d] = lds_b128(sb + 4096 + d * 1024 + lane * 16);
    WAIT_L0();
    SBAR();
    if (it + 2 < nk) issue(wid + 8 * (it + 2), slot);
    SBAR();
#pragma unroll
    for (int i = 0; i < 4; ++i)
#pragma unroll
      for (int d = 0; d < 5; ++d) {
        if (MODE == 0) acc[i][d] = MFMA16(wf[i], af[d], acc[i][d]);
        else acc[i][d] = MFMA16(af[d], wf[i], acc[i][d]);
      }
  }
  WAIT_L_BAR();
  {
    LAS char* dst = lds + wid * 20480 + lane * 16;
#pragma unroll
    for (int i = 0; i < 4; ++i)
#pragma unroll
      for (int d = 0; d < 5; ++d) *(LAS f32x4*)(dst + (i * 5 + d) * 1024) = acc[i][d];
  }
  WAIT_L_BAR();
  if (MODE == 0) {
    for (int u = wid; u < 10; u += 8) {
      const int a = u / 5, d = u % 5;
      f32x4 t0 = {0.f, 0.f, 0.f, 0.f}, t1 = {0.f, 0.f, 0.f, 0.f};
#pragma unroll
      for (int w = 0; w < 8; ++w) {
        t0 += *(LAS const f32x4*)(lds + w * 20480 + ((2 * a) * 5 + d) * 1024 + lane * 16);
        t1 += *(LAS const f32x4*)(lds + w * 20480 + ((2 * a + 1) * 5 + d) * 1024 + lane * 16);
      }
      const int f0 = wt * 64 + a * 32 + 4 * g;
      const u32x2 b0 = *(const u32x2*)(bias + f0), b1 = *(const u32x2*)(bias + f0 + 16);
      t0 += (f32x4){bflo(b0[0]), bfhi(b0[0]), bflo(b0[1]), bfhi(b0[1])};
      t1 += (f32x4){bflo(b1[0]), bfhi(b1[0]), bflo(b1[1]), bfhi(b1[1])};
      u32x4 w = {cvtpk(gelu_tanh(t0[0]), gelu_tanh(t0[1])), cvtpk(gelu_tanh(t0[2]), gelu_tanh(t0[3])),
                 cvtpk(gelu_tanh(t1[0]), gelu_tanh(t1[1])), cvtpk(gelu_tanh(t1[2]), gelu_tanh(t1[3]))};
      *(u32x4*)(outp + ((size_t)(((dt * 5 + d) * 54 + wt * 2 + a) * 64 + lane)) * 8) = w;
    }
  } else {
    for (int u = wid; u < 20; u += 8) {
      const int i = u / 5, d = u % 5;
      f32x4 t0 = {0.f, 0.f, 0.f, 0.f};
#pragma unroll
      for (int w = 0; w < 8; ++w) t0 += *(LAS const f32x4*)(lds + w * 20480 + u * 1024 + lane * 16);
      const int s = wt * 64 + i * 16 + (lane & 15);
      const int d0 = dt * 80 + d * 16 + 4 * g;
      const float bb = bf2f(bias[s]);
      const f32x4 hv = *(const f32x4*)(H + (size_t)s * DM + d0);
      u32x2 w = {cvtpk(t0[0] + bb + hv[0], t0[1] + bb + hv[1]), cvtpk(t0[2] + bb + hv[2], t0[3] + bb + hv[3])};
      *(u32x2*)(outp + (size_t)s * DM + d0) = w;
    }
  }
}

}

extern "C" void kernel_launch(void* const* d_in, const int* in_sizes, int n_in, void* d_out, int out_size, void* d_ws,
                              size_t ws_size, hipStream_t stream) {
  (void)in_sizes; (void)n_in; (void)out_size; (void)ws_size;
  const u16* x = (const u16*)d_in[0];
  const u16* ckv = (const u16*)d_in[1];
  const u16* wq = (const u16*)d_in[2];
  const u16* wuk = (const u16*)d_in[3];
  const u16* wuv = (const u16*)d_in[4];
  const u16* wo = (const u16*)d_in[5];
  const u16* w1 = (const u16*)d_in[7];
  const u16* b1 = (const u16*)d_in[8];
  const u16* w2 = (const u16*)d_in[9];
  const u16* b2 = (const u16*)d_in[10];
  char* ws = (char*)d_ws;
  u16* XN = (u16*)(ws + WS_XN);
  u16* KF = (u16*)(ws + WS_KF);
  u16* VF = (u16*)(ws + WS_VF);
  u16* CF = (u16*)(ws + WS_CF);
  float* PT = (float*)(ws + WS_PT);
  float* H = (float*)(ws + WS_H);
  u16* HN = (u16*)(ws + WS_HN);
  u16* UF = (u16*)(ws + WS_UF);
  (void)hipFuncSetAttribute((const void*)k1_kvproj, hipFuncAttributeMaxDynamicSharedMemorySize, K1_LDS);
  (void)hipFuncSetAttribute((const void*)k2_attn, hipFuncAttributeMaxDynamicSharedMemorySize, K2_LDS);
  (void)hipFuncSetAttribute((const void*)k3_oproj, hipFuncAttributeMaxDynamicSharedMemorySize, K3_LDS);
  (void)hipFuncSetAttribute((const void*)k56_mix<0>, hipFuncAttributeMaxDynamicSharedMemorySize, K56_LDS);
  (void)hipFuncSetAttribute((const void*)k56_mix<1>, hipFuncAttributeMaxDynamicSharedMemorySize, K56_LDS);
  k1_kvproj<<<256, 512, K1_LDS, stream>>>(x, ckv, wuk, wuv, KF, VF, XN);
  k2_attn<<<256, 512, K2_LDS, stream>>>(XN, wq, KF, VF, CF);
  k3_oproj<<<256, 512, K3_LDS, stream>>>(CF, wo, PT);
  k4_ln<<<256, 256, 0, stream>>>(x, PT, H, HN);
  k56_mix<0><<<216, 512, K56_LDS, stream>>>(w1, HN, b1, nullptr, UF);
  k56_mix<1><<<256, 512, K56_LDS, stream>>>(w2, UF, b2, H, (u16*)d_out);
}
```

```cpp
#include <hip/hip_runtime.h>
#include <stdint.h>
#include <utility>

namespace {

typedef unsigned short u16;
typedef __bf16 bf16x8 __attribute__((ext_vector_type(8)));
typedef __bf16 bf16x2 __attribute__((ext_vector_type(2)));
typedef short s16x4 __attribute__((ext_vector_type(4)));
typedef short s16x8 __attribute__((ext_vector_type(8)));
typedef float f32x2 __attribute__((ext_vector_type(2)));
typedef float f32x4 __attribute__((ext_vector_type(4)));
typedef float f32x16 __attribute__((ext_vector_type(16)));
typedef unsigned u32x2 __attribute__((ext_vector_type(2)));
typedef unsigned u32x4 __attribute__((ext_vector_type(4)));
#define LAS __attribute__((address_space(3)))
#define DI __device__ __forceinline__
#define SBAR() __builtin_amdgcn_sched_barrier(0)
#define RFL(x) __builtin_amdgcn_readfirstlane(x)

constexpr int SEQ = 2048, DM = 640, DC = 512, NH = 32, HD = 128, FF = 1728;
constexpr float LN_EPS = 1e-5f;
constexpr float QSCALE = 0.08838834764831845f * 1.4426950408889634f;
constexpr float P_LIMIT = 1073741824.0f;
constexpr float NEG_BIG = -1e30f;

constexpr size_t WS_XN = 0;
constexpr size_t WS_KF = WS_XN + (size_t)SEQ * DM * 2;
constexpr size_t WS_VF = WS_KF + (size_t)NH * SEQ * HD * 2;
constexpr size_t WS_CF = WS_VF + (size_t)NH * SEQ * HD * 2;
constexpr size_t WS_PT = WS_CF + (size_t)NH * SEQ * HD * 2;
constexpr size_t WS_H = WS_PT + (size_t)4 * SEQ * DM * 4;
constexpr size_t WS_HN = WS_H + (size_t)SEQ * DM * 4;
constexpr size_t WS_UF = WS_HN + (size_t)SEQ * DM * 2;
constexpr size_t WS_LAM = WS_UF + (size_t)DM * FF * 2;
constexpr size_t WS_END = WS_LAM + 256 * 512 * 4;

DI __amdgpu_buffer_rsrc_t mk_rsrc(const void* p, unsigned bytes) {
  return __builtin_amdgcn_make_buffer_rsrc((void*)p, 0, bytes, 0x00020000);
}
DI void dma1(__amdgpu_buffer_rsrc_t rsrc, unsigned voff, unsigned soff, unsigned ldsaddr) {
  asm volatile("s_mov_b32 m0, %0\n\ts_nop 0\n\tbuffer_load_dwordx4 %1, %2, %3 offen lds"
               ::"s"(ldsaddr), "v"(voff), "s"(rsrc), "s"(soff) : "memory", "m0");
}
DI void dma4(__amdgpu_buffer_rsrc_t rsrc, unsigned voff, unsigned soff, unsigned ldsaddr) {
  asm volatile("s_mov_b32 m0, %0\n\ts_nop 0\n\t"
               "buffer_load_dwordx4 %1, %2, %3 offen lds\n\t"
               "buffer_load_dwordx4 %1, %2, %3 offen offset:1024 lds\n\t"
               "buffer_load_dwordx4 %1, %2, %3 offen offset:2048 lds\n\t"
               "buffer_load_dwordx4 %1, %2, %3 offen offset:3072 lds"
               ::"s"(ldsaddr), "v"(voff), "s"(rsrc), "s"(soff) : "memory", "m0");
}
#define WAIT_VL_BAR(N) asm volatile("s_waitcnt vmcnt(" #N ") lgkmcnt(0)\n\ts_barrier" ::: "memory")
#define WAIT_L_BAR() asm volatile("s_waitcnt lgkmcnt(0)\n\ts_barrier" ::: "memory")
#define WAIT_V(N) asm volatile("s_waitcnt vmcnt(" #N ")" ::: "memory")
#define WAIT_L0() asm volatile("s_waitcnt lgkmcnt(0)" ::: "memory")

DI bf16x8 lds_b128(LAS const char* p) { return *(LAS const bf16x8*)p; }
DI bf16x8 lds_tr2(LAS const char* p0, LAS const char* p1) {
  s16x4 lo = __builtin_amdgcn_ds_read_tr16_b64_v4i16((LAS s16x4*)p0);
  s16x4 hi = __builtin_amdgcn_ds_read_tr16_b64_v4i16((LAS s16x4*)p1);
  s16x8 w = __builtin_shufflevector(lo, hi, 0, 1, 2, 3, 4, 5, 6, 7);
  return __builtin_bit_cast(bf16x8, w);
}
DI unsigned cvtpk(float lo, float hi) {
  f32x2 v = {lo, hi};
  bf16x2 b = __builtin_convertvector(v, bf16x2);
  return __builtin_bit_cast(unsigned, b);
}
DI u32x4 pack8(const f32x16& a, int o) {
  u32x4 w = {cvtpk(a[o], a[o + 1]), cvtpk(a[o + 2], a[o + 3]), cvtpk(a[o + 4], a[o + 5]), cvtpk(a[o + 6], a[o + 7])};
  return w;
}
DI float bf2f(unsigned short b) { return __uint_as_float(((unsigned)b) << 16); }
DI float bflo(unsigned w) { return __uint_as_float(w << 16); }
DI float bfhi(unsigned w) { return __uint_as_float(w & 0xffff0000u); }
#define MFMA32(a, b, c) __builtin_amdgcn_mfma_f32_32x32x16_bf16((a), (b), (c), 0, 0, 0)
#define MFMA16(a, b, c) __builtin_amdgcn_mfma_f32_16x16x32_bf16((a), (b), (c), 0, 0, 0)
#define STAMP_DECL()
#define STAMP()
#define STAMP_END(kid, dbg)
DI float swap_max(float v) {
  auto rr = __builtin_amdgcn_permlane32_swap(__float_as_uint(v), __float_as_uint(v), false, false);
  return fmaxf(__uint_as_float(rr[0]), __uint_as_float(rr[1]));
}
DI float swap_add(float v) {
  auto rr = __builtin_amdgcn_permlane32_swap(__float_as_uint(v), __float_as_uint(v), false, false);
  return __uint_as_float(rr[0]) + __uint_as_float(rr[1]);
}

constexpr int K1_LDS = 4 * 32768;
#define K1ROT 0

template <bool ISV>
DI void k1_body(LAS char* lds, unsigned lds0, int wid, int lane, int head, int tb, const u16* __restrict__ ckv,
                const u16* __restrict__ wsrc, const u16* __restrict__ x, u16* __restrict__ KF, u16* __restrict__ VF,
                u16* __restrict__ XN
                ) {
  const int wm = wid >> 2, wn = wid & 3;
  const int r = lane & 31, h = lane >> 5;
  const int g1 = (lane >> 4) & 1, q = (lane & 15) >> 2, p = lane & 3;
  const __amdgpu_buffer_rsrc_t rA = mk_rsrc(ckv, SEQ * DC * 2);
  const __amdgpu_buffer_rsrc_t rWm = mk_rsrc(wsrc, DC * NH * HD * 2);
  const unsigned voffA = (lane >> 2) * 1024 + (((lane & 3) ^ ((lane >> 4) & 3)) << 4);
  const unsigned voffW = ((lane >> 2) & 7) * 8192 + (lane >> 5) * 64 + (lane & 3) * 16;
  auto piece = [&](int kt0, int i) {
    const unsigned sb = lds0 + (kt0 & 3) * 32768;
    const int kt = (kt0 + K1ROT) & 15;
    if (i < 2) dma1(rA, voffA, (unsigned)((tb * 256 + (wid * 2 + i) * 16) * 1024 + kt * 64), sb + (wid * 2 + i) * 1024);
    else dma1(rWm, voffW, (unsigned)((kt * 32 + (wid >> 1) * 8) * 8192 + head * 256 + (i - 2) * 128),
              sb + 16384 + ((wid >> 1) * 4 + (wid & 1) * 2 + (i - 2)) * 1024);
  };
  unsigned abase[2];
#pragma unroll
  for (int s = 0; s < 2; ++s) abase[s] = wm * 8192 + r * 64 + (((2 * s + h) ^ ((r >> 2) & 3)) << 4);
  const unsigned wbase = 16384 + h * 4096 + wn * 1024 + q * 64 + g1 * 32 + p * 8;

  f32x16 acc[4][2];
#pragma unroll
  for (int i = 0; i < 4; ++i)
#pragma unroll
    for (int n = 0; n < 2; ++n) acc[i][n] = f32x16{};
  auto rd = [&](bf16x8 (&a)[4], bf16x8 (&w)[2], LAS const char* sb, int s) {
#pragma unroll
    for (int n = 0; n < 2; ++n) w[n] = lds_tr2(sb + wbase + s * 8192 + n * 512, sb + wbase + s * 8192 + n * 512 + 256);
#pragma unroll
    for (int i = 0; i < 4; ++i) a[i] = lds_b128(sb + abase[s] + i * 2048);
  };
  auto mm2 = [&](const bf16x8 (&a)[4], const bf16x8 (&w)[2], int i) {
#pragma unroll
    for (int n = 0; n < 2; ++n) {
      if (ISV) acc[i][n] = MFMA32(a[i], w[n], acc[i][n]);
      else acc[i][n] = MFMA32(w[n], a[i], acc[i][n]);
    }
  };
  bf16x8 aA[4], wA[2], aB[4], wB[2];
  STAMP_DECL();
  STAMP();
#pragma unroll
  for (int kt = 0; kt < 3; ++kt)
#pragma unroll
    for (int i = 0; i < 4; ++i) piece(kt, i);
  STAMP();
#pragma unroll
  for (int kt = 0; kt < 16; ++kt) {
    if (kt < 14) { WAIT_VL_BAR(8); } else if (kt == 14) { WAIT_VL_BAR(4); } else { WAIT_VL_BAR(0); }
    STAMP();
    LAS const char* sb = lds + (kt & 3) * 32768;
    const bool more = kt + 3 < 16;
    rd(aA, wA, sb, 0);
    SBAR();
    if (kt > 0) {
      mm2(aB, wB, 0); mm2(aB, wB, 1);
      if (more) piece(kt + 3, 0);
      SBAR();
      mm2(aB, wB, 2); mm2(aB, wB, 3);
      if (more) piece(kt + 3, 1);
    } else {
      piece(3, 0); piece(3, 1);
    }
    SBAR();
    rd(aB, wB, sb, 1);
    SBAR();
    mm2(aA, wA, 0); mm2(aA, wA, 1);
    if (more) piece(kt + 3, 2);
    SBAR();
    mm2(aA, wA, 2); mm2(aA, wA, 3);
    if (more) piece(kt + 3, 3);
    SBAR();
  }
  mm2(aB, wB, 0); mm2(aB, wB, 1); mm2(aB, wB, 2); mm2(aB, wB, 3);
  STAMP();
  const int token = blockIdx.x * 8 + wid;
  const bool has1 = lane < 16;
  const u32x4 v0 = *(const u32x4*)(x + (size_t)token * DM + lane * 8);
  u32x4 v1 = {0u, 0u, 0u, 0u};
  if (has1) v1 = *(const u32x4*)(x + (size_t)token * DM + 512 + lane * 8);
  SBAR();
#pragma unroll
  for (int i = 0; i < 4; ++i) {
    const int kb32 = tb * 8 + wm * 4 + i;
#pragma unroll
    for (int n = 0; n < 2; ++n) {
#pragma unroll
      for (int sg = 0; sg < 2; ++sg) {
        const u32x4 w = pack8(acc[i][n], 8 * sg);
        if (ISV) {
          const int db = 2 * (wn - 2) + n;
          *(u32x4*)(VF + ((size_t)((((head * 64 + kb32) * 4 + db) * 2 + sg) * 64 + lane)) * 8) = w;
        } else {
          const int db = 2 * wn + n;
          *(u32x4*)(KF + ((size_t)(((head * 64 + kb32) * 8 + 2 * db + sg) * 64 + lane)) * 8) = w;
        }
      }
    }
  }
  STAMP();
  {
    float f0[8], f1[8];
#pragma unroll
    for (int j = 0; j < 4; ++j) {
      f0[2 * j] = bflo(v0[j]); f0[2 * j + 1] = bfhi(v0[j]);
      f1[2 * j] = bflo(v1[j]); f1[2 * j + 1] = bfhi(v1[j]);
    }
    float s = 0.f;
#pragma unroll
    for (int j = 0; j < 8; ++j) s += f0[j] + f1[j];
#pragma unroll
    for (int o = 1; o < 64; o <<= 1) s += __shfl_xor(s, o);
    const float mean = s * (1.0f / DM);
    float sq = 0.f;
#pragma unroll
    for (int j = 0; j < 8; ++j) {
      const float d0 = f0[j] - mean; sq += d0 * d0;
      const float d1 = has1 ? f1[j] - mean : 0.f; sq += d1 * d1;
    }
#pragma unroll
    for (int o = 1; o < 64; o <<= 1) sq += __shfl_xor(sq, o);
    const float rstd = 1.0f / sqrtf(sq * (1.0f / DM) + LN_EPS);
    const int tb32 = token >> 5, rr = token & 31;
    u32x4 o0, o1;
#pragma unroll
    for (int j = 0; j < 4; ++j) {
      o0[j] = cvtpk((f0[2 * j] - mean) * rstd, (f0[2 * j + 1] - mean) * rstd);
      o1[j] = cvtpk((f1[2 * j] - mean) * rstd, (f1[2 * j + 1] - mean) * rstd);
    }
    *(u32x4*)(XN + ((size_t)((tb32 * 40 + (lane >> 1)) * 64 + (lane & 1) * 32 + rr)) * 8) = o0;
    if (has1) *(u32x4*)(XN + ((size_t)((tb32 * 40 + 32 + (lane >> 1)) * 64 + (lane & 1) * 32 + rr)) * 8) = o1;
  }
  STAMP();
  STAMP();
  STAMP_END(0, dbgp);
}

__global__ __launch_bounds__(512, 2) void k1_kvproj(const u16* __restrict__ x, const u16* __restrict__ ckv,
                                                    const u16* __restrict__ wuk, const u16* __restrict__ wuv,
                                                    u16* __restrict__ KF, u16* __restrict__ VF, u16* __restrict__ XN) {
  extern __shared__ __attribute__((aligned(16))) char smem[];
  LAS char* lds = (LAS char*)smem;
  const unsigned lds0 = (unsigned)(uintptr_t)lds;
  const int tid = threadIdx.x, lane = tid & 63;
  const int wid = RFL(tid >> 6);
  const int b = blockIdx.x, xcd = b & 7, idx = b >> 3;
  const int head = xcd * 4 + (idx & 3), tb = idx >> 2;
  const u16* wsrc = (wid & 1) ? wuv : wuk;
#define K1_EXTRA
  if ((wid & 3) >= 2) k1_body<true>(lds, lds0, wid, lane, head, tb, ckv, wsrc, x, KF, VF, XN K1_EXTRA);
  else k1_body<false>(lds, lds0, wid, lane, head, tb, ckv, wsrc, x, KF, VF, XN K1_EXTRA);
}

constexpr int K2_LDS = 163840;
constexpr unsigned L_KB1 = 0, L_VB0 = 16384, L_VA1 = 32768, L_KA0 = 49152, L_KB0 = 65536, L_KA1 = 81920, L_VA0 = 98304,
                   L_VB1 = 114688, L_STASH = 131072;
constexpr int NSTEP = 17;
#define SPRIO 2

template <int OFF>
DI void dma1o(__amdgpu_buffer_rsrc_t rsrc, unsigned voff, unsigned soff, unsigned ldsaddr) {
  asm volatile("s_mov_b32 m0, %0\n\ts_nop 0\n\tbuffer_load_dwordx4 %1, %2, %3 offen offset:%4 lds"
               ::"s"(ldsaddr), "v"(voff), "s"(rsrc), "s"(soff), "i"(OFF) : "memory", "m0");
}
#define XEXP(x) __builtin_amdgcn_exp2f(x)
#define MX3(a, b, c) __builtin_fmaxf(__builtin_fmaxf((a), (b)), (c))
#define PIN(x) asm volatile("" : "+v"(x))
DI float fmul_s(float a, float b) { float r; asm("v_mul_f32 %0, %1, %2" : "=v"(r) : "v"(a), "v"(b)); return r; }
DI float fadd_s(float a, float b) { float r; asm("v_add_f32 %0, %1, %2" : "=v"(r) : "v"(a), "v"(b)); return r; }
DI float fsub_s(float a, float b) { float r; asm("v_sub_f32 %0, %1, %2" : "=v"(r) : "v"(a), "v"(b)); return r; }
DI void add_in(f32x16& s, const f32x16& t) {
#pragma unroll
  for (int e = 0; e < 16; ++e) asm volatile("v_add_f32 %0, %0, %1" : "+v"(s[e]) : "v"(t[e]));
}
DI void add_in(f32x16& s, float t) {
#pragma unroll
  for (int e = 0; e < 16; ++e) asm volatile("v_add_f32 %0, %0, %1" : "+v"(s[e]) : "v"(t));
}
DI void sub_in(f32x16& s, float t) {
#pragma unroll
  for (int e = 0; e < 16; ++e) asm volatile("v_sub_f32 %0, %0, %1" : "+v"(s[e]) : "v"(t));
}
DI void set_in(f32x16& s, float t) {
#pragma unroll
  for (int e = 0; e < 16; ++e) asm volatile("v_mov_b32 %0, %1" : "+v"(s[e]) : "v"(t));
}
DI float ffma_s(float a, float b, float c) { float r; asm("v_fma_f32 %0, %1, %2, %3" : "=v"(r) : "v"(a), "v"(b), "v"(c)); return r; }
#define ACLOB "a8","a9","a10","a11","a12","a13","a14","a15","a16","a17","a18","a19","a20","a21","a22","a23","a24","a25","a26","a27","a28","a29","a30","a31","a32","a33","a34","a35","a36","a37","a38","a39","a40","a41","a42","a43","a44","a45","a46","a47","a48","a49","a50","a51","a52","a53","a54","a55","a56","a57","a58","a59","a60","a61","a62","a63","a64","a65","a66","a67","a68","a69","a70","a71","a72","a73","a74","a75","a76","a77","a78","a79","a80","a81","a82","a83","a84","a85","a86","a87","a88","a89","a90","a91","a92","a93","a94","a95","a96","a97","a98","a99","a100","a101","a102","a103","a104","a105","a106","a107","a108","a109","a110","a111","a112","a113","a114","a115","a116","a117","a118","a119","a120","a121","a122","a123","a124","a125","a126","a127"
constexpr int A_O = 8, A_Q = 72, A_R = 104, NRING = 6;
template <int SLOT, int OFF> DI void ring_read(unsigned addr) {
  asm volatile("ds_read_b128 a[%c1:%c2], %0 offset:%c3" ::"v"(addr), "i"(A_R + 4 * SLOT), "i"(A_R + 4 * SLOT + 3), "i"(OFF) : "memory");
}
template <int S, int OFF> DI void q_read(unsigned addr) {
  asm volatile("ds_read_b128 a[%c1:%c2], %0 offset:%c3" ::"v"(addr), "i"(A_Q + 4 * S), "i"(A_Q + 4 * S + 3), "i"(OFF) : "memory");
}
constexpr int V_NEGM = 64, V_S0 = 80, V_S1 = 96, V_PF = 112;
template <int W, int SLOT, int S, int DST> DI void qk_head() {
  asm volatile("s_waitcnt lgkmcnt(%c0)\n\tv_mfma_f32_32x32x16_bf16 v[%c1:%c2], a[%c3:%c4], a[%c5:%c6], v[%c7:%c8]"
               ::"i"(W), "i"(DST), "i"(DST + 15), "i"(A_R + 4 * SLOT), "i"(A_R + 4 * SLOT + 3), "i"(A_Q + 4 * S), "i"(A_Q + 4 * S + 3),
                 "i"(V_NEGM), "i"(V_NEGM + 15));
}
template <int W, int SLOT, int S, int DST> DI void qk_mfma() {
  asm volatile("s_waitcnt lgkmcnt(%c0)\n\tv_mfma_f32_32x32x16_bf16 v[%c1:%c2], a[%c3:%c4], a[%c5:%c6], v[%c1:%c2]"
               ::"i"(W), "i"(DST), "i"(DST + 15), "i"(A_R + 4 * SLOT), "i"(A_R + 4 * SLOT + 3), "i"(A_Q + 4 * S), "i"(A_Q + 4 * S + 3));
}
template <int W, int SLOT, int D, int P> DI void pv_mfma() {
  asm volatile("s_waitcnt lgkmcnt(%c0)\n\tv_mfma_f32_32x32x16_bf16 a[%c1:%c2], a[%c3:%c4], v[%c5:%c6], a[%c1:%c2]"
               ::"i"(W), "i"(A_O + 16 * D), "i"(A_O + 16 * D + 15), "i"(A_R + 4 * SLOT), "i"(A_R + 4 * SLOT + 3), "i"(V_PF + 4 * P),
                 "i"(V_PF + 4 * P + 3));
}
template <int S, int P> DI void exps8(float& ps0, float& ps1, float& ps2, float& ps3) {
  float t0, t1, t2, t3, t4, t5, t6, t7;
  asm volatile("v_exp_f32 %4, v[%c12]\n\tv_exp_f32 %5, v[%c12+1]\n\tv_exp_f32 %6, v[%c12+2]\n\tv_exp_f32 %7, v[%c12+3]\n\t"
               "v_exp_f32 %8, v[%c12+4]\n\tv_exp_f32 %9, v[%c12+5]\n\tv_exp_f32 %10, v[%c12+6]\n\tv_exp_f32 %11, v[%c12+7]\n\t"
               "v_add_f32 %0, %0, %4\n\tv_add_f32 %1, %1, %5\n\tv_cvt_pk_bf16_f32 v[%c13], %4, %5\n\t"
               "v_add_f32 %2, %2, %6\n\tv_add_f32 %3, %3, %7\n\tv_cvt_pk_bf16_f32 v[%c13+1], %6, %7\n\t"
               "v_add_f32 %0, %0, %8\n\tv_add_f32 %1, %1, %9\n\tv_cvt_pk_bf16_f32 v[%c13+2], %8, %9\n\t"
               "v_add_f32 %2, %2, %10\n\tv_add_f32 %3, %3, %11\n\tv_cvt_pk_bf16_f32 v[%c13+3], %10, %11"
               : "+v"(ps0), "+v"(ps1), "+v"(ps2), "+v"(ps3), "=&v"(t0), "=&v"(t1), "=&v"(t2), "=&v"(t3), "=&v"(t4), "=&v"(t5),
                 "=&v"(t6), "=&v"(t7)
               : "i"(S), "i"(V_PF + 4 * P));
}
DI float exps32() {
  float ps0 = 0.f, ps1 = 0.f, ps2 = 0.f, ps3 = 0.f, r;
  exps8<V_S0, 0>(ps0, ps1, ps2, ps3); exps8<V_S0 + 8, 1>(ps0, ps1, ps2, ps3);
  exps8<V_S1, 2>(ps0, ps1, ps2, ps3); exps8<V_S1 + 8, 3>(ps0, ps1, ps2, ps3);
  asm volatile("v_add_f32 %1, %1, %2\n\tv_add_f32 %3, %3, %4\n\tv_add_f32 %0, %1, %3" : "=v"(r), "+v"(ps0), "+v"(ps1), "+v"(ps2), "+v"(ps3));
  return r;
}
DI float rowmax32() {
  float a, b;
  asm volatile("v_max3_f32 %0, v80, v81, v82\n\tv_max3_f32 %1, v83, v84, v85\n\tv_max3_f32 %0, %0, v86, v87\n\tv_max3_f32 %1, %1, v88, v89\n\t"
               "v_max3_f32 %0, %0, v90, v91\n\tv_max3_f32 %1, %1, v92, v93\n\tv_max3_f32 %0, %0, v94, v95\n\tv_max3_f32 %1, %1, v96, v97\n\t"
               "v_max3_f32 %0, %0, v98, v99\n\tv_max3_f32 %1, %1, v100, v101\n\tv_max3_f32 %0, %0, v102, v103\n\tv_max3_f32 %1, %1, v104, v105\n\t"
               "v_max3_f32 %0, %0, v106, v107\n\tv_max3_f32 %1, %1, v108, v109\n\tv_max3_f32 %0, %0, v110, v111\n\tv_max_f32 %0, %0, %1"
               : "=&v"(a), "=&v"(b));
  return a;
}
template <int R> DI void vreg_add(float t) { asm volatile("v_add_f32 v[%c1], v[%c1], %0" ::"v"(t), "i"(R)); }
template <int S, int... E> DI void mask_all_(float ninf, std::integer_sequence<int, E...>) { (vreg_add<S + E>(ninf), ...); }
template <int S, int... E> DI void mask_tri_(int dq, float ninf, std::integer_sequence<int, E...>) {
  (vreg_add<S + E>(((E & 3) + 8 * (E >> 2) > dq) ? ninf : 0.f), ...);
}
template <int I> DI float acc_rd() { float r; asm volatile("v_accvgpr_read_b32 %0, a[%c1]" : "=v"(r) : "i"(I)); return r; }
template <int I> DI void acc_wr(float v) { asm volatile("v_accvgpr_write_b32 a[%c1], %0" ::"v"(v), "i"(I)); }
template <int D, int... E> DI void o_read_(f32x16& t, std::integer_sequence<int, E...>) { ((t[E] = acc_rd<A_O + 16 * D + E>()), ...); }
template <int D> DI f32x16 o_read() { f32x16 t; o_read_<D>(t, std::make_integer_sequence<int, 16>{}); return t; }
template <int... E> DI void o_zero_(std::integer_sequence<int, E...>) { (acc_wr<A_O + E>(0.f), ...); }
template <int... E> DI void o_scale_(float f, std::integer_sequence<int, E...>) { (acc_wr<A_O + E>(fmul_s(acc_rd<A_O + E>(), f)), ...); }
template <int R> DI u32x4 acc_pack8() {
  u32x4 o; float t0, t1;
  asm volatile("v_accvgpr_read_b32 %4, a[%c6]\n\tv_accvgpr_read_b32 %5, a[%c6+1]\n\ts_nop 0\n\tv_cvt_pk_bf16_f32 %0, %4, %5\n\t"
               "v_accvgpr_read_b32 %4, a[%c6+2]\n\tv_accvgpr_read_b32 %5, a[%c6+3]\n\ts_nop 0\n\tv_cvt_pk_bf16_f32 %1, %4, %5\n\t"
               "v_accvgpr_read_b32 %4, a[%c6+4]\n\tv_accvgpr_read_b32 %5, a[%c6+5]\n\ts_nop 0\n\tv_cvt_pk_bf16_f32 %2, %4, %5\n\t"
               "v_accvgpr_read_b32 %4, a[%c6+6]\n\tv_accvgpr_read_b32 %5, a[%c6+7]\n\ts_nop 0\n\tv_cvt_pk_bf16_f32 %3, %4, %5"
               : "=&v"(o[0]), "=&v"(o[1]), "=&v"(o[2]), "=&v"(o[3]), "=&v"(t0), "=&v"(t1) : "i"(R));
  return o;
}
template <int R> DI u32x4 acc_combine8(const u32x4& w, float ca, float cb) {
  u32x4 o; float t0, t1, u0, u1;
#define CMB(K, OFF) "v_accvgpr_read_b32 %4, a[%c14+" #OFF "]\n\tv_accvgpr_read_b32 %5, a[%c14+" #OFF "+1]\n\t" \
                    "v_lshlrev_b32 %6, 16, %" #K "\n\tv_and_b32 %7, 0xffff0000, %" #K "\n\t" \
                    "v_mul_f32 %4, %4, %13\n\tv_mul_f32 %5, %5, %13\n\tv_fma_f32 %4, %6, %12, %4\n\tv_fma_f32 %5, %7, %12, %5\n\t"
  asm volatile(CMB(8, 0) "v_cvt_pk_bf16_f32 %0, %4, %5\n\t" CMB(9, 2) "v_cvt_pk_bf16_f32 %1, %4, %5\n\t"
               CMB(10, 4) "v_cvt_pk_bf16_f32 %2, %4, %5\n\t" CMB(11, 6) "v_cvt_pk_bf16_f32 %3, %4, %5"
               : "=&v"(o[0]), "=&v"(o[1]), "=&v"(o[2]), "=&v"(o[3]), "=&v"(t0), "=&v"(t1), "=&v"(u0), "=&v"(u1)
               : "v"(w[0]), "v"(w[1]), "v"(w[2]), "v"(w[3]), "v"(ca), "v"(cb), "i"(R));
#undef CMB
  return o;
}
DI void scores_add(float aone, float v) {
  asm volatile("s_nop 1\n\tv_mfma_f32_32x32x2_f32 v[80:95], %0, %1, v[80:95]\n\tv_mfma_f32_32x32x2_f32 v[96:111], %0, %1, v[96:111]" ::"v"(aone), "v"(v));
}
DI void negm_set(float aone, float v) { asm volatile("s_nop 1\n\tv_mfma_f32_32x32x2_f32 v[64:79], %0, %1, 0" ::"v"(aone), "v"(v)); }
#define ACC_FENCE() asm volatile("s_nop 15\n\ts_nop 3" ::: "memory")
#define BAR_A(WAITS) asm volatile("s_waitcnt " WAITS "\n\ts_barrier" ::: "memory", "v127", ACLOB)

template <int J> DI void frag_read(unsigned ka, unsigned va) {
  if constexpr (J < 16) ring_read<J % NRING, (J & 1) * 8192 + (J >> 1) * 1024>(ka);
  else { constexpr int i = J - 16; ring_read<J % NRING, (i >> 3) * 8192 + ((i & 3) * 2 + ((i >> 2) & 1)) * 1024>(va); }
}
constexpr bool XREAD = true;
constexpr bool XDMA = true;
constexpr int PD = 4;

__global__ __launch_bounds__(512, 2) __attribute__((amdgpu_num_vgpr(64))) void k2_attn(const u16* __restrict__ XN, const u16* __restrict__ wq,
                                                  const u16* __restrict__ KF, const u16* __restrict__ VF,
                                                  u16* __restrict__ CF, float* __restrict__ LAM) {
  extern __shared__ __attribute__((aligned(16))) char smem[];
  LAS char* lds = (LAS char*)smem;
  const unsigned lds0 = (unsigned)(uintptr_t)lds;
  const int tid = threadIdx.x, lane = tid & 63;
  const int wid = RFL(tid >> 6);
  const int b = blockIdx.x, xcd = b & 7, idx = b >> 3;
  const int head = xcd * 4 + (idx >> 3), px = idx & 7;
  const int qbH = 15 - px, qbL = px;
  const int nH = 16 - px;
  const int rr = wid & 3, c = wid >> 2;
  const int r = lane & 31, h = lane >> 5;
  const int g1 = (lane >> 4) & 1, q4 = (lane & 15) >> 2, p4 = lane & 3;
  const __amdgpu_buffer_rsrc_t rX = mk_rsrc(XN, SEQ * DM * 2);
  const __amdgpu_buffer_rsrc_t rW = mk_rsrc(wq, DM * NH * HD * 2);
  const __amdgpu_buffer_rsrc_t rK = mk_rsrc(KF, NH * SEQ * HD * 2);
  const __amdgpu_buffer_rsrc_t rV = mk_rsrc(VF, NH * SEQ * HD * 2);
  const unsigned vlin = lane * 16;
  const unsigned voffW = ((lane >> 2) & 7) * 8192 + (lane >> 5) * 64 + (lane & 3) * 16;
  auto kvoff = [&](int sg, int cc) -> unsigned {
    const int t = sg < nH ? sg : sg - nH;
    return (unsigned)((head * 64 + 4 * t + 2 * cc) * 8192);
  };
  STAMP_DECL();
  STAMP();
  const int tbx = (wid < 4) ? (4 * qbH + wid) : (4 * qbL + wid - 4);
  auto q_load = [&](unsigned addr) {
    q_read<0, 0>(addr); q_read<1, 1024>(addr); q_read<2, 2048>(addr); q_read<3, 3072>(addr);
    q_read<4, 4096>(addr); q_read<5, 5120>(addr); q_read<6, 6144>(addr); q_read<7, 7168>(addr);
  };
  {
    auto q_piece = [&](int kt, int i) {
      const unsigned sb = lds0 + (unsigned)(kt % 3) * 49152;
      if (i < 2) dma1(rW, voffW, (unsigned)((kt * 64 + wid * 8) * 8192 + head * 256 + i * 128), sb + (wid * 2 + i) * 1024);
      else dma1(rX, vlin, (unsigned)((tbx * 40 + 4 * kt + (i - 2)) * 1024), sb + 16384 + wid * 4096 + (i - 2) * 1024);
    };
    auto kv_piece = [&](int i) {
      const unsigned part = wid * 2048 + (i & 1) * 1024;
      if (i < 2) dma1(rK, vlin, kvoff(0, 0) + part, lds0 + L_KA0 + part);
      else dma1(rK, vlin, kvoff(0, 1) + part, lds0 + L_KB0 + part);
    };
#pragma unroll
    for (int i = 0; i < 6; ++i) q_piece(0, i);
#pragma unroll
    for (int i = 0; i < 6; ++i) q_piece(1, i);
    f32x16 qa[2][2];
#pragma unroll
    for (int i = 0; i < 2; ++i)
#pragma unroll
      for (int n = 0; n < 2; ++n) qa[i][n] = f32x16{};
    const unsigned wbase = h * 2048 + c * 1024 + q4 * 64 + g1 * 32 + p4 * 8;
    const unsigned xbase = 16384 + rr * 4096 + lane * 16;
    bf16x8 wA[2], xA[2], wB[2], xB[2];
    auto rdq = [&](bf16x8 (&w)[2], bf16x8 (&x)[2], LAS const char* sb, int s) {
      w[0] = lds_tr2(sb + wbase + s * 4096, sb + wbase + s * 4096 + 256);
      x[0] = lds_b128(sb + xbase + s * 1024);
      w[1] = lds_tr2(sb + wbase + s * 4096 + 512, sb + wbase + s * 4096 + 768);
      x[1] = lds_b128(sb + xbase + 16384 + s * 1024);
    };
    auto mmq = [&](const bf16x8 (&w)[2], const bf16x8 (&x)[2]) {
      qa[0][0] = MFMA32(w[0], x[0], qa[0][0]);
      qa[1][0] = MFMA32(w[0], x[1], qa[1][0]);
      qa[0][1] = MFMA32(w[1], x[0], qa[0][1]);
      qa[1][1] = MFMA32(w[1], x[1], qa[1][1]);
    };
    for (int kt = 0; kt < 10; ++kt) {
      if (kt < 9) { WAIT_VL_BAR(6); } else { WAIT_VL_BAR(4); }
      LAS const char* sb = lds + (unsigned)(kt % 3) * 49152;
      auto piece = [&](int i) {
        if (kt < 8) q_piece(kt + 2, i);
        else if (kt == 8 && i < 4) kv_piece(i);
      };
      rdq(wA, xA, sb, 0);
      SBAR();
      if (kt > 0) mmq(wB, xB);
      piece(0); SBAR();
      rdq(wB, xB, sb, 1);
      SBAR();
      mmq(wA, xA);
      piece(1); piece(2); SBAR();
      rdq(wA, xA, sb, 2);
      SBAR();
      mmq(wB, xB);
      piece(3); piece(4); SBAR();
      rdq(wB, xB, sb, 3);
      SBAR();
      mmq(wA, xA);
      piece(5); SBAR();
    }
    mmq(wB, xB);
    STAMP();
    WAIT_L_BAR();
#pragma unroll
    for (int i = 0; i < 2; ++i) {
      LAS char* dst = lds + (i == 0 ? 0u : L_STASH) + rr * 8192 + lane * 16;
#pragma unroll
      for (int n = 0; n < 2; ++n) {
        const f32x16 t = qa[i][n] * QSCALE;
#pragma unroll
        for (int sg = 0; sg < 2; ++sg) *(LAS u32x4*)(dst + (4 * c + 2 * n + sg) * 1024) = pack8(t, 8 * sg);
      }
    }
    WAIT_L_BAR();
    q_load(lds0 + rr * 8192 + lane * 16);
    BAR_A("vmcnt(0) lgkmcnt(0)");
  }
  STAMP();

  o_zero_(std::make_integer_sequence<int, 64>{});
  negm_set(0.f, 0.f);
  float mref = 0.f, lsum = 0.f, mfin = 0.f, lfin = 0.f;
  const unsigned kslot0 = c ? L_KB0 : L_KA0, kslot1 = c ? L_KB1 : L_KA1;
  const unsigned vslot0 = c ? L_VB0 : L_VA0, vslot1 = c ? L_VB1 : L_VA1;
  const unsigned lrd = lds0 + lane * 16;
  float* lamp = LAM + (size_t)b * 512 + rr * 64;
  auto epi = [&](int qb, f32x2 mlA) __attribute__((always_inline)) {
    int ln = (int)tid & 63;
    asm volatile("" : "+v"(ln));
    LAS char* st = lds + L_STASH + rr * 8192 + ln * 16;
    ACC_FENCE();
    if (c == 0) {
      *(LAS u32x4*)(st) = acc_pack8<A_O + 0>();          *(LAS u32x4*)(st + 1024) = acc_pack8<A_O + 8>();
      *(LAS u32x4*)(st + 2048) = acc_pack8<A_O + 16>();  *(LAS u32x4*)(st + 3072) = acc_pack8<A_O + 24>();
      *(LAS u32x4*)(st + 4096) = acc_pack8<A_O + 32>();  *(LAS u32x4*)(st + 5120) = acc_pack8<A_O + 40>();
      *(LAS u32x4*)(st + 6144) = acc_pack8<A_O + 48>();  *(LAS u32x4*)(st + 7168) = acc_pack8<A_O + 56>();
    } else {
      u32x4 w[8];
#pragma unroll
      for (int i = 0; i < 4; ++i) w[i] = *(LAS const u32x4*)(st + i * 1024);
      const float m = fmaxf(mlA[0], mfin);
      const float a = __builtin_amdgcn_exp2f(mlA[0] - m), e = __builtin_amdgcn_exp2f(mfin - m);
      const float inv = 1.0f / (mlA[1] * a + lfin * e);
      const float ca = a * inv, cb = e * inv;
      u16* dstp = CF + ((size_t)(((4 * qb + rr) * 32 + head) * 8) * 64 + ln) * 8;
      *(u32x4*)(dstp) = acc_combine8<A_O + 0>(w[0], ca, cb);          *(u32x4*)(dstp + 512) = acc_combine8<A_O + 8>(w[1], ca, cb);
      *(u32x4*)(dstp + 1024) = acc_combine8<A_O + 16>(w[2], ca, cb);  *(u32x4*)(dstp + 1536) = acc_combine8<A_O + 24>(w[3], ca, cb);
      SBAR();
#pragma unroll
      for (int i = 4; i < 8; ++i) w[i] = *(LAS const u32x4*)(st + i * 1024);
      *(u32x4*)(dstp + 2048) = acc_combine8<A_O + 32>(w[4], ca, cb);  *(u32x4*)(dstp + 2560) = acc_combine8<A_O + 40>(w[5], ca, cb);
      *(u32x4*)(dstp + 3072) = acc_combine8<A_O + 48>(w[6], ca, cb);  *(u32x4*)(dstp + 3584) = acc_combine8<A_O + 56>(w[7], ca, cb);
    }
    WAIT_L0();
  };
  f32x2 mlA = {0.f, 0.f};

  auto refix = [&](const bool first, const bool have_o) __attribute__((always_inline)) {
    const float rm = swap_max(rowmax32());
    const float dl = first ? fmaxf(rm, NEG_BIG) : fmaxf(rm, 0.f);
    const float f = first ? 0.f : __builtin_amdgcn_exp2f(-dl);
    mref = first ? dl : mref + dl;
    lsum = first ? 0.f : lsum * f;
    int ln = lane;
    asm volatile("" : "+v"(ln));
    const float aone = ln < 32 ? 1.f : 0.f;
    scores_add(aone, -dl);
    negm_set(aone, -mref);
    ACC_FENCE();
    if (have_o) {
      if (first) o_zero_(std::make_integer_sequence<int, 64>{});
      else o_scale_(f, std::make_integer_sequence<int, 64>{});
    }
    ACC_FENCE();
  };
#define TRAIL() do { pv_mfma<0, 28 % NRING, 0, 3>(); pv_mfma<0, 29 % NRING, 1, 3>(); pv_mfma<0, 30 % NRING, 2, 3>(); pv_mfma<0, 31 % NRING, 3, 3>(); SBAR(); } while (0)
  auto splain = [&]() __attribute__((always_inline)) {
    __builtin_amdgcn_s_setprio(SPRIO);
    TRAIL();
    float pst = exps32();
    if (__builtin_expect(__any(pst > P_LIMIT), 0)) { refix(false, true); pst = exps32(); }
    asm volatile("v_add_f32 %0, %0, %1" : "+v"(lsum) : "v"(pst));
    __builtin_amdgcn_s_setprio(0);
  };
  auto sgen = [&](const int tau) __attribute__((always_inline)) {
    __builtin_amdgcn_s_setprio(SPRIO);
    if (tau > 0) TRAIL();
    const bool last = tau == nH - 1 || tau == NSTEP - 1;
    const bool first = tau == nH || tau == 0;
    if (last) {
      int ln = lane;
      asm volatile("" : "+v"(ln));
      float ninf = -INFINITY;
      asm volatile("" : "+v"(ninf));
      const int dq = (ln & 31) - 4 * (ln >> 5);
      if (2 * c > rr) mask_all_<V_S0>(ninf, std::make_integer_sequence<int, 16>{});
      else if (2 * c == rr) mask_tri_<V_S0>(dq, ninf, std::make_integer_sequence<int, 16>{});
      if (2 * c + 1 > rr) mask_all_<V_S1>(ninf, std::make_integer_sequence<int, 16>{});
      else if (2 * c + 1 == rr) mask_tri_<V_S1>(dq, ninf, std::make_integer_sequence<int, 16>{});
    }
    if (tau == nH) epi(qbH, mlA);
    if (first) refix(true, tau > 0);
    float pst = exps32();
    if (!first && __any(pst > P_LIMIT)) { refix(false, true); pst = exps32(); }
    asm volatile("v_add_f32 %0, %0, %1" : "+v"(lsum) : "v"(pst));
    if (last) {
      int ln = lane;
      asm volatile("" : "+v"(ln));
      mfin = mref; lfin = swap_add(lsum);
      negm_set(0.f, 0.f);
      f32x2* lp = (f32x2*)(lamp + (tau == NSTEP - 1 ? 256 : 0)) + (ln & 31);
      if (c == 0) { if (ln < 32) *lp = (f32x2){mfin, lfin}; }
      else mlA = *lp;
      if (tau == nH - 1) q_load(lds0 + L_STASH + rr * 8192 + ln * 16);
    }
    __builtin_amdgcn_s_setprio(0);
  };
#define KVDMA(i) do { if ((i) == 0) dma1o<0>(rK, vlin, gk, lk); else if ((i) == 1) dma1o<1024>(rK, vlin, gk, lk); \
    else if ((i) == 2) dma1o<2048>(rK, vlin, gk, lk); else if ((i) == 3) dma1o<3072>(rK, vlin, gk, lk); \
    else if ((i) == 4) dma1o<0>(rV, vlin, gv, lv); else if ((i) == 5) dma1o<1024>(rV, vlin, gv, lv); \
    else if ((i) == 6) dma1o<2048>(rV, vlin, gv, lv); else dma1o<3072>(rV, vlin, gv, lv); } while (0)
#define MSTEP(J, FIRST, LAST, DMA) do { \
    constexpr int out_ = ((J) + PD - 1 < (LAST) ? (J) + PD - 1 : (LAST)) - (J);     \
    if constexpr ((J) < 2) qk_head<out_, (J) % NRING, 0, ((J) & 1) ? V_S1 : V_S0>(); \
    else if constexpr ((J) < 16) qk_mfma<out_, (J) % NRING, ((J) >> 1), ((J) & 1) ? V_S1 : V_S0>(); \
    else pv_mfma<out_, (J) % NRING, (J) & 3, (((J) - 16) >> 2)>(); \
    if constexpr (XREAD && (J) + PD <= (LAST)) frag_read<(J) + PD>(ka, va); \
    if constexpr (XDMA && (DMA) && ((J) & 1) && (J) < 16) KVDMA((J) >> 1); \
    SBAR(); } while (0)
#define MSTEP4(J, FIRST, LAST, DMA) MSTEP(J, FIRST, LAST, DMA); MSTEP((J) + 1, FIRST, LAST, DMA); MSTEP((J) + 2, FIRST, LAST, DMA); MSTEP((J) + 3, FIRST, LAST, DMA)

  if (c == 1) asm volatile("s_barrier" ::: "memory");
  {
    const unsigned ka = lrd + kslot0, va = 0;
    const unsigned gk = kvoff(1, c) + rr * 4096, gv = kvoff(0, c) + rr * 4096;
    const unsigned lk = lds0 + kslot1 + rr * 4096, lv = lds0 + vslot0 + rr * 4096;
    frag_read<0>(ka, va); frag_read<1>(ka, va); frag_read<2>(ka, va); frag_read<3>(ka, va);
    SBAR();
    MSTEP4(0, 0, 15, true); MSTEP4(4, 0, 15, true); MSTEP4(8, 0, 15, true); MSTEP4(12, 0, 15, true);
    BAR_A("lgkmcnt(0)");
    ACC_FENCE();
    sgen(0);
    BAR_A("vmcnt(0) lgkmcnt(0)");
  }
#define MPHASE(tau) do { \
      const unsigned ka = lrd + (((tau) & 1) ? kslot1 : kslot0); \
      const unsigned va = lrd + (((tau) & 1) ? vslot0 : vslot1);    \
        \
      const unsigned gk = (tau) + 1 < NSTEP ? kvoff((tau) + 1, c) + rr * 4096 : 0x7ff00000u; \
      const unsigned gv = kvoff((tau), c) + rr * 4096; \
      const unsigned lk = lds0 + (((tau) & 1) ? kslot0 : kslot1) + rr * 4096; \
      const unsigned lv = lds0 + (((tau) & 1) ? vslot1 : vslot0) + rr * 4096; \
      frag_read<0>(ka, va); frag_read<1>(ka, va); frag_read<2>(ka, va); frag_read<3>(ka, va); \
      SBAR(); \
      MSTEP4(0, 0, 31, true); MSTEP4(4, 0, 31, true); MSTEP4(8, 0, 31, true); MSTEP4(12, 0, 31, true); \
      MSTEP4(16, 0, 31, true); MSTEP4(20, 0, 31, true); MSTEP4(24, 0, 31, true); \
      BAR_A("lgkmcnt(0)"); } while (0)
  int tau = 1;
  while (tau < NSTEP) {
    if (tau == nH - 1 || tau == nH || tau == NSTEP - 1) {
      STAMP();
      MPHASE(tau);
      STAMP();
      sgen(tau);
      BAR_A("vmcnt(0) lgkmcnt(0)");
      ++tau;
    } else {
      const int stop = tau < nH - 1 ? nH - 1 : NSTEP - 1;
      do {
        STAMP();
        MPHASE(tau);
        STAMP();
        splain();
        BAR_A("vmcnt(0) lgkmcnt(0)");
        ++tau;
      } while (tau < stop);
    }
  }
#undef MPHASE
#undef TRAIL
  {
    const unsigned ka = 0, va = lrd + (((NSTEP - 1) & 1) ? vslot1 : vslot0);
    const unsigned gk = 0, gv = 0, lk = 0, lv = 0;
    (void)gk; (void)gv; (void)lk; (void)lv;
    frag_read<16>(ka, va); frag_read<17>(ka, va); frag_read<18>(ka, va); frag_read<19>(ka, va);
    SBAR();
    MSTEP4(16, 16, 31, false); MSTEP4(20, 16, 31, false); MSTEP4(24, 16, 31, false); MSTEP4(28, 16, 31, false);
    BAR_A("lgkmcnt(0)");
    epi(qbL, mlA);
    WAIT_L_BAR();
  }
#undef KVDMA
#undef MSTEP
#undef MSTEP4
  STAMP();
  if (c == 0) asm volatile("s_barrier" ::: "memory");
  STAMP_END(1, DBGP(CF, WS_CF));
}

constexpr int K3_STAGE = 37888, K3_NST = 4, K3_LDS = K3_STAGE * K3_NST;

__global__ __launch_bounds__(512, 2) void k3_oproj(const u16* __restrict__ CF, const u16* __restrict__ wo,
                                                   float* __restrict__ PT) {
  extern __shared__ __attribute__((aligned(16))) char smem[];
  LAS char* lds = (LAS char*)smem;
  const unsigned lds0 = (unsigned)(uintptr_t)lds;
  const int tid = threadIdx.x, lane = tid & 63;
  const int wid = RFL(tid >> 6);
  const int b = blockIdx.x, xcd = b & 7, idx = b >> 3;
  const int ks = xcd >> 1, panel = (xcd & 1) * 8 + (idx >> 2), nt = idx & 3;
  const int tbi = wid & 3, kp = wid >> 2;
  const int h = lane >> 5, g1 = (lane >> 4) & 1, q4 = (lane & 15) >> 2, p4 = lane & 3;
  const __amdgpu_buffer_rsrc_t rC = mk_rsrc(CF, NH * SEQ * HD * 2);
  const __amdgpu_buffer_rsrc_t rW = mk_rsrc(wo, NH * HD * DM * 2);
  const unsigned vlin = lane * 16;
  unsigned voffW[3];
#pragma unroll
  for (int i = 0; i < 3; ++i) {
    const int pc = wid + 8 * i;
    const int u = 2 * pc + (lane >> 5);
    const int kb = u / 5, nb = u % 5;
    voffW[i] = (unsigned)((kb * 8 + ((lane >> 2) & 7)) * 1280 + (nb * 32 + (lane & 3) * 8) * 2);
  }
  const bool lo = wid < 4;
  const unsigned sW0 = (unsigned)(ks * 1024 * 1280 + nt * 320);
  const unsigned sC0 = (unsigned)(((panel * 4 + tbi) * 256 + ks * 64) * 1024);
  const __amdgpu_buffer_rsrc_t r2 = lo ? rW : rC;
  const unsigned v2 = lo ? voffW[2] : vlin;
  const unsigned s2 = lo ? sW0 : sC0, st2 = lo ? 81920u : 4096u, l2 = lo ? (wid + 16) * 1024 : 20480 + tbi * 4096;
  const unsigned s3 = sC0 + (lo ? 3072u : 1024u), l3 = 20480 + tbi * 4096 + (lo ? 3072 : 1024);
  const unsigned s4 = lo ? 0x7ff00000u : sC0 + 2048u, l4 = lo ? 36864 : 20480 + tbi * 4096 + 2048;
  auto piece = [&](int j, int i) {
    const unsigned sb = lds0 + (j & 3) * K3_STAGE;
    if (i == 0) dma1(rW, voffW[0], sW0 + j * 81920, sb + wid * 1024);
    else if (i == 1) dma1(rW, voffW[1], sW0 + j * 81920, sb + (wid + 8) * 1024);
    else if (i == 2) dma1(r2, v2, s2 + j * st2, sb + l2);
    else if (i == 3) dma1(rC, vlin, s3 + j * 4096, sb + l3);
    else dma1(rC, vlin, s4 + j * 4096, sb + l4);
  };
  f32x16 acc[5];
#pragma unroll
  for (int n = 0; n < 5; ++n) acc[n] = f32x16{};
  const unsigned wbase = h * 256 + q4 * 64 + g1 * 32 + p4 * 8 + kp * 4 * 2560;
  const unsigned cbase = 20480 + tbi * 4096 + lane * 16 + kp * 2048;
  auto rd = [&](bf16x8& cf, bf16x8 (&w)[5], LAS const char* sb, int si) {
    cf = lds_b128(sb + cbase + si * 1024);
#pragma unroll
    for (int n = 0; n < 5; ++n)
      w[n] = lds_tr2(sb + wbase + (2 * si) * 2560 + n * 512, sb + wbase + (2 * si + 1) * 2560 + n * 512);
  };
  bf16x8 cA, cB, wA[5], wB[5];
  STAMP_DECL();
  STAMP();
#pragma unroll
  for (int j = 0; j < 3; ++j)
#pragma unroll
    for (int i = 0; i < 5; ++i) piece(j, i);
  STAMP();
#pragma unroll
  for (int j = 0; j < 16; ++j) {
    if (j + 2 < 16) { WAIT_VL_BAR(10); } else if (j + 1 < 16) { WAIT_VL_BAR(5); } else { WAIT_VL_BAR(0); }
    STAMP();
    LAS const char* sb = lds + (j & 3) * K3_STAGE;
    const bool more = j + 3 < 16;
    rd(cA, wA, sb, 0);
    SBAR();
    if (j > 0) {
      acc[0] = MFMA32(wB[0], cB, acc[0]); acc[1] = MFMA32(wB[1], cB, acc[1]);
      if (more) piece(j + 3, 0);
      SBAR();
      acc[2] = MFMA32(wB[2], cB, acc[2]); acc[3] = MFMA32(wB[3], cB, acc[3]);
      if (more) piece(j + 3, 1);
      SBAR();
      acc[4] = MFMA32(wB[4], cB, acc[4]);
    } else {
      piece(3, 0); piece(3, 1);
    }
    SBAR();
    rd(cB, wB, sb, 1);
    SBAR();
    acc[0] = MFMA32(wA[0], cA, acc[0]);
    if (more) piece(j + 3, 2);
    SBAR();
    acc[1] = MFMA32(wA[1], cA, acc[1]); acc[2] = MFMA32(wA[2], cA, acc[2]);
    if (more) piece(j + 3, 3);
    SBAR();
    acc[3] = MFMA32(wA[3], cA, acc[3]); acc[4] = MFMA32(wA[4], cA, acc[4]);
    if (more) piece(j + 3, 4);
    SBAR();
  }
#pragma unroll
  for (int n = 0; n < 5; ++n) acc[n] = MFMA32(wB[n], cB, acc[n]);
  STAMP();
  WAIT_L_BAR();
  if (kp == 1) {
    LAS char* dst = lds + tbi * 20480 + lane * 16;
#pragma unroll
    for (int n = 0; n < 5; ++n)
#pragma unroll
      for (int gq = 0; gq < 4; ++gq)
        *(LAS f32x4*)(dst + (n * 4 + gq) * 1024) =
            (f32x4){acc[n][4 * gq], acc[n][4 * gq + 1], acc[n][4 * gq + 2], acc[n][4 * gq + 3]};
  }
  WAIT_L_BAR();
  if (kp == 0) {
    LAS const char* src = lds + tbi * 20480 + lane * 16;
    const int tb32 = panel * 4 + tbi;
#pragma unroll
    for (int n = 0; n < 5; ++n)
#pragma unroll
      for (int gq = 0; gq < 4; ++gq) {
        const f32x4 v = *(LAS const f32x4*)(src + (n * 4 + gq) * 1024);
        const f32x4 o = {acc[n][4 * gq] + v[0], acc[n][4 * gq + 1] + v[1], acc[n][4 * gq + 2] + v[2],
                         acc[n][4 * gq + 3] + v[3]};
        *(f32x4*)(PT + ((size_t)((((ks * 64 + tb32) * 20 + nt * 5 + n) * 4 + gq) * 64 + lane)) * 4) = o;
      }
  }
  STAMP();
  STAMP();
  STAMP_END(2, DBGP(PT, WS_PT));
}

__global__ __launch_bounds__(256) void k4_ln(const u16* __restrict__ x, const float* __restrict__ PT,
                                             float* __restrict__ H, u16* __restrict__ HN) {
  __shared__ __attribute__((aligned(16))) u16 tile[8 * 648];
  __shared__ float red[2][4][8];
  const int tid = threadIdx.x, lane = tid & 63, wid = tid >> 6;
  const int b = blockIdx.x;
  const int r8 = lane & 7, hh = (lane >> 3) & 1, gq = lane >> 4;
  const int token = b * 8 + r8, tb32 = token >> 5, rr = token & 31;
  float v[5][4];
  float s = 0.f;
#pragma unroll
  for (int i = 0; i < 5; ++i) {
    const int mb = wid * 5 + i;
    const int m0 = mb * 32 + gq * 8 + hh * 4;
    const u32x2 xv = *(const u32x2*)(x + (size_t)token * DM + m0);
    f32x4 a = {bflo(xv[0]), bfhi(xv[0]), bflo(xv[1]), bfhi(xv[1])};
#pragma unroll
    for (int ks = 0; ks < 4; ++ks)
      a += *(const f32x4*)(PT + ((size_t)((((ks * 64 + tb32) * 20 + mb) * 4 + gq) * 64 + hh * 32 + rr)) * 4);
    *(f32x4*)(H + (size_t)token * DM + m0) = a;
#pragma unroll
    for (int e = 0; e < 4; ++e) { v[i][e] = a[e]; s += a[e]; }
  }
  s += __shfl_xor(s, 8); s += __shfl_xor(s, 16); s += __shfl_xor(s, 32);
  if (lane < 8) red[0][wid][lane] = s;
  __syncthreads();
  const float mean = (red[0][0][r8] + red[0][1][r8] + red[0][2][r8] + red[0][3][r8]) * (1.0f / DM);
  float sq = 0.f;
#pragma unroll
  for (int i = 0; i < 5; ++i)
#pragma unroll
    for (int e = 0; e < 4; ++e) { v[i][e] -= mean; sq += v[i][e] * v[i][e]; }
  sq += __shfl_xor(sq, 8); sq += __shfl_xor(sq, 16); sq += __shfl_xor(sq, 32);
  if (lane < 8) red[1][wid][lane] = sq;
  __syncthreads();
  const float var = (red[1][0][r8] + red[1][1][r8] + red[1][2][r8] + red[1][3][r8]) * (1.0f / DM);
  const float r1 = 1.0f / sqrtf(var + LN_EPS);
  const float r2 = 1.0f / sqrtf(var * r1 * r1 + LN_EPS);
  const float sc = r1 * r2;
#pragma unroll
  for (int i = 0; i < 5; ++i) {
    const int m0 = (wid * 5 + i) * 32 + gq * 8 + hh * 4;
    u32x2 w = {cvtpk(v[i][0] * sc, v[i][1] * sc), cvtpk(v[i][2] * sc, v[i][3] * sc)};
    *(u32x2*)(tile + r8 * 648 + m0) = w;
  }
  __syncthreads();
  const int ks32 = b >> 2, g = b & 3;
  for (int m = tid; m < DM; m += 256) {
    u16 e[8];
#pragma unroll
    for (int j = 0; j < 8; ++j) e[j] = tile[j * 648 + m];
    u32x4 w = {(unsigned)e[0] | ((unsigned)e[1] << 16), (unsigned)e[2] | ((unsigned)e[3] << 16),
               (unsigned)e[4] | ((unsigned)e[5] << 16), (unsigned)e[6] | ((unsigned)e[7] << 16)};
    *(u32x4*)(HN + ((size_t)(((m >> 4) * 64 + ks32) * 64 + g * 16 + (m & 15))) * 8) = w;
  }
}

constexpr int K56_LDS = 163840;

DI float gelu_tanh(float x) {
  const float u = 0.7978845608028654f * (x + 0.044715f * x * x * x);
  const float e = __builtin_amdgcn_exp2f(u * 2.8853900817779268f);
  const float t = 1.0f - 2.0f * __builtin_amdgcn_rcpf(e + 1.0f);
  return 0.5f * x * (1.0f + t);
}

#define ACLOB56 "a0","a1","a2","a3","a4","a5","a6","a7","a8","a9","a10","a11","a12","a13","a14","a15","a16","a17","a18","a19","a20","a21","a22","a23","a24","a25","a26","a27","a28","a29","a30","a31","a32","a33","a34","a35","a36","a37","a38","a39","a40","a41","a42","a43","a44","a45","a46","a47","a48","a49","a50","a51","a52","a53","a54","a55","a56","a57","a58","a59","a60","a61","a62","a63","a64","a65","a66","a67","a68","a69","a70","a71","a72","a73","a74","a75","a76","a77","a78","a79","a80","a81","a82","a83","a84","a85","a86","a87","a88","a89","a90","a91"
template <int R> DI void m_aload(__amdgpu_buffer_rsrc_t rsrc, unsigned voff, unsigned soff) {
  asm volatile("buffer_load_dwordx4 a[%c3:%c4], %0, %1, %2 offen" ::"v"(voff), "s"(rsrc), "s"(soff), "i"(R), "i"(R + 3) : "memory");
}
template <int R, int OFF> DI void m_trread(unsigned addr) {
  asm volatile("ds_read_b64_tr_b16 a[%c1:%c2], %0 offset:%c3" ::"v"(addr), "i"(R), "i"(R + 1), "i"(OFF) : "memory");
}
template <int RA, int RB> DI void m_mfma(f32x4& d) {
  asm volatile("v_mfma_f32_16x16x32_bf16 %0, a[%c1:%c2], a[%c3:%c4], %0" : "+v"(d) : "i"(RA), "i"(RA + 3), "i"(RB), "i"(RB + 3));
}

template <int MODE>
__global__ __launch_bounds__(512, 2) void k56_mix(const u16* __restrict__ W, const u16* __restrict__ AF,
                                                  const u16* __restrict__ bias, const float* __restrict__ H,
                                                  u16* __restrict__ outp) {
  constexpr int LDW = MODE == 0 ? FF : SEQ;
  constexpr int NKS = MODE == 0 ? 64 : 54;
  constexpr int NK = MODE == 0 ? 8 : 7;
  constexpr int KROWS = MODE == 0 ? SEQ : FF;
  extern __shared__ __attribute__((aligned(16))) char smem[];
  LAS char* lds = (LAS char*)smem;
  const unsigned lds0 = (unsigned)(uintptr_t)lds;
  const int tid = threadIdx.x, lane = tid & 63;
  const int wid = RFL(tid >> 6);
  const int b = blockIdx.x, xcd = b & 7, idx = b >> 3;
  int wt, dt;
  if (MODE == 0) {
    const int t = xcd * 27 + idx;
    wt = t >> 3; dt = t & 7;
  } else {
    wt = xcd * 4 + (idx >> 3); dt = idx & 7;
  }
  const __amdgpu_buffer_rsrc_t rW = mk_rsrc(W, KROWS * LDW * 2);
  const __amdgpu_buffer_rsrc_t rA = mk_rsrc(AF, 40 * NKS * 1024);
  const unsigned vlin = lane * 16;
  const int prow = (lane >> 2) & 7;
  const unsigned voffW0 = (unsigned)(prow * LDW * 2 + (lane >> 5) * 64 +
                                     (((lane & 3) ^ (MODE == 1 ? (((prow >> 2) & 1) << 1) : 0)) << 4));
  const unsigned voffW1 = MODE == 1 ? voffW0 : (voffW0 ^ 32u);
  const unsigned myl = lds0 + wid * 20480;
  const int g = lane >> 4, q4 = (lane & 15) >> 2, p4 = lane & 3;
  unsigned wb[2][2];
#pragma unroll
  for (int t = 0; t < 2; ++t)
#pragma unroll
    for (int hf = 0; hf < 2; ++hf) {
      const int kb = MODE == 0 ? g : 2 * t + (g >> 1);
      const int row = MODE == 0 ? 4 * t + q4 : 4 * (g & 1) + q4;
      const int sw = MODE == 0 ? (kb & 1) : ((row >> 2) & 1);
      wb[t][hf] = myl + kb * 1024 + row * 64 + ((hf ^ sw) * 32) + p4 * 8;
    }
  f32x4 acc[4][5];
#pragma unroll
  for (int i = 0; i < 4; ++i)
#pragma unroll
    for (int d = 0; d < 5; ++d) acc[i][d] = (f32x4){0.f, 0.f, 0.f, 0.f};
  STAMP_DECL();
  STAMP();
  u32x2 eb0[2], eb1[2];
  f32x4 eh[3]; u16 ebs[3];
  if (MODE == 0) {
#pragma unroll
    for (int i = 0; i < 2; ++i) {
      const int u = wid + 8 * i < 10 ? wid + 8 * i : wid;
      const int f0 = wt * 64 + (u / 5) * 32 + 4 * g;
      eb0[i] = *(const u32x2*)(bias + f0); eb1[i] = *(const u32x2*)(bias + f0 + 16);
    }
  } else {
#pragma unroll
    for (int i = 0; i < 3; ++i) {
      const int u = wid + 8 * i < 20 ? wid + 8 * i : wid;
      const int s = wt * 64 + (u / 5) * 16 + (lane & 15), d0 = dt * 80 + (u % 5) * 16 + 4 * g;
      ebs[i] = bias[s];
      eh[i] = *(const f32x4*)(H + (size_t)s * DM + d0);
    }
  }
  SBAR();
#define WDMA(IT, KB) dma1(rW, ((KB) & 1) ? voffW1 : voffW0, (unsigned)(((wid + 8 * (IT)) * 32 + (KB) * 8) * LDW * 2 + wt * 128), myl + ((IT) % 5) * 4096 + (KB) * 1024)
#define ALOAD(IT, D) m_aload<((IT) % 3) * 20 + 4 * (D)>(rA, vlin, (unsigned)(((dt * 5 + (D)) * NKS + wid + 8 * (IT)) * 1024))
#define WDMA4(IT) do { WDMA(IT, 0); WDMA(IT, 1); WDMA(IT, 2); WDMA(IT, 3); } while (0)
#define ALOAD5(IT) do { ALOAD(IT, 0); ALOAD(IT, 1); ALOAD(IT, 2); ALOAD(IT, 3); ALOAD(IT, 4); } while (0)
#define WREAD1(IT, I) do { m_trread<60 + 16 * ((IT) & 1) + 4 * (I), ((IT) % 5) * 4096 + ((I) >> 1) * 512>(wb[0][(I) & 1]); \
                           m_trread<60 + 16 * ((IT) & 1) + 4 * (I) + 2, ((IT) % 5) * 4096 + ((I) >> 1) * 512>(wb[1][(I) & 1]); } while (0)
#define WREAD(IT) do { WREAD1(IT, 0); WREAD1(IT, 1); WREAD1(IT, 2); WREAD1(IT, 3); } while (0)
#define MM(IT, I, D) do { if constexpr (MODE == 0) m_mfma<60 + 16 * ((IT) & 1) + 4 * (I), ((IT) % 3) * 20 + 4 * (D)>(acc[I][D]); \
                          else m_mfma<((IT) % 3) * 20 + 4 * (D), 60 + 16 * ((IT) & 1) + 4 * (I)>(acc[I][D]); } while (0)
#define MMROW(IT, I) do { MM(IT, I, 0); MM(IT, I, 1); MM(IT, I, 2); MM(IT, I, 3); MM(IT, I, 4); } while (0)
#define NV(IT) (((IT) + 4 < NK ? 4 : 0) + ((IT) + 2 < NK ? 5 : 0))
#define ALLOW(IT) ((IT) == 0 ? (5 + (NK > 2 ? 4 : 0) + (NK > 3 ? 4 : 0)) : (IT) == 1 ? ((NK > 3 ? 4 : 0) + NV(0)) : NV((IT) - 1))
#define STEP(IT) do { if constexpr ((IT) < NK) { \
    asm volatile("s_waitcnt vmcnt(%c0) lgkmcnt(0)" ::"i"(ALLOW(IT)) : "memory", ACLOB56); \
    if constexpr ((IT) + 1 < NK) WREAD((IT) + 1); \
    SBAR(); \
    MMROW(IT, 0); if constexpr ((IT) + 4 < NK) { WDMA((IT) + 4, 0); WDMA((IT) + 4, 1); } SBAR(); \
    MMROW(IT, 1); if constexpr ((IT) + 4 < NK) { WDMA((IT) + 4, 2); WDMA((IT) + 4, 3); } SBAR(); \
    MMROW(IT, 2); if constexpr ((IT) + 2 < NK) { ALOAD((IT) + 2, 0); ALOAD((IT) + 2, 1); ALOAD((IT) + 2, 2); } SBAR(); \
    MMROW(IT, 3); if constexpr ((IT) + 2 < NK) { ALOAD((IT) + 2, 3); ALOAD((IT) + 2, 4); } SBAR(); \
    } } while (0)
  asm volatile("" ::: "memory", ACLOB56);
  WDMA4(0); ALOAD5(0); WDMA4(1); ALOAD5(1); WDMA4(2); WDMA4(3);
  asm volatile("s_waitcnt vmcnt(22)" ::: "memory");
  WREAD(0);
  STAMP();
  STEP(0); STEP(1); STEP(2); STEP(3); STEP(4); STEP(5); STEP(6); STEP(7);
  asm volatile("s_nop 15" ::: "memory");
  STAMP();
#undef WDMA
#undef ALOAD
#undef WDMA4
#undef ALOAD5
#undef WREAD1
#undef WREAD
#undef MM
#undef MMROW
#undef NV
#undef ALLOW
#undef STEP
  WAIT_L_BAR();
  STAMP();
  {
    LAS char* dst = lds + wid * 20480 + lane * 16;
#pragma unroll
    for (int i = 0; i < 4; ++i)
#pragma unroll
      for (int d = 0; d < 5; ++d) *(LAS f32x4*)(dst + (i * 5 + d) * 1024) = acc[i][d];
  }
  WAIT_L_BAR();
  if (MODE == 0) {
#pragma unroll
    for (int i = 0; i < 2; ++i) {
      const int u = wid + 8 * i;
      if (u < 10) {
        const int a = u / 5, d = u % 5;
        f32x4 t0 = {0.f, 0.f, 0.f, 0.f}, t1 = {0.f, 0.f, 0.f, 0.f};
#pragma unroll
        for (int w = 0; w < 8; ++w) {
          t0 += *(LAS const f32x4*)(lds + w * 20480 + ((2 * a) * 5 + d) * 1024 + lane * 16);
          t1 += *(LAS const f32x4*)(lds + w * 20480 + ((2 * a + 1) * 5 + d) * 1024 + lane * 16);
        }
        const u32x2 b0 = eb0[i], b1 = eb1[i];
        t0 += (f32x4){bflo(b0[0]), bfhi(b0[0]), bflo(b0[1]), bfhi(b0[1])};
        t1 += (f32x4){bflo(b1[0]), bfhi(b1[0]), bflo(b1[1]), bfhi(b1[1])};
        u32x4 w = {cvtpk(gelu_tanh(t0[0]), gelu_tanh(t0[1])), cvtpk(gelu_tanh(t0[2]), gelu_tanh(t0[3])),
                   cvtpk(gelu_tanh(t1[0]), gelu_tanh(t1[1])), cvtpk(gelu_tanh(t1[2]), gelu_tanh(t1[3]))};
        *(u32x4*)(outp + ((size_t)(((dt * 5 + d) * 54 + wt * 2 + a) * 64 + lane)) * 8) = w;
      }
    }
  } else {
#pragma unroll
    for (int i = 0; i < 3; ++i) {
      const int u = wid + 8 * i;
      if (u < 20) {
        f32x4 t0 = {0.f, 0.f, 0.f, 0.f};
#pragma unroll
        for (int w = 0; w < 8; ++w) t0 += *(LAS const f32x4*)(lds + w * 20480 + u * 1024 + lane * 16);
        const int s = wt * 64 + (u / 5) * 16 + (lane & 15);
        const int d0 = dt * 80 + (u % 5) * 16 + 4 * g;
        const float bb = bf2f(ebs[i]);
        const f32x4 hv = eh[i];
        u32x2 w = {cvtpk(t0[0] + bb + hv[0], t0[1] + bb + hv[1]), cvtpk(t0[2] + bb + hv[2], t0[3] + bb + hv[3])};
        *(u32x2*)(outp + (size_t)s * DM + d0) = w;
      }
    }
  }
  STAMP();
  STAMP_END(4 + MODE, DBGP(AF, MODE == 0 ? WS_HN : WS_UF));
}

}

extern "C" void kernel_launch(void* const* d_in, const int* in_sizes, int n_in, void* d_out, int out_size, void* d_ws,
                              size_t ws_size, hipStream_t stream) {
  (void)in_sizes; (void)n_in; (void)out_size; (void)ws_size;
  const u16* x = (const u16*)d_in[0];
  const u16* ckv = (const u16*)d_in[1];
  const u16* wq = (const u16*)d_in[2];
  const u16* wuk = (const u16*)d_in[3];
  const u16* wuv = (const u16*)d_in[4];
  const u16* wo = (const u16*)d_in[5];
  const u16* w1 = (const u16*)d_in[7];
  const u16* b1 = (const u16*)d_in[8];
  const u16* w2 = (const u16*)d_in[9];
  const u16* b2 = (const u16*)d_in[10];
  char* ws = (char*)d_ws;
  u16* XN = (u16*)(ws + WS_XN);
  u16* KF = (u16*)(ws + WS_KF);
  u16* VF = (u16*)(ws + WS_VF);
  u16* CF = (u16*)(ws + WS_CF);
  float* PT = (float*)(ws + WS_PT);
  float* H = (float*)(ws + WS_H);
  u16* HN = (u16*)(ws + WS_HN);
  u16* UF = (u16*)(ws + WS_UF);
  (void)hipFuncSetAttribute((const void*)k1_kvproj, hipFuncAttributeMaxDynamicSharedMemorySize, K1_LDS);
  (void)hipFuncSetAttribute((const void*)k2_attn, hipFuncAttributeMaxDynamicSharedMemorySize, K2_LDS);
  (void)hipFuncSetAttribute((const void*)k3_oproj, hipFuncAttributeMaxDynamicSharedMemorySize, K3_LDS);
  (void)hipFuncSetAttribute((const void*)k56_mix<0>, hipFuncAttributeMaxDynamicSharedMemorySize, K56_LDS);
  (void)hipFuncSetAttribute((const void*)k56_mix<1>, hipFuncAttributeMaxDynamicSharedMemorySize, K56_LDS);
  k1_kvproj<<<256, 512, K1_LDS, stream>>>(x, ckv, wuk, wuv, KF, VF, XN);
  k2_attn<<<256, 512, K2_LDS, stream>>>(XN, wq, KF, VF, CF, (float*)(ws + WS_LAM));
  k3_oproj<<<256, 512, K3_LDS, stream>>>(CF, wo, PT);
  k4_ln<<<256, 256, 0, stream>>>(x, PT, H, HN);
  k56_mix<0><<<216, 512, K56_LDS, stream>>>(w1, HN, b1, nullptr, UF);
  k56_mix<1><<<256, 512, K56_LDS, stream>>>(w2, UF, b2, H, (u16*)d_out);
}
```

```cpp
#include <hip/hip_runtime.h>
#include <stdint.h>
#include <utility>

namespace {

typedef unsigned short u16;
typedef __bf16 bf16x8 __attribute__((ext_vector_type(8)));
typedef __bf16 bf16x2 __attribute__((ext_vector_type(2)));
typedef short s16x4 __attribute__((ext_vector_type(4)));
typedef short s16x8 __attribute__((ext_vector_type(8)));
typedef float f32x2 __attribute__((ext_vector_type(2)));
typedef float f32x4 __attribute__((ext_vector_type(4)));
typedef float f32x16 __attribute__((ext_vector_type(16)));
typedef unsigned u32x2 __attribute__((ext_vector_type(2)));
typedef unsigned u32x4 __attribute__((ext_vector_type(4)));
#define LAS __attribute__((address_space(3)))
#define DI __device__ __forceinline__
#define SBAR() __builtin_amdgcn_sched_barrier(0)
#define RFL(x) __builtin_amdgcn_readfirstlane(x)

constexpr int SEQ = 2048, DM = 640, DC = 512, NH = 32, HD = 128, FF = 1728;
constexpr float LN_EPS = 1e-5f;
constexpr float QSCALE = 0.08838834764831845f * 1.4426950408889634f;
constexpr float P_LIMIT = 1073741824.0f;
constexpr float NEG_BIG = -1e30f;

constexpr size_t WS_XN = 0;
constexpr size_t WS_KF = WS_XN + (size_t)SEQ * DM * 2;
constexpr size_t WS_VF = WS_KF + (size_t)NH * SEQ * HD * 2;
constexpr size_t WS_CF = WS_VF + (size_t)NH * SEQ * HD * 2;
constexpr size_t WS_PT = WS_CF + (size_t)NH * SEQ * HD * 2;
constexpr size_t WS_H = WS_PT + (size_t)4 * SEQ * DM * 2;
constexpr size_t WS_HN = WS_H + (size_t)SEQ * DM * 4;
constexpr size_t WS_UF = WS_HN + (size_t)SEQ * DM * 2;
constexpr size_t WS_LAM = WS_UF + (size_t)DM * FF * 2;
constexpr size_t WS_END = WS_LAM + 256 * 256 * 4;

DI __amdgpu_buffer_rsrc_t mk_rsrc(const void* p, unsigned bytes) {
  return __builtin_amdgcn_make_buffer_rsrc((void*)p, 0, bytes, 0x00020000);
}
DI void dma1(__amdgpu_buffer_rsrc_t rsrc, unsigned voff, unsigned soff, unsigned ldsaddr) {
  asm volatile("s_mov_b32 m0, %0\n\ts_nop 0\n\tbuffer_load_dwordx4 %1, %2, %3 offen lds"
               ::"s"(ldsaddr), "v"(voff), "s"(rsrc), "s"(soff) : "memory", "m0");
}
DI void dma4(__amdgpu_buffer_rsrc_t rsrc, unsigned voff, unsigned soff, unsigned ldsaddr) {
  asm volatile("s_mov_b32 m0, %0\n\ts_nop 0\n\t"
               "buffer_load_dwordx4 %1, %2, %3 offen lds\n\t"
               "buffer_load_dwordx4 %1, %2, %3 offen offset:1024 lds\n\t"
               "buffer_load_dwordx4 %1, %2, %3 offen offset:2048 lds\n\t"
               "buffer_load_dwordx4 %1, %2, %3 offen offset:3072 lds"
               ::"s"(ldsaddr), "v"(voff), "s"(rsrc), "s"(soff) : "memory", "m0");
}
DI void warm8k(__amdgpu_buffer_rsrc_t rsrc, unsigned voff128, unsigned soff, unsigned ldsaddr) {
  asm volatile("s_mov_b32 m0, %0\n\ts_nop 0\n\tbuffer_load_dword %1, %2, %3 offen lds"
               ::"s"(ldsaddr), "v"(voff128), "s"(rsrc), "s"(soff) : "memory", "m0");
}
#define WAIT_VL_BAR(N) asm volatile("s_waitcnt vmcnt(" #N ") lgkmcnt(0)\n\ts_barrier" ::: "memory")
#define WAIT_L_BAR() asm volatile("s_waitcnt lgkmcnt(0)\n\ts_barrier" ::: "memory")
#define WAIT_V(N) asm volatile("s_waitcnt vmcnt(" #N ")" ::: "memory")
#define WAIT_L0() asm volatile("s_waitcnt lgkmcnt(0)" ::: "memory")

DI bf16x8 lds_b128(LAS const char* p) { return *(LAS const bf16x8*)p; }
DI bf16x8 lds_tr2(LAS const char* p0, LAS const char* p1) {
  s16x4 lo = __builtin_amdgcn_ds_read_tr16_b64_v4i16((LAS s16x4*)p0);
  s16x4 hi = __builtin_amdgcn_ds_read_tr16_b64_v4i16((LAS s16x4*)p1);
  s16x8 w = __builtin_shufflevector(lo, hi, 0, 1, 2, 3, 4, 5, 6, 7);
  return __builtin_bit_cast(bf16x8, w);
}
DI unsigned cvtpk(float lo, float hi) {
  f32x2 v = {lo, hi};
  bf16x2 b = __builtin_convertvector(v, bf16x2);
  return __builtin_bit_cast(unsigned, b);
}
DI u32x4 pack8(const f32x16& a, int o) {
  u32x4 w = {cvtpk(a[o], a[o + 1]), cvtpk(a[o + 2], a[o + 3]), cvtpk(a[o + 4], a[o + 5]), cvtpk(a[o + 6], a[o + 7])};
  return w;
}
DI float bf2f(unsigned short b) { return __uint_as_float(((unsigned)b) << 16); }
DI float bflo(unsigned w) { return __uint_as_float(w << 16); }
DI float bfhi(unsigned w) { return __uint_as_float(w & 0xffff0000u); }
#define MFMA32(a, b, c) __builtin_amdgcn_mfma_f32_32x32x16_bf16((a), (b), (c), 0, 0, 0)
#define MFMA16(a, b, c) __builtin_amdgcn_mfma_f32_16x16x32_bf16((a), (b), (c), 0, 0, 0)
#define STAMP_DECL()
#define STAMP()
#define STAMP_END(kid, dbg)
DI float swap_max(float v) {
  auto rr = __builtin_amdgcn_permlane32_swap(__float_as_uint(v), __float_as_uint(v), false, false);
  return fmaxf(__uint_as_float(rr[0]), __uint_as_float(rr[1]));
}
DI float swap_add(float v) {
  auto rr = __builtin_amdgcn_permlane32_swap(__float_as_uint(v), __float_as_uint(v), false, false);
  return __uint_as_float(rr[0]) + __uint_as_float(rr[1]);
}

constexpr int K1_LDS = 4 * 32768;
#define K1ROT 0

template <bool ISV>
DI void k1_body(LAS char* lds, unsigned lds0, int wid, int lane, int head, int tb, const u16* __restrict__ ckv,
                const u16* __restrict__ wsrc, const u16* __restrict__ x, u16* __restrict__ KF, u16* __restrict__ VF,
                u16* __restrict__ XN, const u16* __restrict__ wq
                ) {
  const int wm = wid >> 2, wn = wid & 3;
  const int r = lane & 31, h = lane >> 5;
  const int g1 = (lane >> 4) & 1, q = (lane & 15) >> 2, p = lane & 3;
  const __amdgpu_buffer_rsrc_t rA = mk_rsrc(ckv, SEQ * DC * 2);
  const __amdgpu_buffer_rsrc_t rWm = mk_rsrc(wsrc, DC * NH * HD * 2);
  const unsigned voffA = (lane >> 2) * 1024 + (((lane & 3) ^ ((lane >> 4) & 3)) << 4);
  const unsigned voffW = ((lane >> 2) & 7) * 8192 + (lane >> 5) * 64 + (lane & 3) * 16;
  auto piece = [&](int kt0, int i) {
    const unsigned sb = lds0 + (kt0 & 3) * 32768;
    const int kt = (kt0 + K1ROT) & 15;
    if (i < 2) dma1(rA, voffA, (unsigned)((tb * 256 + (wid * 2 + i) * 16) * 1024 + kt * 64), sb + (wid * 2 + i) * 1024);
    else dma1(rWm, voffW, (unsigned)((kt * 32 + (wid >> 1) * 8) * 8192 + head * 256 + (i - 2) * 128),
              sb + 16384 + ((wid >> 1) * 4 + (wid & 1) * 2 + (i - 2)) * 1024);
  };
  unsigned abase[2];
#pragma unroll
  for (int s = 0; s < 2; ++s) abase[s] = wm * 8192 + r * 64 + (((2 * s + h) ^ ((r >> 2) & 3)) << 4);
  const unsigned wbase = 16384 + h * 4096 + wn * 1024 + q * 64 + g1 * 32 + p * 8;

  f32x16 acc[4][2];
#pragma unroll
  for (int i = 0; i < 4; ++i)
#pragma unroll
    for (int n = 0; n < 2; ++n) acc[i][n] = f32x16{};
  auto rd = [&](bf16x8 (&a)[4], bf16x8 (&w)[2], LAS const char* sb, int s) {
#pragma unroll
    for (int n = 0; n < 2; ++n) w[n] = lds_tr2(sb + wbase + s * 8192 + n * 512, sb + wbase + s * 8192 + n * 512 + 256);
#pragma unroll
    for (int i = 0; i < 4; ++i) a[i] = lds_b128(sb + abase[s] + i * 2048);
  };
  auto mm2 = [&](const bf16x8 (&a)[4], const bf16x8 (&w)[2], int i) {
#pragma unroll
    for (int n = 0; n < 2; ++n) {
      if (ISV) acc[i][n] = MFMA32(a[i], w[n], acc[i][n]);
      else acc[i][n] = MFMA32(w[n], a[i], acc[i][n]);
    }
  };
  bf16x8 aA[4], wA[2], aB[4], wB[2];
  STAMP_DECL();
  STAMP();
#pragma unroll
  for (int kt = 0; kt < 3; ++kt)
#pragma unroll
    for (int i = 0; i < 4; ++i) piece(kt, i);
  STAMP();
#pragma unroll
  for (int kt = 0; kt < 16; ++kt) {
    if (kt < 14) { WAIT_VL_BAR(8); } else if (kt == 14) { WAIT_VL_BAR(4); } else { WAIT_VL_BAR(0); }
    STAMP();
    LAS const char* sb = lds + (kt & 3) * 32768;
    const bool more = kt + 3 < 16;
    rd(aA, wA, sb, 0);
    SBAR();
    if (kt > 0) {
      mm2(aB, wB, 0); mm2(aB, wB, 1);
      if (more) piece(kt + 3, 0);
      SBAR();
      mm2(aB, wB, 2); mm2(aB, wB, 3);
      if (more) piece(kt + 3, 1);
    } else {
      piece(3, 0); piece(3, 1);
    }
    SBAR();
    rd(aB, wB, sb, 1);
    SBAR();
    mm2(aA, wA, 0); mm2(aA, wA, 1);
    if (more) piece(kt + 3, 2);
    SBAR();
    mm2(aA, wA, 2); mm2(aA, wA, 3);
    if (more) piece(kt + 3, 3);
    SBAR();
  }
  mm2(aB, wB, 0); mm2(aB, wB, 1); mm2(aB, wB, 2); mm2(aB, wB, 3);
  STAMP();
  {
    const __amdgpu_buffer_rsrc_t rQ = mk_rsrc(wq, DM * NH * HD * 2);
    const int u = (blockIdx.x >> 3) * 8 + wid;
    if (u < 80) warm8k(rQ, (unsigned)((lane >> 3) * 8192 + (lane & 7) * 128), (unsigned)(u * 8 * 8192 + (blockIdx.x & 7) * 1024), lds0);
  }
  const int token = blockIdx.x * 8 + wid;
  const bool has1 = lane < 16;
  const u32x4 v0 = *(const u32x4*)(x + (size_t)token * DM + lane * 8);
  u32x4 v1 = {0u, 0u, 0u, 0u};
  if (has1) v1 = *(const u32x4*)(x + (size_t)token * DM + 512 + lane * 8);
  SBAR();
#pragma unroll
  for (int i = 0; i < 4; ++i) {
    const int kb32 = tb * 8 + wm * 4 + i;
#pragma unroll
    for (int n = 0; n < 2; ++n) {
#pragma unroll
      for (int sg = 0; sg < 2; ++sg) {
        const u32x4 w = pack8(acc[i][n], 8 * sg);
        if (ISV) {
          const int db = 2 * (wn - 2) + n;
          *(u32x4*)(VF + ((size_t)((((head * 64 + kb32) * 4 + db) * 2 + sg) * 64 + lane)) * 8) = w;
        } else {
          const int db = 2 * wn + n;
          *(u32x4*)(KF + ((size_t)(((head * 64 + kb32) * 8 + 2 * db + sg) * 64 + lane)) * 8) = w;
        }
      }
    }
  }
  STAMP();
  {
    float f0[8], f1[8];
#pragma unroll
    for (int j = 0; j < 4; ++j) {
      f0[2 * j] = bflo(v0[j]); f0[2 * j + 1] = bfhi(v0[j]);
      f1[2 * j] = bflo(v1[j]); f1[2 * j + 1] = bfhi(v1[j]);
    }
    float s = 0.f;
#pragma unroll
    for (int j = 0; j < 8; ++j) s += f0[j] + f1[j];
#pragma unroll
    for (int o = 1; o < 64; o <<= 1) s += __shfl_xor(s, o);
    const float mean = s * (1.0f / DM);
    float sq = 0.f;
#pragma unroll
    for (int j = 0; j < 8; ++j) {
      const float d0 = f0[j] - mean; sq += d0 * d0;
      const float d1 = has1 ? f1[j] - mean : 0.f; sq += d1 * d1;
    }
#pragma unroll
    for (int o = 1; o < 64; o <<= 1) sq += __shfl_xor(sq, o);
    const float rstd = 1.0f / sqrtf(sq * (1.0f / DM) + LN_EPS);
    const int tb32 = token >> 5, rr = token & 31;
    u32x4 o0, o1;
#pragma unroll
    for (int j = 0; j < 4; ++j) {
      o0[j] = cvtpk((f0[2 * j] - mean) * rstd, (f0[2 * j + 1] - mean) * rstd);
      o1[j] = cvtpk((f1[2 * j] - mean) * rstd, (f1[2 * j + 1] - mean) * rstd);
    }
    *(u32x4*)(XN + ((size_t)((tb32 * 40 + (lane >> 1)) * 64 + (lane & 1) * 32 + rr)) * 8) = o0;
    if (has1) *(u32x4*)(XN + ((size_t)((tb32 * 40 + 32 + (lane >> 1)) * 64 + (lane & 1) * 32 + rr)) * 8) = o1;
  }
  STAMP();
  STAMP();
  STAMP_END(0, dbgp);
}

__global__ __launch_bounds__(512, 2) void k1_kvproj(const u16* __restrict__ x, const u16* __restrict__ ckv,
                                                    const u16* __restrict__ wuk, const u16* __restrict__ wuv,
                                                    u16* __restrict__ KF, u16* __restrict__ VF, u16* __restrict__ XN,
                                                    const u16* __restrict__ wq) {
  extern __shared__ __attribute__((aligned(16))) char smem[];
  LAS char* lds = (LAS char*)smem;
  const unsigned lds0 = (unsigned)(uintptr_t)lds;
  const int tid = threadIdx.x, lane = tid & 63;
  const int wid = RFL(tid >> 6);
  const int b = blockIdx.x, xcd = b & 7, idx = b >> 3;
  const int head = xcd * 4 + (idx & 3), tb = idx >> 2;
  const u16* wsrc = (wid & 1) ? wuv : wuk;
#define K1_EXTRA
  if ((wid & 3) >= 2) k1_body<true>(lds, lds0, wid, lane, head, tb, ckv, wsrc, x, KF, VF, XN, wq K1_EXTRA);
  else k1_body<false>(lds, lds0, wid, lane, head, tb, ckv, wsrc, x, KF, VF, XN, wq K1_EXTRA);
}

constexpr int K2_LDS = 163840;
constexpr unsigned L_KB1 = 0, L_VB0 = 16384, L_VA1 = 32768, L_KA0 = 49152, L_KB0 = 65536, L_KA1 = 81920, L_VA0 = 98304,
                   L_VB1 = 114688, L_STASH = 131072;
constexpr int NSTEP = 17;
#define SPRIO 2

template <int OFF>
DI void dma1o(__amdgpu_buffer_rsrc_t rsrc, unsigned voff, unsigned soff, unsigned ldsaddr) {
  asm volatile("s_mov_b32 m0, %0\n\ts_nop 0\n\tbuffer_load_dwordx4 %1, %2, %3 offen offset:%4 lds"
               ::"s"(ldsaddr), "v"(voff), "s"(rsrc), "s"(soff), "i"(OFF) : "memory", "m0");
}
#define XEXP(x) __builtin_amdgcn_exp2f(x)
#define MX3(a, b, c) __builtin_fmaxf(__builtin_fmaxf((a), (b)), (c))
#define PIN(x) asm volatile("" : "+v"(x))
DI float fmul_s(float a, float b) { float r; asm("v_mul_f32 %0, %1, %2" : "=v"(r) : "v"(a), "v"(b)); return r; }
DI float fadd_s(float a, float b) { float r; asm("v_add_f32 %0, %1, %2" : "=v"(r) : "v"(a), "v"(b)); return r; }
DI float fsub_s(float a, float b) { float r; asm("v_sub_f32 %0, %1, %2" : "=v"(r) : "v"(a), "v"(b)); return r; }
DI void add_in(f32x16& s, const f32x16& t) {
#pragma unroll
  for (int e = 0; e < 16; ++e) asm volatile("v_add_f32 %0, %0, %1" : "+v"(s[e]) : "v"(t[e]));
}
DI void add_in(f32x16& s, float t) {
#pragma unroll
  for (int e = 0; e < 16; ++e) asm volatile("v_add_f32 %0, %0, %1" : "+v"(s[e]) : "v"(t));
}
DI void sub_in(f32x16& s, float t) {
#pragma unroll
  for (int e = 0; e < 16; ++e) asm volatile("v_sub_f32 %0, %0, %1" : "+v"(s[e]) : "v"(t));
}
DI void set_in(f32x16& s, float t) {
#pragma unroll
  for (int e = 0; e < 16; ++e) asm volatile("v_mov_b32 %0, %1" : "+v"(s[e]) : "v"(t));
}
DI float ffma_s(float a, float b, float c) { float r; asm("v_fma_f32 %0, %1, %2, %3" : "=v"(r) : "v"(a), "v"(b), "v"(c)); return r; }
#define ACLOB "a8","a9","a10","a11","a12","a13","a14","a15","a16","a17","a18","a19","a20","a21","a22","a23","a24","a25","a26","a27","a28","a29","a30","a31","a32","a33","a34","a35","a36","a37","a38","a39","a40","a41","a42","a43","a44","a45","a46","a47","a48","a49","a50","a51","a52","a53","a54","a55","a56","a57","a58","a59","a60","a61","a62","a63","a64","a65","a66","a67","a68","a69","a70","a71","a72","a73","a74","a75","a76","a77","a78","a79","a80","a81","a82","a83","a84","a85","a86","a87","a88","a89","a90","a91","a92","a93","a94","a95","a96","a97","a98","a99","a100","a101","a102","a103","a104","a105","a106","a107","a108","a109","a110","a111","a112","a113","a114","a115","a116","a117","a118","a119","a120","a121","a122","a123","a124","a125","a126","a127"
constexpr int A_O = 8, A_Q = 72, A_R = 104, NRING = 6;
template <int SLOT, int OFF> DI void ring_read(unsigned addr) {
  asm volatile("ds_read_b128 a[%c1:%c2], %0 offset:%c3" ::"v"(addr), "i"(A_R + 4 * SLOT), "i"(A_R + 4 * SLOT + 3), "i"(OFF) : "memory");
}
template <int S, int OFF> DI void q_read(unsigned addr) {
  asm volatile("ds_read_b128 a[%c1:%c2], %0 offset:%c3" ::"v"(addr), "i"(A_Q + 4 * S), "i"(A_Q + 4 * S + 3), "i"(OFF) : "memory");
}
constexpr int V_NEGM = 64, V_S0 = 80, V_S1 = 96, V_PF = 112;
template <int W, int SLOT, int S, int DST> DI void qk_head() {
  asm volatile("s_waitcnt lgkmcnt(%c0)\n\tv_mfma_f32_32x32x16_bf16 v[%c1:%c2], a[%c3:%c4], a[%c5:%c6], v[%c7:%c8]"
               ::"i"(W), "i"(DST), "i"(DST + 15), "i"(A_R + 4 * SLOT), "i"(A_R + 4 * SLOT + 3), "i"(A_Q + 4 * S), "i"(A_Q + 4 * S + 3),
                 "i"(V_NEGM), "i"(V_NEGM + 15));
}
template <int W, int SLOT, int S, int DST> DI void qk_mfma() {
  asm volatile("s_waitcnt lgkmcnt(%c0)\n\tv_mfma_f32_32x32x16_bf16 v[%c1:%c2], a[%c3:%c4], a[%c5:%c6], v[%c1:%c2]"
               ::"i"(W), "i"(DST), "i"(DST + 15), "i"(A_R + 4 * SLOT), "i"(A_R + 4 * SLOT + 3), "i"(A_Q + 4 * S), "i"(A_Q + 4 * S + 3));
}
template <int W, int SLOT, int D, int P> DI void pv_mfma() {
  asm volatile("s_waitcnt lgkmcnt(%c0)\n\tv_mfma_f32_32x32x16_bf16 a[%c1:%c2], a[%c3:%c4], v[%c5:%c6], a[%c1:%c2]"
               ::"i"(W), "i"(A_O + 16 * D), "i"(A_O + 16 * D + 15), "i"(A_R + 4 * SLOT), "i"(A_R + 4 * SLOT + 3), "i"(V_PF + 4 * P),
                 "i"(V_PF + 4 * P + 3));
}
template <int W, int SLOT, int D, int P> DI void pv_zero() {
  asm volatile("s_waitcnt lgkmcnt(%c0)\n\tv_mfma_f32_32x32x16_bf16 a[%c1:%c2], a[%c3:%c4], v[%c5:%c6], 0"
               ::"i"(W), "i"(A_O + 16 * D), "i"(A_O + 16 * D + 15), "i"(A_R + 4 * SLOT), "i"(A_R + 4 * SLOT + 3), "i"(V_PF + 4 * P),
                 "i"(V_PF + 4 * P + 3));
}
template <int S, int P> DI void exps8(float& ps0, float& ps1, float& ps2, float& ps3) {
  float t0, t1, t2, t3, t4, t5, t6, t7;
  asm volatile("v_exp_f32 %4, v[%c12]\n\tv_exp_f32 %5, v[%c12+1]\n\tv_exp_f32 %6, v[%c12+2]\n\tv_exp_f32 %7, v[%c12+3]\n\t"
               "v_exp_f32 %8, v[%c12+4]\n\tv_exp_f32 %9, v[%c12+5]\n\tv_exp_f32 %10, v[%c12+6]\n\tv_exp_f32 %11, v[%c12+7]\n\t"
               "v_add_f32 %0, %0, %4\n\tv_add_f32 %1, %1, %5\n\tv_cvt_pk_bf16_f32 v[%c13], %4, %5\n\t"
               "v_add_f32 %2, %2, %6\n\tv_add_f32 %3, %3, %7\n\tv_cvt_pk_bf16_f32 v[%c13+1], %6, %7\n\t"
               "v_add_f32 %0, %0, %8\n\tv_add_f32 %1, %1, %9\n\tv_cvt_pk_bf16_f32 v[%c13+2], %8, %9\n\t"
               "v_add_f32 %2, %2, %10\n\tv_add_f32 %3, %3, %11\n\tv_cvt_pk_bf16_f32 v[%c13+3], %10, %11"
               : "+v"(ps0), "+v"(ps1), "+v"(ps2), "+v"(ps3), "=&v"(t0), "=&v"(t1), "=&v"(t2), "=&v"(t3), "=&v"(t4), "=&v"(t5),
                 "=&v"(t6), "=&v"(t7)
               : "i"(S), "i"(V_PF + 4 * P));
}
DI float exps32() {
  float ps0 = 0.f, ps1 = 0.f, ps2 = 0.f, ps3 = 0.f, r;
  exps8<V_S0, 0>(ps0, ps1, ps2, ps3); exps8<V_S0 + 8, 1>(ps0, ps1, ps2, ps3);
  exps8<V_S1, 2>(ps0, ps1, ps2, ps3); exps8<V_S1 + 8, 3>(ps0, ps1, ps2, ps3);
  asm volatile("v_add_f32 %1, %1, %2\n\tv_add_f32 %3, %3, %4\n\tv_add_f32 %0, %1, %3" : "=v"(r), "+v"(ps0), "+v"(ps1), "+v"(ps2), "+v"(ps3));
  return r;
}
DI float rowmax32() {
  float a, b;
  asm volatile("v_max3_f32 %0, v80, v81, v82\n\tv_max3_f32 %1, v83, v84, v85\n\tv_max3_f32 %0, %0, v86, v87\n\tv_max3_f32 %1, %1, v88, v89\n\t"
               "v_max3_f32 %0, %0, v90, v91\n\tv_max3_f32 %1, %1, v92, v93\n\tv_max3_f32 %0, %0, v94, v95\n\tv_max3_f32 %1, %1, v96, v97\n\t"
               "v_max3_f32 %0, %0, v98, v99\n\tv_max3_f32 %1, %1, v100, v101\n\tv_max3_f32 %0, %0, v102, v103\n\tv_max3_f32 %1, %1, v104, v105\n\t"
               "v_max3_f32 %0, %0, v106, v107\n\tv_max3_f32 %1, %1, v108, v109\n\tv_max3_f32 %0, %0, v110, v111\n\tv_max_f32 %0, %0, %1"
               : "=&v"(a), "=&v"(b));
  return a;
}
template <int R> DI void vreg_add(float t) { asm volatile("v_add_f32 v[%c1], v[%c1], %0" ::"v"(t), "i"(R)); }
template <int S, int... E> DI void mask_all_(float ninf, std::integer_sequence<int, E...>) { (vreg_add<S + E>(ninf), ...); }
template <int S, int... E> DI void mask_tri_(int dq, float ninf, std::integer_sequence<int, E...>) {
  (vreg_add<S + E>(((E & 3) + 8 * (E >> 2) > dq) ? ninf : 0.f), ...);
}
template <int I> DI float acc_rd() { float r; asm volatile("v_accvgpr_read_b32 %0, a[%c1]" : "=v"(r) : "i"(I)); return r; }
template <int I> DI void acc_wr(float v) { asm volatile("v_accvgpr_write_b32 a[%c1], %0" ::"v"(v), "i"(I)); }
template <int D, int... E> DI void o_read_(f32x16& t, std::integer_sequence<int, E...>) { ((t[E] = acc_rd<A_O + 16 * D + E>()), ...); }
template <int D> DI f32x16 o_read() { f32x16 t; o_read_<D>(t, std::make_integer_sequence<int, 16>{}); return t; }
template <int... E> DI void o_zero_(std::integer_sequence<int, E...>) { (acc_wr<A_O + E>(0.f), ...); }
template <int... E> DI void o_scale_(float f, std::integer_sequence<int, E...>) { (acc_wr<A_O + E>(fmul_s(acc_rd<A_O + E>(), f)), ...); }
template <int R> DI u32x4 acc_scale8(float f) {
  u32x4 o; float t0, t1, t2, t3, t4, t5, t6, t7;
  asm volatile("v_accvgpr_read_b32 %4, a[%c13]\n\tv_accvgpr_read_b32 %5, a[%c13+1]\n\tv_accvgpr_read_b32 %6, a[%c13+2]\n\t"
               "v_accvgpr_read_b32 %7, a[%c13+3]\n\tv_accvgpr_read_b32 %8, a[%c13+4]\n\tv_accvgpr_read_b32 %9, a[%c13+5]\n\t"
               "v_accvgpr_read_b32 %10, a[%c13+6]\n\tv_accvgpr_read_b32 %11, a[%c13+7]\n\t"
               "v_mul_f32 %4, %4, %12\n\tv_mul_f32 %5, %5, %12\n\tv_mul_f32 %6, %6, %12\n\tv_mul_f32 %7, %7, %12\n\t"
               "v_mul_f32 %8, %8, %12\n\tv_mul_f32 %9, %9, %12\n\tv_mul_f32 %10, %10, %12\n\tv_mul_f32 %11, %11, %12\n\t"
               "v_cvt_pk_bf16_f32 %0, %4, %5\n\tv_cvt_pk_bf16_f32 %1, %6, %7\n\tv_cvt_pk_bf16_f32 %2, %8, %9\n\tv_cvt_pk_bf16_f32 %3, %10, %11"
               : "=&v"(o[0]), "=&v"(o[1]), "=&v"(o[2]), "=&v"(o[3]), "=&v"(t0), "=&v"(t1), "=&v"(t2), "=&v"(t3), "=&v"(t4),
                 "=&v"(t5), "=&v"(t6), "=&v"(t7)
               : "v"(f), "i"(R));
  return o;
}
template <int R, int OFF> DI void acc_to_lds(unsigned addr) {
  asm volatile("ds_write_b128 %0, a[%c1:%c2] offset:%c3" ::"v"(addr), "i"(R), "i"(R + 3), "i"(OFF) : "memory");
}
template <int R> DI u32x4 acc_combine8f(const f32x4& w0, const f32x4& w1, float cw, float ca) {
  u32x4 o; float t0, t1, t2, t3, t4, t5, t6, t7;
  asm volatile("v_accvgpr_read_b32 %4, a[%c22]\n\tv_accvgpr_read_b32 %5, a[%c22+1]\n\tv_accvgpr_read_b32 %6, a[%c22+2]\n\t"
               "v_accvgpr_read_b32 %7, a[%c22+3]\n\tv_accvgpr_read_b32 %8, a[%c22+4]\n\tv_accvgpr_read_b32 %9, a[%c22+5]\n\t"
               "v_accvgpr_read_b32 %10, a[%c22+6]\n\tv_accvgpr_read_b32 %11, a[%c22+7]\n\t"
               "v_mul_f32 %4, %4, %21\n\tv_mul_f32 %5, %5, %21\n\tv_mul_f32 %6, %6, %21\n\tv_mul_f32 %7, %7, %21\n\t"
               "v_mul_f32 %8, %8, %21\n\tv_mul_f32 %9, %9, %21\n\tv_mul_f32 %10, %10, %21\n\tv_mul_f32 %11, %11, %21\n\t"
               "v_fma_f32 %4, %12, %20, %4\n\tv_fma_f32 %5, %13, %20, %5\n\tv_fma_f32 %6, %14, %20, %6\n\tv_fma_f32 %7, %15, %20, %7\n\t"
               "v_fma_f32 %8, %16, %20, %8\n\tv_fma_f32 %9, %17, %20, %9\n\tv_fma_f32 %10, %18, %20, %10\n\tv_fma_f32 %11, %19, %20, %11\n\t"
               "v_cvt_pk_bf16_f32 %0, %4, %5\n\tv_cvt_pk_bf16_f32 %1, %6, %7\n\tv_cvt_pk_bf16_f32 %2, %8, %9\n\tv_cvt_pk_bf16_f32 %3, %10, %11"
               : "=&v"(o[0]), "=&v"(o[1]), "=&v"(o[2]), "=&v"(o[3]), "=&v"(t0), "=&v"(t1), "=&v"(t2), "=&v"(t3), "=&v"(t4),
                 "=&v"(t5), "=&v"(t6), "=&v"(t7)
               : "v"(w0[0]), "v"(w0[1]), "v"(w0[2]), "v"(w0[3]), "v"(w1[0]), "v"(w1[1]), "v"(w1[2]), "v"(w1[3]), "v"(cw), "v"(ca),
                 "i"(R));
  return o;
}
DI void scores_add(float aone, float v) {
  asm volatile("s_nop 1\n\tv_mfma_f32_32x32x2_f32 v[80:95], %0, %1, v[80:95]\n\tv_mfma_f32_32x32x2_f32 v[96:111], %0, %1, v[96:111]" ::"v"(aone), "v"(v));
}
DI void negm_set(float aone, float v) { asm volatile("s_nop 1\n\tv_mfma_f32_32x32x2_f32 v[64:79], %0, %1, 0" ::"v"(aone), "v"(v)); }
#define ACC_FENCE() asm volatile("s_nop 15\n\ts_nop 3" ::: "memory")
#define BAR_A(WAITS) asm volatile("s_waitcnt " WAITS "\n\ts_barrier" ::: "memory", "v127", ACLOB)

template <int J> DI void frag_read(unsigned ka, unsigned va) {
  if constexpr (J < 16) ring_read<J % NRING, (J & 1) * 8192 + (J >> 1) * 1024>(ka);
  else { constexpr int i = J - 16; ring_read<J % NRING, (i >> 3) * 8192 + ((i & 3) * 2 + ((i >> 2) & 1)) * 1024>(va); }
}
constexpr bool XREAD = true;
constexpr bool XDMA = true;
constexpr int PD = 4;

__global__ __launch_bounds__(512, 2) __attribute__((amdgpu_num_vgpr(64))) void k2_attn(const u16* __restrict__ XN, const u16* __restrict__ wq,
                                                  const u16* __restrict__ KF, const u16* __restrict__ VF,
                                                  u16* __restrict__ CF, float* __restrict__ LAM,
                                                  const u16* __restrict__ wo) {
  extern __shared__ __attribute__((aligned(16))) char smem[];
  LAS char* lds = (LAS char*)smem;
  const unsigned lds0 = (unsigned)(uintptr_t)lds;
  const int tid = threadIdx.x, lane = tid & 63;
  const int wid = RFL(tid >> 6);
  const int b = blockIdx.x, xcd = b & 7, idx = b >> 3;
  const int head = xcd * 4 + (idx >> 3), px = idx & 7;
  const int qbH = 15 - px, qbL = px;
  const int rr = wid & 3, c = wid >> 2;
  const int r = lane & 31, h = lane >> 5;
  const int g1 = (lane >> 4) & 1, q4 = (lane & 15) >> 2, p4 = lane & 3;
  const __amdgpu_buffer_rsrc_t rX = mk_rsrc(XN, SEQ * DM * 2);
  const __amdgpu_buffer_rsrc_t rW = mk_rsrc(wq, DM * NH * HD * 2);
  const __amdgpu_buffer_rsrc_t rK = mk_rsrc(KF, NH * SEQ * HD * 2);
  const __amdgpu_buffer_rsrc_t rV = mk_rsrc(VF, NH * SEQ * HD * 2);
  const unsigned vlin = lane * 16;
  const unsigned voffW = ((lane >> 2) & 7) * 8192 + (lane >> 5) * 64 + (lane & 3) * 16;
  const int p1 = 2 * (px + 1);
  auto kvoff = [&](int sg) -> unsigned {
    const int hidx = sg < p1 ? sg : 2 * sg - p1 + c;
    return (unsigned)((head * 64 + 2 * hidx) * 8192);
  };
  STAMP_DECL();
  STAMP();
  const int tbx = (wid < 4) ? (4 * qbH + wid) : (4 * qbL + wid - 4);
  auto q_load = [&](unsigned addr) {
    q_read<0, 0>(addr); q_read<1, 1024>(addr); q_read<2, 2048>(addr); q_read<3, 3072>(addr);
    q_read<4, 4096>(addr); q_read<5, 5120>(addr); q_read<6, 6144>(addr); q_read<7, 7168>(addr);
  };
  {
    auto q_piece = [&](int kt, int i) {
      const unsigned sb = lds0 + (unsigned)(kt % 3) * 49152;
      if (i < 2) dma1(rW, voffW, (unsigned)((kt * 64 + wid * 8) * 8192 + head * 256 + i * 128), sb + (wid * 2 + i) * 1024);
      else dma1(rX, vlin, (unsigned)((tbx * 40 + 4 * kt + (i - 2)) * 1024), sb + 16384 + wid * 4096 + (i - 2) * 1024);
    };
    auto kv_piece = [&](int i) {
      const unsigned part = wid * 2048 + i * 1024;
      dma1(rK, vlin, (unsigned)(head * 64 * 8192) + part, lds0 + L_KA0 + part);
    };
#pragma unroll
    for (int i = 0; i < 6; ++i) q_piece(0, i);
#pragma unroll
    for (int i = 0; i < 6; ++i) q_piece(1, i);
    f32x16 qa[2][2];
#pragma unroll
    for (int i = 0; i < 2; ++i)
#pragma unroll
      for (int n = 0; n < 2; ++n) qa[i][n] = f32x16{};
    const unsigned wbase = h * 2048 + c * 1024 + q4 * 64 + g1 * 32 + p4 * 8;
    const unsigned xbase = 16384 + rr * 4096 + lane * 16;
    bf16x8 wA[2], xA[2], wB[2], xB[2];
    auto rdq = [&](bf16x8 (&w)[2], bf16x8 (&x)[2], LAS const char* sb, int s) {
      w[0] = lds_tr2(sb + wbase + s * 4096, sb + wbase + s * 4096 + 256);
      x[0] = lds_b128(sb + xbase + s * 1024);
      w[1] = lds_tr2(sb + wbase + s * 4096 + 512, sb + wbase + s * 4096 + 768);
      x[1] = lds_b128(sb + xbase + 16384 + s * 1024);
    };
    auto mmq = [&](const bf16x8 (&w)[2], const bf16x8 (&x)[2]) {
      qa[0][0] = MFMA32(w[0], x[0], qa[0][0]);
      qa[1][0] = MFMA32(w[0], x[1], qa[1][0]);
      qa[0][1] = MFMA32(w[1], x[0], qa[0][1]);
      qa[1][1] = MFMA32(w[1], x[1], qa[1][1]);
    };
    for (int kt = 0; kt < 10; ++kt) {
      if (kt < 9) { WAIT_VL_BAR(6); } else { WAIT_VL_BAR(2); }
      LAS const char* sb = lds + (unsigned)(kt % 3) * 49152;
      auto piece = [&](int i) {
        if (kt < 8) q_piece(kt + 2, i);
        else if (kt == 8 && i < 2) kv_piece(i);
      };
      rdq(wA, xA, sb, 0);
      SBAR();
      if (kt > 0) mmq(wB, xB);
      piece(0); SBAR();
      rdq(wB, xB, sb, 1);
      SBAR();
      mmq(wA, xA);
      piece(1); piece(2); SBAR();
      rdq(wA, xA, sb, 2);
      SBAR();
      mmq(wB, xB);
      piece(3); piece(4); SBAR();
      rdq(wB, xB, sb, 3);
      SBAR();
      mmq(wA, xA);
      piece(5); SBAR();
    }
    mmq(wB, xB);
    STAMP();
    WAIT_L_BAR();
#pragma unroll
    for (int i = 0; i < 2; ++i) {
      LAS char* dst = lds + (i == 0 ? L_STASH : 0u) + rr * 8192 + lane * 16;
#pragma unroll
      for (int n = 0; n < 2; ++n) {
        const f32x16 t = qa[i][n] * QSCALE;
#pragma unroll
        for (int sg = 0; sg < 2; ++sg) *(LAS u32x4*)(dst + (4 * c + 2 * n + sg) * 1024) = pack8(t, 8 * sg);
      }
    }
    WAIT_L_BAR();
    q_load(lds0 + (c ? 0u : L_STASH) + rr * 8192 + lane * 16);
    BAR_A("vmcnt(0) lgkmcnt(0)");
  }
  STAMP();

  negm_set(0.f, 0.f);
  float mref = 0.f, lsum = 0.f, mfin = 0.f, lfin = 0.f;
  const unsigned kown0 = c ? L_KB0 : L_KA0, kown1 = c ? L_KB1 : L_KA1;
  const unsigned vown0 = c ? L_VB0 : L_VA0, vown1 = c ? L_VB1 : L_VA1;
  const unsigned lrd = lds0 + lane * 16;
  float* lamp = LAM + (size_t)b * 256 + rr * 64;
  f32x2 mlA = {0.f, 0.f};

  auto refix = [&](const bool first) __attribute__((always_inline)) {
    const float rm = swap_max(rowmax32());
    const float dl = first ? fmaxf(rm, NEG_BIG) : fmaxf(rm, 0.f);
    const float f = first ? 0.f : __builtin_amdgcn_exp2f(-dl);
    mref = first ? dl : mref + dl;
    lsum = first ? 0.f : lsum * f;
    int ln = lane;
    asm volatile("" : "+v"(ln));
    const float aone = ln < 32 ? 1.f : 0.f;
    scores_add(aone, -dl);
    negm_set(aone, -mref);
    ACC_FENCE();
    if (!first) { o_scale_(f, std::make_integer_sequence<int, 64>{}); ACC_FENCE(); }
  };
#define TRAIL() do { pv_mfma<0, 28 % NRING, 0, 3>(); pv_mfma<0, 29 % NRING, 1, 3>(); pv_mfma<0, 30 % NRING, 2, 3>(); pv_mfma<0, 31 % NRING, 3, 3>(); SBAR(); } while (0)
  auto splain = [&]() __attribute__((always_inline)) {
    __builtin_amdgcn_s_setprio(SPRIO);
    TRAIL();
    float pst = exps32();
    if (__builtin_expect(__any(pst > P_LIMIT), 0)) { refix(false); pst = exps32(); }
    asm volatile("v_add_f32 %0, %0, %1" : "+v"(lsum) : "v"(pst));
    __builtin_amdgcn_s_setprio(0);
  };
  auto sgen = [&](const int tau) __attribute__((always_inline)) {
    __builtin_amdgcn_s_setprio(SPRIO);
    if (tau > 0) TRAIL();
    const bool lastL = c == 1 && tau == p1 - 1;
    const bool diag = tau == NSTEP - 1 || (c == 1 && (tau == p1 - 2 || tau == p1 - 1));
    const bool first = tau == 0 || (c == 1 && tau == p1);
    int ln = lane;
    asm volatile("" : "+v"(ln));
    if (diag) {
      const int hp = tau == NSTEP - 1 ? c : tau - (p1 - 2);
      float ninf = -INFINITY;
      asm volatile("" : "+v"(ninf));
      const int dq = (ln & 31) - 4 * (ln >> 5);
      if (2 * hp > rr) mask_all_<V_S0>(ninf, std::make_integer_sequence<int, 16>{});
      else if (2 * hp == rr) mask_tri_<V_S0>(dq, ninf, std::make_integer_sequence<int, 16>{});
      if (2 * hp + 1 > rr) mask_all_<V_S1>(ninf, std::make_integer_sequence<int, 16>{});
      else if (2 * hp + 1 == rr) mask_tri_<V_S1>(dq, ninf, std::make_integer_sequence<int, 16>{});
    }
    if (first && tau > 0) {
      ACC_FENCE();
      const float inv = 1.0f / lfin;
      u16* dstp = CF + ((size_t)(((4 * qbL + rr) * 32 + head) * 8) * 64 + ln) * 8;
      *(u32x4*)(dstp) = acc_scale8<A_O + 0>(inv);          *(u32x4*)(dstp + 512) = acc_scale8<A_O + 8>(inv);
      *(u32x4*)(dstp + 1024) = acc_scale8<A_O + 16>(inv);  *(u32x4*)(dstp + 1536) = acc_scale8<A_O + 24>(inv);
      *(u32x4*)(dstp + 2048) = acc_scale8<A_O + 32>(inv);  *(u32x4*)(dstp + 2560) = acc_scale8<A_O + 40>(inv);
      *(u32x4*)(dstp + 3072) = acc_scale8<A_O + 48>(inv);  *(u32x4*)(dstp + 3584) = acc_scale8<A_O + 56>(inv);
    }
    if (first) refix(true);
    float pst = exps32();
    if (!first && __any(pst > P_LIMIT)) { refix(false); pst = exps32(); }
    asm volatile("v_add_f32 %0, %0, %1" : "+v"(lsum) : "v"(pst));
    if (lastL || tau == NSTEP - 1) {
      mfin = mref; lfin = swap_add(lsum);
      if (lastL) {
        negm_set(0.f, 0.f);
        q_load(lds0 + L_STASH + rr * 8192 + ln * 16);
      } else {
        f32x2* lp = (f32x2*)lamp + (ln & 31);
        if (c == 0) { if (ln < 32) *lp = (f32x2){mfin, lfin}; }
        else mlA = *lp;
      }
    }
    __builtin_amdgcn_s_setprio(0);
  };
#define KVDMA(i) do { if ((i) == 0) dma1o<0>(rK, vlin, gk, lk); else if ((i) == 1) dma1o<1024>(rK, vlin, gk, lk); \
    else if ((i) == 2) dma1o<2048>(rK, vlin, gk, lk); else if ((i) == 3) dma1o<3072>(rK, vlin, gk, lk); \
    else if ((i) == 4) dma1o<0>(rV, vlin, gv, lv); else if ((i) == 5) dma1o<1024>(rV, vlin, gv, lv); \
    else if ((i) == 6) dma1o<2048>(rV, vlin, gv, lv); else dma1o<3072>(rV, vlin, gv, lv); } while (0)
#define MSTEP(J, FIRST, LAST, DMA, ZO) do { \
    constexpr int out_ = ((J) + PD - 1 < (LAST) ? (J) + PD - 1 : (LAST)) - (J);     \
    if constexpr ((J) < 2) qk_head<out_, (J) % NRING, 0, ((J) & 1) ? V_S1 : V_S0>(); \
    else if constexpr ((J) < 16) qk_mfma<out_, (J) % NRING, ((J) >> 1), ((J) & 1) ? V_S1 : V_S0>(); \
    else if constexpr ((J) < 20 && (ZO)) { if (zero_o) pv_zero<out_, (J) % NRING, (J) & 3, 0>(); else pv_mfma<out_, (J) % NRING, (J) & 3, 0>(); } \
    else pv_mfma<out_, (J) % NRING, (J) & 3, (((J) - 16) >> 2)>(); \
    if constexpr ((J) + PD <= (LAST)) frag_read<(J) + PD>(ka, va); \
    if constexpr ((DMA) && ((J) & 1) && (J) < 16) KVDMA((J) >> 1); \
    SBAR(); } while (0)
#define MSTEP4(J, FIRST, LAST, DMA, ZO) MSTEP(J, FIRST, LAST, DMA, ZO); MSTEP((J) + 1, FIRST, LAST, DMA, ZO); MSTEP((J) + 2, FIRST, LAST, DMA, ZO); MSTEP((J) + 3, FIRST, LAST, DMA, ZO)
#define KSLOT(t) (((t) < p1) ? (((t) & 1) ? L_KA1 : L_KA0) : (((t) & 1) ? kown1 : kown0))
#define VSLOT(t) (((t) < p1) ? (((t) & 1) ? L_VA1 : L_VA0) : (((t) & 1) ? vown1 : vown0))
#define STAGE_ADDR(t) \
      const unsigned gk = ((t) + 1 < NSTEP && (c == 0 || (t) + 1 >= p1)) ? kvoff((t) + 1) + rr * 4096 : 0x7ff00000u; \
      const unsigned gv = (c == 0 || (t) >= p1) ? kvoff(t) + rr * 4096 : 0x7ff00000u; \
      const unsigned lk = lds0 + ((((t) + 1) & 1) ? kown1 : kown0) + rr * 4096; \
      const unsigned lv = lds0 + (((t) & 1) ? vown1 : vown0) + rr * 4096;

  if (c == 1) asm volatile("s_barrier" ::: "memory");
  {
    const unsigned ka = lrd + L_KA0, va = 0;
    STAGE_ADDR(0)
    constexpr bool zero_o = false;
    frag_read<0>(ka, va); frag_read<1>(ka, va); frag_read<2>(ka, va); frag_read<3>(ka, va);
    SBAR();
    MSTEP4(0, 0, 15, true, false); MSTEP4(4, 0, 15, true, false); MSTEP4(8, 0, 15, true, false); MSTEP4(12, 0, 15, true, false);
    BAR_A("lgkmcnt(0)");
    ACC_FENCE();
    sgen(0);
    BAR_A("vmcnt(0) lgkmcnt(0)");
  }
#define MPHASE(tau, DMA, ZO) do { \
      const bool zero_o = (tau) == 1 || (c == 1 && (tau) == p1 + 1);     \
      (void)zero_o; \
      const unsigned ka = lrd + KSLOT(tau); \
      const unsigned va = lrd + VSLOT((tau) - 1); \
      STAGE_ADDR(tau) \
      (void)gk; (void)gv; (void)lk; (void)lv; \
      frag_read<0>(ka, va); frag_read<1>(ka, va); frag_read<2>(ka, va); frag_read<3>(ka, va); \
      SBAR(); \
      MSTEP4(0, 0, 31, DMA, ZO); MSTEP4(4, 0, 31, DMA, ZO); MSTEP4(8, 0, 31, DMA, ZO); MSTEP4(12, 0, 31, DMA, ZO); \
      MSTEP4(16, 0, 31, DMA, ZO); MSTEP4(20, 0, 31, DMA, ZO); MSTEP4(24, 0, 31, DMA, ZO); \
      BAR_A("lgkmcnt(0)"); } while (0)
  int tau = 1;
  while (tau < NSTEP) {
    const bool special = tau == 1 || tau == NSTEP - 1 || (c == 1 && tau >= p1 - 2 && tau <= p1 + 1);
    if (special) {
      STAMP();
      MPHASE(tau, true, true);
      STAMP();
      sgen(tau);
      BAR_A("vmcnt(0) lgkmcnt(0)");
      ++tau;
    } else if (c == 1 && tau < p1 - 2) {
      do {
        STAMP();
        MPHASE(tau, false, false);
        STAMP();
        splain();
        BAR_A("lgkmcnt(0)");
        ++tau;
      } while (tau < p1 - 2);
    } else {
      do {
        STAMP();
        MPHASE(tau, true, false);
        STAMP();
        splain();
        BAR_A("vmcnt(0) lgkmcnt(0)");
        ++tau;
      } while (tau < NSTEP - 1);
    }
  }
#undef MPHASE
#undef TRAIL
  {
    const unsigned v128 = lane * 128;
    const unsigned dead = lds0 + vown1 + rr * 256;
    const int u = idx * 8 + wid;
    if (u < 160) warm8k(mk_rsrc(wo, NH * HD * DM * 2), v128, (unsigned)((xcd >> 1) * 1024 * 1280 + u * 8192), dead);
  }
  {
    const unsigned ka = 0, va = lrd + VSLOT(NSTEP - 1);
    const unsigned gk = 0, gv = 0, lk = 0, lv = 0;
    (void)gk; (void)gv; (void)lk; (void)lv; (void)ka;
    const bool zero_o = c == 1 && p1 == NSTEP - 1;
    frag_read<16>(ka, va); frag_read<17>(ka, va); frag_read<18>(ka, va); frag_read<19>(ka, va);
    SBAR();
    MSTEP4(16, 16, 31, false, true); MSTEP4(20, 16, 31, false, true); MSTEP4(24, 16, 31, false, true); MSTEP4(28, 16, 31, false, true);
    BAR_A("lgkmcnt(0)");
    int ln = lane;
    asm volatile("" : "+v"(ln));
    const unsigned mine = lds0 + (c ? L_KB1 : L_KA0) + ((rr & 2) ? (c ? L_KB0 - L_KB1 : L_KA1 - L_KA0) : 0u) + (rr & 1) * 8192 + ln * 16;
    const unsigned theirs = (c ? L_KA0 : L_KB1) + ((rr & 2) ? (c ? L_KA1 - L_KA0 : L_KB0 - L_KB1) : 0u) + (rr & 1) * 8192 + ln * 16;
    ACC_FENCE();
    if (c == 0) {
      acc_to_lds<A_O + 32, 0>(mine); acc_to_lds<A_O + 36, 1024>(mine); acc_to_lds<A_O + 40, 2048>(mine); acc_to_lds<A_O + 44, 3072>(mine);
      acc_to_lds<A_O + 48, 4096>(mine); acc_to_lds<A_O + 52, 5120>(mine); acc_to_lds<A_O + 56, 6144>(mine); acc_to_lds<A_O + 60, 7168>(mine);
      BAR_A("lgkmcnt(0)");
    } else {
      acc_to_lds<A_O + 0, 0>(mine); acc_to_lds<A_O + 4, 1024>(mine); acc_to_lds<A_O + 8, 2048>(mine); acc_to_lds<A_O + 12, 3072>(mine);
      acc_to_lds<A_O + 16, 4096>(mine); acc_to_lds<A_O + 20, 5120>(mine); acc_to_lds<A_O + 24, 6144>(mine); acc_to_lds<A_O + 28, 7168>(mine);
      if (ln < 32) *(LAS f32x2*)(lds + L_STASH + rr * 256 + ln * 8) = (f32x2){mfin, lfin};
    }
    BAR_A("lgkmcnt(0)");
    {
      const f32x2 mo = c ? mlA : *(LAS const f32x2*)(lds + L_STASH + rr * 256 + (ln & 31) * 8);
      const float m = fmaxf(mo[0], mfin);
      const float eo = __builtin_amdgcn_exp2f(mo[0] - m), em = __builtin_amdgcn_exp2f(mfin - m);
      const float inv = 1.0f / (mo[1] * eo + lfin * em);
      const float co = eo * inv, cm = em * inv;
      f32x4 w[8];
#pragma unroll
      for (int i = 0; i < 8; ++i) w[i] = *(LAS const f32x4*)(lds + theirs + i * 1024);
      u16* dstp = CF + ((size_t)(((4 * qbH + rr) * 32 + head) * 8 + 4 * c) * 64 + ln) * 8;
      if (c == 0) {
        *(u32x4*)(dstp) = acc_combine8f<A_O + 0>(w[0], w[1], co, cm);         *(u32x4*)(dstp + 512) = acc_combine8f<A_O + 8>(w[2], w[3], co, cm);
        *(u32x4*)(dstp + 1024) = acc_combine8f<A_O + 16>(w[4], w[5], co, cm); *(u32x4*)(dstp + 1536) = acc_combine8f<A_O + 24>(w[6], w[7], co, cm);
      } else {
        *(u32x4*)(dstp) = acc_combine8f<A_O + 32>(w[0], w[1], co, cm);        *(u32x4*)(dstp + 512) = acc_combine8f<A_O + 40>(w[2], w[3], co, cm);
        *(u32x4*)(dstp + 1024) = acc_combine8f<A_O + 48>(w[4], w[5], co, cm); *(u32x4*)(dstp + 1536) = acc_combine8f<A_O + 56>(w[6], w[7], co, cm);
      }
    }
  }
#undef KSLOT
#undef VSLOT
#undef STAGE_ADDR
#undef KVDMA
#undef MSTEP
#undef MSTEP4
  STAMP();
  STAMP_END(1, DBGP(CF, WS_CF));
}

constexpr int K3_STAGE = 37888, K3_NST = 4, K3_LDS = K3_STAGE * K3_NST;

__global__ __launch_bounds__(512, 2) void k3_oproj(const u16* __restrict__ CF, const u16* __restrict__ wo,
                                                   u16* __restrict__ PT) {
  extern __shared__ __attribute__((aligned(16))) char smem[];
  LAS char* lds = (LAS char*)smem;
  const unsigned lds0 = (unsigned)(uintptr_t)lds;
  const int tid = threadIdx.x, lane = tid & 63;
  const int wid = RFL(tid >> 6);
  const int b = blockIdx.x, xcd = b & 7, idx = b >> 3;
  const int ks = xcd >> 1, panel = (xcd & 1) * 8 + (idx >> 2), nt = idx & 3;
  const int tbi = wid & 3, kp = wid >> 2;
  const int h = lane >> 5, g1 = (lane >> 4) & 1, q4 = (lane & 15) >> 2, p4 = lane & 3;
  const __amdgpu_buffer_rsrc_t rC = mk_rsrc(CF, NH * SEQ * HD * 2);
  const __amdgpu_buffer_rsrc_t rW = mk_rsrc(wo, NH * HD * DM * 2);
  const unsigned vlin = lane * 16;
  unsigned voffW[3];
#pragma unroll
  for (int i = 0; i < 3; ++i) {
    const int pc = wid + 8 * i;
    const int u = 2 * pc + (lane >> 5);
    const int kb = u / 5, nb = u % 5;
    voffW[i] = (unsigned)((kb * 8 + ((lane >> 2) & 7)) * 1280 + (nb * 32 + (lane & 3) * 8) * 2);
  }
  const bool lo = wid < 4;
  const unsigned sW0 = (unsigned)(ks * 1024 * 1280 + nt * 320);
  const unsigned sC0 = (unsigned)(((panel * 4 + tbi) * 256 + ks * 64) * 1024);
  const __amdgpu_buffer_rsrc_t r2 = lo ? rW : rC;
  const unsigned v2 = lo ? voffW[2] : vlin;
  const unsigned s2 = lo ? sW0 : sC0, st2 = lo ? 81920u : 4096u, l2 = lo ? (wid + 16) * 1024 : 20480 + tbi * 4096;
  const unsigned s3 = sC0 + (lo ? 3072u : 1024u), l3 = 20480 + tbi * 4096 + (lo ? 3072 : 1024);
  const unsigned s4 = lo ? 0x7ff00000u : sC0 + 2048u, l4 = lo ? 36864 : 20480 + tbi * 4096 + 2048;
  auto piece = [&](int j, int i) {
    const unsigned sb = lds0 + (j & 3) * K3_STAGE;
    if (i == 0) dma1(rW, voffW[0], sW0 + j * 81920, sb + wid * 1024);
    else if (i == 1) dma1(rW, voffW[1], sW0 + j * 81920, sb + (wid + 8) * 1024);
    else if (i == 2) dma1(r2, v2, s2 + j * st2, sb + l2);
    else if (i == 3) dma1(rC, vlin, s3 + j * 4096, sb + l3);
    else dma1(rC, vlin, s4 + j * 4096, sb + l4);
  };
  f32x16 acc[5];
#pragma unroll
  for (int n = 0; n < 5; ++n) acc[n] = f32x16{};
  const unsigned wbase = h * 256 + q4 * 64 + g1 * 32 + p4 * 8 + kp * 4 * 2560;
  const unsigned cbase = 20480 + tbi * 4096 + lane * 16 + kp * 2048;
  auto rd = [&](bf16x8& cf, bf16x8 (&w)[5], LAS const char* sb, int si) {
    cf = lds_b128(sb + cbase + si * 1024);
#pragma unroll
    for (int n = 0; n < 5; ++n)
      w[n] = lds_tr2(sb + wbase + (2 * si) * 2560 + n * 512, sb + wbase + (2 * si + 1) * 2560 + n * 512);
  };
  bf16x8 cA, cB, wA[5], wB[5];
  STAMP_DECL();
  STAMP();
#pragma unroll
  for (int j = 0; j < 3; ++j)
#pragma unroll
    for (int i = 0; i < 5; ++i) piece(j, i);
  STAMP();
#pragma unroll
  for (int j = 0; j < 16; ++j) {
    if (j + 2 < 16) { WAIT_VL_BAR(10); } else if (j + 1 < 16) { WAIT_VL_BAR(5); } else { WAIT_VL_BAR(0); }
    STAMP();
    LAS const char* sb = lds + (j & 3) * K3_STAGE;
    const bool more = j + 3 < 16;
    rd(cA, wA, sb, 0);
    SBAR();
    if (j > 0) {
      acc[0] = MFMA32(wB[0], cB, acc[0]); acc[1] = MFMA32(wB[1], cB, acc[1]);
      if (more) piece(j + 3, 0);
      SBAR();
      acc[2] = MFMA32(wB[2], cB, acc[2]); acc[3] = MFMA32(wB[3], cB, acc[3]);
      if (more) piece(j + 3, 1);
      SBAR();
      acc[4] = MFMA32(wB[4], cB, acc[4]);
    } else {
      piece(3, 0); piece(3, 1);
    }
    SBAR();
    rd(cB, wB, sb, 1);
    SBAR();
    acc[0] = MFMA32(wA[0], cA, acc[0]);
    if (more) piece(j + 3, 2);
    SBAR();
    acc[1] = MFMA32(wA[1], cA, acc[1]); acc[2] = MFMA32(wA[2], cA, acc[2]);
    if (more) piece(j + 3, 3);
    SBAR();
    acc[3] = MFMA32(wA[3], cA, acc[3]); acc[4] = MFMA32(wA[4], cA, acc[4]);
    if (more) piece(j + 3, 4);
    SBAR();
  }
#pragma unroll
  for (int n = 0; n < 5; ++n) acc[n] = MFMA32(wB[n], cB, acc[n]);
  STAMP();
  WAIT_L_BAR();
  {
    LAS char* mine = lds + (tbi * 2 + kp) * 12288 + lane * 16;
    LAS const char* theirs = lds + (tbi * 2 + (kp ^ 1)) * 12288 + lane * 16;
    auto put = [&](const f32x16& a, int slot) {
#pragma unroll
      for (int gq = 0; gq < 4; ++gq)
        *(LAS f32x4*)(mine + (slot * 4 + gq) * 1024) = (f32x4){a[4 * gq], a[4 * gq + 1], a[4 * gq + 2], a[4 * gq + 3]};
    };
    auto fin = [&](const f32x16& a, int slot, int n) {
      f32x16 t;
#pragma unroll
      for (int gq = 0; gq < 4; ++gq) {
        const f32x4 v = *(LAS const f32x4*)(theirs + (slot * 4 + gq) * 1024);
#pragma unroll
        for (int e = 0; e < 4; ++e) t[4 * gq + e] = a[4 * gq + e] + v[e];
      }
      u16* dst = PT + ((size_t)((((ks * 64 + panel * 4 + tbi) * 20 + nt * 5 + n) * 2) * 64 + lane)) * 8;
      *(u32x4*)dst = pack8(t, 0);
      *(u32x4*)(dst + 512) = pack8(t, 8);
    };
    if (kp == 0) { put(acc[3], 0); put(acc[4], 1); } else { put(acc[0], 0); put(acc[1], 1); put(acc[2], 2); }
    WAIT_L_BAR();
    if (kp == 0) { fin(acc[0], 0, 0); fin(acc[1], 1, 1); fin(acc[2], 2, 2); } else { fin(acc[3], 0, 3); fin(acc[4], 1, 4); }
  }
  STAMP();
  STAMP();
  STAMP_END(2, DBGP(PT, WS_PT));
}

__global__ __launch_bounds__(320) void k4_ln(const u16* __restrict__ x, const u16* __restrict__ PT,
                                             float* __restrict__ H, u16* __restrict__ HN, const u16* __restrict__ w1) {
  __shared__ __attribute__((aligned(16))) u16 tile[8 * 648];
  __shared__ float red[2][5][8];
  __shared__ __attribute__((aligned(16))) char dead[5 * 256];
  const int tid = threadIdx.x, lane = tid & 63, wid = RFL(tid >> 6);
  const int b = blockIdx.x;
  const int r8 = lane & 7, hh = (lane >> 3) & 1, sg = (lane >> 4) & 1, ml = lane >> 5;
  const int token = b * 8 + r8, tb32 = token >> 5, rr = token & 31;
  u32x2 xv[2][2];
  u32x4 pv[2][4];
#pragma unroll
  for (int i = 0; i < 2; ++i) {
    const int mb = wid * 4 + 2 * i + ml;
    const int m0 = mb * 32 + sg * 16 + hh * 4;
    xv[i][0] = *(const u32x2*)(x + (size_t)token * DM + m0);
    xv[i][1] = *(const u32x2*)(x + (size_t)token * DM + m0 + 8);
#pragma unroll
    for (int ks = 0; ks < 4; ++ks)
      pv[i][ks] = *(const u32x4*)(PT + ((size_t)((((ks * 64 + tb32) * 20 + mb) * 2 + sg) * 64 + hh * 32 + rr)) * 8);
  }
  SBAR();
  float v[2][8];
  float s = 0.f;
#pragma unroll
  for (int i = 0; i < 2; ++i) {
    const int m0 = (wid * 4 + 2 * i + ml) * 32 + sg * 16 + hh * 4;
#pragma unroll
    for (int q = 0; q < 4; ++q) {
      const unsigned xw = xv[i][q >> 1][q & 1];
      v[i][2 * q] = bflo(xw) + ((bflo(pv[i][0][q]) + bflo(pv[i][1][q])) + (bflo(pv[i][2][q]) + bflo(pv[i][3][q])));
      v[i][2 * q + 1] = bfhi(xw) + ((bfhi(pv[i][0][q]) + bfhi(pv[i][1][q])) + (bfhi(pv[i][2][q]) + bfhi(pv[i][3][q])));
    }
    *(f32x4*)(H + (size_t)token * DM + m0) = (f32x4){v[i][0], v[i][1], v[i][2], v[i][3]};
    *(f32x4*)(H + (size_t)token * DM + m0 + 8) = (f32x4){v[i][4], v[i][5], v[i][6], v[i][7]};
#pragma unroll
    for (int e = 0; e < 8; ++e) s += v[i][e];
  }
  {
    const int u = (b >> 3) * 5 + wid;
    const int a4 = (b & 7) >> 1;
    const unsigned dd = (unsigned)(uintptr_t)(LAS char*)dead + wid * 256;
    const __amdgpu_buffer_rsrc_t r1 = mk_rsrc(w1, SEQ * FF * 2);
#pragma unroll
    for (int i = 0; i < 2; ++i) {
      const int line = (u + 160 * i) * 64 + lane;
      const unsigned off = line < 14336 ? (unsigned)((line / 7) * (FF * 2) + a4 * 896 + (line % 7) * 128) : 0x7ff00000u;
      warm8k(r1, off, 0u, dd);
    }
  }
  s += __shfl_xor(s, 8); s += __shfl_xor(s, 16); s += __shfl_xor(s, 32);
  if (lane < 8) red[0][wid][lane] = s;
  WAIT_L_BAR();
  const float mean = ((red[0][0][r8] + red[0][1][r8]) + (red[0][2][r8] + red[0][3][r8]) + red[0][4][r8]) * (1.0f / DM);
  float sq = 0.f;
#pragma unroll
  for (int i = 0; i < 2; ++i)
#pragma unroll
    for (int e = 0; e < 8; ++e) { v[i][e] -= mean; sq += v[i][e] * v[i][e]; }
  sq += __shfl_xor(sq, 8); sq += __shfl_xor(sq, 16); sq += __shfl_xor(sq, 32);
  if (lane < 8) red[1][wid][lane] = sq;
  WAIT_L_BAR();
  const float var = ((red[1][0][r8] + red[1][1][r8]) + (red[1][2][r8] + red[1][3][r8]) + red[1][4][r8]) * (1.0f / DM);
  const float r1 = 1.0f / sqrtf(var + LN_EPS);
  const float r2 = 1.0f / sqrtf(var * r1 * r1 + LN_EPS);
  const float sc = r1 * r2;
#pragma unroll
  for (int i = 0; i < 2; ++i) {
    const int m0 = (wid * 4 + 2 * i + ml) * 32 + sg * 16 + hh * 4;
    *(u32x2*)(tile + r8 * 648 + m0) = (u32x2){cvtpk(v[i][0] * sc, v[i][1] * sc), cvtpk(v[i][2] * sc, v[i][3] * sc)};
    *(u32x2*)(tile + r8 * 648 + m0 + 8) = (u32x2){cvtpk(v[i][4] * sc, v[i][5] * sc), cvtpk(v[i][6] * sc, v[i][7] * sc)};
  }
  WAIT_L_BAR();
  const int ks32 = b >> 2, g = b & 3;
#pragma unroll
  for (int i = 0; i < 2; ++i) {
    const int m = tid + 320 * i;
    u16 e[8];
#pragma unroll
    for (int j = 0; j < 8; ++j) e[j] = tile[j * 648 + m];
    u32x4 w = {(unsigned)e[0] | ((unsigned)e[1] << 16), (unsigned)e[2] | ((unsigned)e[3] << 16),
               (unsigned)e[4] | ((unsigned)e[5] << 16), (unsigned)e[6] | ((unsigned)e[7] << 16)};
    *(u32x4*)(HN + ((size_t)(((m >> 4) * 64 + ks32) * 64 + g * 16 + (m & 15))) * 8) = w;
  }
}

constexpr int K56_LDS = 163840;

DI float gelu_tanh(float x) {
  const float u = 0.7978845608028654f * (x + 0.044715f * x * x * x);
  const float e = __builtin_amdgcn_exp2f(u * 2.8853900817779268f);
  const float t = 1.0f - 2.0f * __builtin_amdgcn_rcpf(e + 1.0f);
  return 0.5f * x * (1.0f + t);
}

#define ACLOB56 "a0","a1","a2","a3","a4","a5","a6","a7","a8","a9","a10","a11","a12","a13","a14","a15","a16","a17","a18","a19","a20","a21","a22","a23","a24","a25","a26","a27","a28","a29","a30","a31","a32","a33","a34","a35","a36","a37","a38","a39","a40","a41","a42","a43","a44","a45","a46","a47","a48","a49","a50","a51","a52","a53","a54","a55","a56","a57","a58","a59","a60","a61","a62","a63","a64","a65","a66","a67","a68","a69","a70","a71","a72","a73","a74","a75","a76","a77","a78","a79","a80","a81","a82","a83","a84","a85","a86","a87","a88","a89","a90","a91"
template <int R> DI void m_aload(__amdgpu_buffer_rsrc_t rsrc, unsigned voff, unsigned soff) {
  asm volatile("buffer_load_dwordx4 a[%c3:%c4], %0, %1, %2 offen" ::"v"(voff), "s"(rsrc), "s"(soff), "i"(R), "i"(R + 3) : "memory");
}
template <int R, int OFF> DI void m_trread(unsigned addr) {
  asm volatile("ds_read_b64_tr_b16 a[%c1:%c2], %0 offset:%c3" ::"v"(addr), "i"(R), "i"(R + 1), "i"(OFF) : "memory");
}
template <int RA, int RB> DI void m_mfma(f32x4& d) {
  asm volatile("v_mfma_f32_16x16x32_bf16 %0, a[%c1:%c2], a[%c3:%c4], %0" : "+v"(d) : "i"(RA), "i"(RA + 3), "i"(RB), "i"(RB + 3));
}

template <int MODE>
__global__ __launch_bounds__(512, 2) void k56_mix(const u16* __restrict__ W, const u16* __restrict__ AF,
                                                  const u16* __restrict__ bias, const float* __restrict__ H,
                                                  u16* __restrict__ outp, const u16* __restrict__ wnext) {
  constexpr int LDW = MODE == 0 ? FF : SEQ;
  constexpr int NKS = MODE == 0 ? 64 : 54;
  constexpr int NK = MODE == 0 ? 8 : 7;
  constexpr int KROWS = MODE == 0 ? SEQ : FF;
  extern __shared__ __attribute__((aligned(16))) char smem[];
  LAS char* lds = (LAS char*)smem;
  const unsigned lds0 = (unsigned)(uintptr_t)lds;
  const int tid = threadIdx.x, lane = tid & 63;
  const int wid = RFL(tid >> 6);
  const int b = blockIdx.x, xcd = b & 7, idx = b >> 3;
  int wt, dt;
  if (MODE == 0) {
    wt = (xcd >> 1) * 7 + (idx >> 2); dt = (xcd & 1) * 4 + (idx & 3);
    if (idx >= 28 || wt >= 27) {
      if (idx >= 28) {
        const __amdgpu_buffer_rsrc_t r2 = mk_rsrc(wnext, SEQ * FF * 2);
#pragma unroll
        for (int i = 0; i < 7; ++i) {
          const int line = (((idx - 28) * 8 + wid) * 7 + i) * 64 + lane;
          const unsigned off = line < 13824 ? (unsigned)((line >> 3) * 4096 + (xcd >> 1) * 1024 + (line & 7) * 128) : 0x7ff00000u;
          warm8k(r2, off, 0u, lds0 + wid * 256);
        }
      }
      return;
    }
  } else {
    wt = (xcd >> 1) * 8 + (idx >> 2); dt = (xcd & 1) * 4 + (idx & 3);
  }
  const __amdgpu_buffer_rsrc_t rW = mk_rsrc(W, KROWS * LDW * 2);
  const __amdgpu_buffer_rsrc_t rA = mk_rsrc(AF, 40 * NKS * 1024);
  const unsigned vlin = lane * 16;
  const int prow = (lane >> 2) & 7;
  const unsigned voffW0 = (unsigned)(prow * LDW * 2 + (lane >> 5) * 64 +
                                     (((lane & 3) ^ (MODE == 1 ? (((prow >> 2) & 1) << 1) : 0)) << 4));
  const unsigned voffW1 = MODE == 1 ? voffW0 : (voffW0 ^ 32u);
  const unsigned myl = lds0 + wid * 20480;
  const int g = lane >> 4, q4 = (lane & 15) >> 2, p4 = lane & 3;
  unsigned wb[2][2];
#pragma unroll
  for (int t = 0; t < 2; ++t)
#pragma unroll
    for (int hf = 0; hf < 2; ++hf) {
      const int kb = MODE == 0 ? g : 2 * t + (g >> 1);
      const int row = MODE == 0 ? 4 * t + q4 : 4 * (g & 1) + q4;
      const int sw = MODE == 0 ? (kb & 1) : ((row >> 2) & 1);
      wb[t][hf] = myl + kb * 1024 + row * 64 + ((hf ^ sw) * 32) + p4 * 8;
    }
  f32x4 acc[4][5];
#pragma unroll
  for (int i = 0; i < 4; ++i)
#pragma unroll
    for (int d = 0; d < 5; ++d) acc[i][d] = (f32x4){0.f, 0.f, 0.f, 0.f};
  STAMP_DECL();
  STAMP();
  u32x2 eb0[2], eb1[2];
  f32x4 eh[3]; u16 ebs[3];
  if (MODE == 0) {
#pragma unroll
    for (int i = 0; i < 2; ++i) {
      const int u = wid + 8 * i < 10 ? wid + 8 * i : wid;
      const int f0 = wt * 64 + (u / 5) * 32 + 4 * g;
      eb0[i] = *(const u32x2*)(bias + f0); eb1[i] = *(const u32x2*)(bias + f0 + 16);
    }
  } else {
#pragma unroll
    for (int i = 0; i < 3; ++i) {
      const int u = wid + 8 * i < 20 ? wid + 8 * i : wid;
      const int s = wt * 64 + (u / 5) * 16 + (lane & 15), d0 = dt * 80 + (u % 5) * 16 + 4 * g;
      ebs[i] = bias[s];
      eh[i] = *(const f32x4*)(H + (size_t)s * DM + d0);
    }
  }
  SBAR();
#define WDMA(IT, KB) dma1(rW, ((KB) & 1) ? voffW1 : voffW0, (unsigned)(((wid + 8 * (IT)) * 32 + (KB) * 8) * LDW * 2 + wt * 128), myl + ((IT) % 5) * 4096 + (KB) * 1024)
#define ALOAD(IT, D) m_aload<((IT) % 3) * 20 + 4 * (D)>(rA, vlin, (unsigned)(((dt * 5 + (D)) * NKS + wid + 8 * (IT)) * 1024))
#define WDMA4(IT) do { WDMA(IT, 0); WDMA(IT, 1); WDMA(IT, 2); WDMA(IT, 3); } while (0)
#define ALOAD5(IT) do { ALOAD(IT, 0); ALOAD(IT, 1); ALOAD(IT, 2); ALOAD(IT, 3); ALOAD(IT, 4); } while (0)
#define WREAD1(IT, I) do { m_trread<60 + 16 * ((IT) & 1) + 4 * (I), ((IT) % 5) * 4096 + ((I) >> 1) * 512>(wb[0][(I) & 1]); \
                           m_trread<60 + 16 * ((IT) & 1) + 4 * (I) + 2, ((IT) % 5) * 4096 + ((I) >> 1) * 512>(wb[1][(I) & 1]); } while (0)
#define WREAD(IT) do { WREAD1(IT, 0); WREAD1(IT, 1); WREAD1(IT, 2); WREAD1(IT, 3); } while (0)
#define MM(IT, I, D) do { if constexpr (MODE == 0) m_mfma<60 + 16 * ((IT) & 1) + 4 * (I), ((IT) % 3) * 20 + 4 * (D)>(acc[I][D]); \
                          else m_mfma<((IT) % 3) * 20 + 4 * (D), 60 + 16 * ((IT) & 1) + 4 * (I)>(acc[I][D]); } while (0)
#define MMROW(IT, I) do { MM(IT, I, 0); MM(IT, I, 1); MM(IT, I, 2); MM(IT, I, 3); MM(IT, I, 4); } while (0)
#define NV(IT) (((IT) + 4 < NK ? 4 : 0) + ((IT) + 2 < NK ? 5 : 0))
#define ALLOW(IT) ((IT) == 0 ? (5 + (NK > 2 ? 4 : 0) + (NK > 3 ? 4 : 0)) : (IT) == 1 ? ((NK > 3 ? 4 : 0) + NV(0)) : NV((IT) - 1))
#define STEP(IT) do { if constexpr ((IT) < NK) { \
    asm volatile("s_waitcnt vmcnt(%c0) lgkmcnt(0)" ::"i"(ALLOW(IT)) : "memory", ACLOB56); \
    if constexpr ((IT) + 1 < NK) WREAD((IT) + 1); \
    SBAR(); \
    MMROW(IT, 0); if constexpr ((IT) + 4 < NK) { WDMA((IT) + 4, 0); WDMA((IT) + 4, 1); } SBAR(); \
    MMROW(IT, 1); if constexpr ((IT) + 4 < NK) { WDMA((IT) + 4, 2); WDMA((IT) + 4, 3); } SBAR(); \
    MMROW(IT, 2); if constexpr ((IT) + 2 < NK) { ALOAD((IT) + 2, 0); ALOAD((IT) + 2, 1); ALOAD((IT) + 2, 2); } SBAR(); \
    MMROW(IT, 3); if constexpr ((IT) + 2 < NK) { ALOAD((IT) + 2, 3); ALOAD((IT) + 2, 4); } SBAR(); \
    } } while (0)
  asm volatile("" ::: "memory", ACLOB56);
  WDMA4(0); ALOAD5(0); asm volatile("s_barrier" ::: "memory");
  WDMA4(1); ALOAD5(1); asm volatile("s_barrier" ::: "memory");
  WDMA4(2); WDMA4(3);
  asm volatile("s_waitcnt vmcnt(22)" ::: "memory");
  WREAD(0);
  STAMP();
  STEP(0); STEP(1); STEP(2); STEP(3); STEP(4); STEP(5); STEP(6); STEP(7);
  asm volatile("s_nop 15" ::: "memory");
  STAMP();
#undef WDMA
#undef ALOAD
#undef WDMA4
#undef ALOAD5
#undef WREAD1
#undef WREAD
#undef MM
#undef MMROW
#undef NV
#undef ALLOW
#undef STEP
  WAIT_L0();
  STAMP();
  {
    LAS char* dst = lds + wid * 20480 + lane * 16;
#pragma unroll
    for (int i = 0; i < 4; ++i)
#pragma unroll
      for (int d = 0; d < 5; ++d) *(LAS f32x4*)(dst + (i * 5 + d) * 1024) = acc[i][d];
  }
  WAIT_L_BAR();
  if (MODE == 0) {
#pragma unroll
    for (int i = 0; i < 2; ++i) {
      const int u = wid + 8 * i;
      if (u < 10) {
        const int a = u / 5, d = u % 5;
        f32x4 t0 = {0.f, 0.f, 0.f, 0.f}, t1 = {0.f, 0.f, 0.f, 0.f};
#pragma unroll
        for (int w = 0; w < 8; ++w) {
          t0 += *(LAS const f32x4*)(lds + w * 20480 + ((2 * a) * 5 + d) * 1024 + lane * 16);
          t1 += *(LAS const f32x4*)(lds + w * 20480 + ((2 * a + 1) * 5 + d) * 1024 + lane * 16);
        }
        const u32x2 b0 = eb0[i], b1 = eb1[i];
        t0 += (f32x4){bflo(b0[0]), bfhi(b0[0]), bflo(b0[1]), bfhi(b0[1])};
        t1 += (f32x4){bflo(b1[0]), bfhi(b1[0]), bflo(b1[1]), bfhi(b1[1])};
        u32x4 w = {cvtpk(gelu_tanh(t0[0]), gelu_tanh(t0[1])), cvtpk(gelu_tanh(t0[2]), gelu_tanh(t0[3])),
                   cvtpk(gelu_tanh(t1[0]), gelu_tanh(t1[1])), cvtpk(gelu_tanh(t1[2]), gelu_tanh(t1[3]))};
        *(u32x4*)(outp + ((size_t)(((dt * 5 + d) * 54 + wt * 2 + a) * 64 + lane)) * 8) = w;
      }
    }
  } else {
#pragma unroll
    for (int i = 0; i < 3; ++i) {
      const int u = wid + 8 * i;
      if (u < 20) {
        f32x4 t0 = {0.f, 0.f, 0.f, 0.f};
#pragma unroll
        for (int w = 0; w < 8; ++w) t0 += *(LAS const f32x4*)(lds + w * 20480 + u * 1024 + lane * 16);
        const int s = wt * 64 + (u / 5) * 16 + (lane & 15);
        const int d0 = dt * 80 + (u % 5) * 16 + 4 * g;
        const float bb = bf2f(ebs[i]);
        const f32x4 hv = eh[i];
        u32x2 w = {cvtpk(t0[0] + bb + hv[0], t0[1] + bb + hv[1]), cvtpk(t0[2] + bb + hv[2], t0[3] + bb + hv[3])};
        *(u32x2*)(outp + (size_t)s * DM + d0) = w;
      }
    }
  }
  STAMP();
  STAMP_END(4 + MODE, DBGP(AF, MODE == 0 ? WS_HN : WS_UF));
}

}

extern "C" void kernel_launch(void* const* d_in, const int* in_sizes, int n_in, void* d_out, int out_size, void* d_ws,
                              size_t ws_size, hipStream_t stream) {
  (void)in_sizes; (void)n_in; (void)out_size; (void)ws_size;
  const u16* x = (const u16*)d_in[0];
  const u16* ckv = (const u16*)d_in[1];
  const u16* wq = (const u16*)d_in[2];
  const u16* wuk = (const u16*)d_in[3];
  const u16* wuv = (const u16*)d_in[4];
  const u16* wo = (const u16*)d_in[5];
  const u16* w1 = (const u16*)d_in[7];
  const u16* b1 = (const u16*)d_in[8];
  const u16* w2 = (const u16*)d_in[9];
  const u16* b2 = (const u16*)d_in[10];
  char* ws = (char*)d_ws;
  u16* XN = (u16*)(ws + WS_XN);
  u16* KF = (u16*)(ws + WS_KF);
  u16* VF = (u16*)(ws + WS_VF);
  u16* CF = (u16*)(ws + WS_CF);
  u16* PT = (u16*)(ws + WS_PT);
  float* H = (float*)(ws + WS_H);
  u16* HN = (u16*)(ws + WS_HN);
  u16* UF = (u16*)(ws + WS_UF);
  (void)hipFuncSetAttribute((const void*)k1_kvproj, hipFuncAttributeMaxDynamicSharedMemorySize, K1_LDS);
  (void)hipFuncSetAttribute((const void*)k2_attn, hipFuncAttributeMaxDynamicSharedMemorySize, K2_LDS);
  (void)hipFuncSetAttribute((const void*)k3_oproj, hipFuncAttributeMaxDynamicSharedMemorySize, K3_LDS);
  (void)hipFuncSetAttribute((const void*)k56_mix<0>, hipFuncAttributeMaxDynamicSharedMemorySize, K56_LDS);
  (void)hipFuncSetAttribute((const void*)k56_mix<1>, hipFuncAttributeMaxDynamicSharedMemorySize, K56_LDS);
  k1_kvproj<<<256, 512, K1_LDS, stream>>>(x, ckv, wuk, wuv, KF, VF, XN, wq);
  k2_attn<<<256, 512, K2_LDS, stream>>>(XN, wq, KF, VF, CF, (float*)(ws + WS_LAM), wo);
  k3_oproj<<<256, 512, K3_LDS, stream>>>(CF, wo, PT);
  k4_ln<<<256, 320, 0, stream>>>(x, PT, H, HN, w1);
  k56_mix<0><<<256, 512, K56_LDS, stream>>>(w1, HN, b1, nullptr, UF, w2);
  k56_mix<1><<<256, 512, K56_LDS, stream>>>(w2, UF, b2, H, (u16*)d_out, nullptr);
}
```

```cpp
#include <hip/hip_runtime.h>
#include <stdint.h>
#include <utility>

namespace {

typedef unsigned short u16;
typedef __bf16 bf16x8 __attribute__((ext_vector_type(8)));
typedef __bf16 bf16x2 __attribute__((ext_vector_type(2)));
typedef short s16x4 __attribute__((ext_vector_type(4)));
typedef short s16x8 __attribute__((ext_vector_type(8)));
typedef float f32x2 __attribute__((ext_vector_type(2)));
typedef float f32x4 __attribute__((ext_vector_type(4)));
typedef float f32x16 __attribute__((ext_vector_type(16)));
typedef unsigned u32x2 __attribute__((ext_vector_type(2)));
typedef unsigned u32x4 __attribute__((ext_vector_type(4)));
#define LAS __attribute__((address_space(3)))
#define DI __device__ __forceinline__
#define SBAR() __builtin_amdgcn_sched_barrier(0)
#define RFL(x) __builtin_amdgcn_readfirstlane(x)

constexpr int SEQ = 2048, DM = 640, DC = 512, NH = 32, HD = 128, FF = 1728;
constexpr float LN_EPS = 1e-5f;
constexpr float QSCALE = 0.08838834764831845f * 1.4426950408889634f;
constexpr float P_LIMIT = 1073741824.0f;
constexpr float NEG_BIG = -1e30f;

constexpr size_t WS_XN = 0;
constexpr size_t WS_KF = WS_XN + (size_t)SEQ * DM * 2;
constexpr size_t WS_VF = WS_KF + (size_t)NH * SEQ * HD * 2;
constexpr size_t WS_CF = WS_VF + (size_t)NH * SEQ * HD * 2;
constexpr size_t WS_PT = WS_CF + (size_t)NH * SEQ * HD * 2;
constexpr size_t WS_H = WS_PT + (size_t)4 * SEQ * DM * 2;
constexpr size_t WS_HN = WS_H + (size_t)SEQ * DM * 4;
constexpr size_t WS_UF = WS_HN + (size_t)SEQ * DM * 2;
constexpr size_t WS_LAM = WS_UF + (size_t)DM * FF * 2;
constexpr size_t WS_END = WS_LAM + 256 * 256 * 4;

DI __amdgpu_buffer_rsrc_t mk_rsrc(const void* p, unsigned bytes) {
  return __builtin_amdgcn_make_buffer_rsrc((void*)p, 0, bytes, 0x00020000);
}
DI void dma1(__amdgpu_buffer_rsrc_t rsrc, unsigned voff, unsigned soff, unsigned ldsaddr) {
  asm volatile("s_mov_b32 m0, %0\n\ts_nop 0\n\tbuffer_load_dwordx4 %1, %2, %3 offen lds"
               ::"s"(ldsaddr), "v"(voff), "s"(rsrc), "s"(soff) : "memory", "m0");
}
DI void dma4(__amdgpu_buffer_rsrc_t rsrc, unsigned voff, unsigned soff, unsigned ldsaddr) {
  asm volatile("s_mov_b32 m0, %0\n\ts_nop 0\n\t"
               "buffer_load_dwordx4 %1, %2, %3 offen lds\n\t"
               "buffer_load_dwordx4 %1, %2, %3 offen offset:1024 lds\n\t"
               "buffer_load_dwordx4 %1, %2, %3 offen offset:2048 lds\n\t"
               "buffer_load_dwordx4 %1, %2, %3 offen offset:3072 lds"
               ::"s"(ldsaddr), "v"(voff), "s"(rsrc), "s"(soff) : "memory", "m0");
}
DI void warm8k(__amdgpu_buffer_rsrc_t rsrc, unsigned voff128, unsigned soff, unsigned ldsaddr) {
  asm volatile("s_mov_b32 m0, %0\n\ts_nop 0\n\tbuffer_load_dword %1, %2, %3 offen lds"
               ::"s"(ldsaddr), "v"(voff128), "s"(rsrc), "s"(soff) : "memory", "m0");
}
#define WAIT_VL_BAR(N) asm volatile("s_waitcnt vmcnt(" #N ") lgkmcnt(0)\n\ts_barrier" ::: "memory")
#define WAIT_L_BAR() asm volatile("s_waitcnt lgkmcnt(0)\n\ts_barrier" ::: "memory")
#define WAIT_V(N) asm volatile("s_waitcnt vmcnt(" #N ")" ::: "memory")
#define WAIT_L0() asm volatile("s_waitcnt lgkmcnt(0)" ::: "memory")

DI bf16x8 lds_b128(LAS const char* p) { return *(LAS const bf16x8*)p; }
DI bf16x8 lds_tr2(LAS const char* p0, LAS const char* p1) {
  s16x4 lo = __builtin_amdgcn_ds_read_tr16_b64_v4i16((LAS s16x4*)p0);
  s16x4 hi = __builtin_amdgcn_ds_read_tr16_b64_v4i16((LAS s16x4*)p1);
  s16x8 w = __builtin_shufflevector(lo, hi, 0, 1, 2, 3, 4, 5, 6, 7);
  return __builtin_bit_cast(bf16x8, w);
}
DI unsigned cvtpk(float lo, float hi) {
  f32x2 v = {lo, hi};
  bf16x2 b = __builtin_convertvector(v, bf16x2);
  return __builtin_bit_cast(unsigned, b);
}
DI u32x4 pack8(const f32x16& a, int o) {
  u32x4 w = {cvtpk(a[o], a[o + 1]), cvtpk(a[o + 2], a[o + 3]), cvtpk(a[o + 4], a[o + 5]), cvtpk(a[o + 6], a[o + 7])};
  return w;
}
DI float bf2f(unsigned short b) { return __uint_as_float(((unsigned)b) << 16); }
DI float bflo(unsigned w) { return __uint_as_float(w << 16); }
DI float bfhi(unsigned w) { return __uint_as_float(w & 0xffff0000u); }
#define MFMA32(a, b, c) __builtin_amdgcn_mfma_f32_32x32x16_bf16((a), (b), (c), 0, 0, 0)
#define MFMA16(a, b, c) __builtin_amdgcn_mfma_f32_16x16x32_bf16((a), (b), (c), 0, 0, 0)
#define STAMP_DECL()
#define STAMP()
#define STAMP_END(kid, dbg)
DI float swap_max(float v) {
  auto rr = __builtin_amdgcn_permlane32_swap(__float_as_uint(v), __float_as_uint(v), false, false);
  return fmaxf(__uint_as_float(rr[0]), __uint_as_float(rr[1]));
}
DI float swap_add(float v) {
  auto rr = __builtin_amdgcn_permlane32_swap(__float_as_uint(v), __float_as_uint(v), false, false);
  return __uint_as_float(rr[0]) + __uint_as_float(rr[1]);
}

constexpr int K1_LDS = 4 * 32768 + 2048;

template <bool ISV>
DI void k1_body(LAS char* lds, unsigned lds0, int wid, int lane, int head, int tb, const u16* __restrict__ ckv,
                const u16* __restrict__ wsrc, const u16* __restrict__ x, u16* __restrict__ KF, u16* __restrict__ VF,
                u16* __restrict__ XN, const u16* __restrict__ wq
                ) {
  const int wm = wid >> 2, wn = wid & 3;
  const int r = lane & 31, h = lane >> 5;
  const int g1 = (lane >> 4) & 1, q = (lane & 15) >> 2, p = lane & 3;
  const __amdgpu_buffer_rsrc_t rA = mk_rsrc(ckv, SEQ * DC * 2);
  const __amdgpu_buffer_rsrc_t rWm = mk_rsrc(wsrc, DC * NH * HD * 2);
  const unsigned voffA = (lane >> 2) * 1024 + (((lane & 3) ^ ((lane >> 4) & 3)) << 4);
  const unsigned voffW = ((lane >> 2) & 7) * 8192 + (lane >> 5) * 64 + (lane & 3) * 16;
  auto piece = [&](int kt, int slot, int i) __attribute__((always_inline)) {
    const unsigned sb = lds0 + slot * 32768;
    if (i < 2) dma1(rA, voffA, (unsigned)((tb * 256 + (wid * 2 + i) * 16) * 1024 + kt * 64), sb + (wid * 2 + i) * 1024);
    else dma1(rWm, voffW, (unsigned)((kt * 32 + (wid >> 1) * 8) * 8192 + head * 256 + (i - 2) * 128),
              sb + 16384 + ((wid >> 1) * 4 + (wid & 1) * 2 + (i - 2)) * 1024);
  };
  unsigned abase[2];
#pragma unroll
  for (int s = 0; s < 2; ++s) abase[s] = wm * 8192 + r * 64 + (((2 * s + h) ^ ((r >> 2) & 3)) << 4);
  const unsigned wbase = 16384 + h * 4096 + wn * 1024 + q * 64 + g1 * 32 + p * 8;

  f32x16 acc[4][2];
#pragma unroll
  for (int i = 0; i < 4; ++i)
#pragma unroll
    for (int n = 0; n < 2; ++n) acc[i][n] = f32x16{};
  auto rd = [&](bf16x8 (&a)[4], bf16x8 (&w)[2], LAS const char* sb, int s) {
#pragma unroll
    for (int n = 0; n < 2; ++n) w[n] = lds_tr2(sb + wbase + s * 8192 + n * 512, sb + wbase + s * 8192 + n * 512 + 256);
#pragma unroll
    for (int i = 0; i < 4; ++i) a[i] = lds_b128(sb + abase[s] + i * 2048);
  };
  auto mm2 = [&](const bf16x8 (&a)[4], const bf16x8 (&w)[2], int i) {
#pragma unroll
    for (int n = 0; n < 2; ++n) {
      if (ISV) acc[i][n] = MFMA32(a[i], w[n], acc[i][n]);
      else acc[i][n] = MFMA32(w[n], a[i], acc[i][n]);
    }
  };
  bf16x8 aA[4], wA[2], aB[4], wB[2];
  STAMP_DECL();
  STAMP();
#pragma unroll
  for (int kt = 0; kt < 3; ++kt)
#pragma unroll
    for (int i = 0; i < 4; ++i) piece(kt, kt, i);
  auto stage = [&](const int kt, const int slot, const bool more, const bool first) __attribute__((always_inline)) {
    STAMP();
    LAS const char* sb = lds + slot * 32768;
    rd(aA, wA, sb, 0);
    SBAR();
    if (!first) {
      mm2(aB, wB, 0); mm2(aB, wB, 1);
      if (more) piece(kt + 3, (slot + 3) & 3, 0);
      SBAR();
      mm2(aB, wB, 2); mm2(aB, wB, 3);
      if (more) piece(kt + 3, (slot + 3) & 3, 1);
    } else {
      piece(3, 3, 0); piece(3, 3, 1);
    }
    SBAR();
    rd(aB, wB, sb, 1);
    SBAR();
    mm2(aA, wA, 0); mm2(aA, wA, 1);
    if (more) piece(kt + 3, (slot + 3) & 3, 2);
    SBAR();
    mm2(aA, wA, 2); mm2(aA, wA, 3);
    if (more) piece(kt + 3, (slot + 3) & 3, 3);
    SBAR();
  };
  WAIT_VL_BAR(8); stage(0, 0, true, true);
  for (int k4 = 1; k4 < 13; k4 += 4) {
    WAIT_VL_BAR(8); stage(k4, 1, true, false);
    WAIT_VL_BAR(8); stage(k4 + 1, 2, true, false);
    WAIT_VL_BAR(8); stage(k4 + 2, 3, true, false);
    WAIT_VL_BAR(8); stage(k4 + 3, 0, true, false);
  }
  WAIT_VL_BAR(8); stage(13, 1, false, false);
  WAIT_VL_BAR(4); stage(14, 2, false, false);
  WAIT_VL_BAR(0); stage(15, 3, false, false);
  mm2(aB, wB, 0); mm2(aB, wB, 1); mm2(aB, wB, 2); mm2(aB, wB, 3);
  STAMP();
  {
    const __amdgpu_buffer_rsrc_t rQ = mk_rsrc(wq, DM * NH * HD * 2);
    const int u = (blockIdx.x >> 3) * 8 + wid;
    if (u < 80) warm8k(rQ, (unsigned)((lane >> 3) * 8192 + (lane & 7) * 128), (unsigned)(u * 8 * 8192 + (blockIdx.x & 7) * 1024), lds0);
  }
  const int token = blockIdx.x * 8 + wid;
  const bool has1 = lane < 16;
  const u32x4 v0 = *(const u32x4*)(x + (size_t)token * DM + lane * 8);
  u32x4 v1 = {0u, 0u, 0u, 0u};
  if (has1) v1 = *(const u32x4*)(x + (size_t)token * DM + 512 + lane * 8);
  SBAR();
#pragma unroll
  for (int i = 0; i < 4; ++i) {
    const int kb32 = tb * 8 + wm * 4 + i;
#pragma unroll
    for (int n = 0; n < 2; ++n) {
#pragma unroll
      for (int sg = 0; sg < 2; ++sg) {
        const u32x4 w = pack8(acc[i][n], 8 * sg);
        if (ISV) {
          const int db = 2 * (wn - 2) + n;
          *(u32x4*)(VF + ((size_t)((((head * 64 + kb32) * 4 + db) * 2 + sg) * 64 + lane)) * 8) = w;
        } else {
          const int db = 2 * wn + n;
          *(u32x4*)(KF + ((size_t)(((head * 64 + kb32) * 8 + 2 * db + sg) * 64 + lane)) * 8) = w;
        }
      }
    }
  }
  STAMP();
  {
    float f0[8], f1[8];
#pragma unroll
    for (int j = 0; j < 4; ++j) {
      f0[2 * j] = bflo(v0[j]); f0[2 * j + 1] = bfhi(v0[j]);
      f1[2 * j] = bflo(v1[j]); f1[2 * j + 1] = bfhi(v1[j]);
    }
    float s = 0.f;
#pragma unroll
    for (int j = 0; j < 8; ++j) s += f0[j] + f1[j];
#pragma unroll
    for (int o = 1; o < 64; o <<= 1) s += __shfl_xor(s, o);
    const float mean = s * (1.0f / DM);
    float sq = 0.f;
#pragma unroll
    for (int j = 0; j < 8; ++j) {
      const float d0 = f0[j] - mean; sq += d0 * d0;
      const float d1 = has1 ? f1[j] - mean : 0.f; sq += d1 * d1;
    }
#pragma unroll
    for (int o = 1; o < 64; o <<= 1) sq += __shfl_xor(sq, o);
    const float rstd = 1.0f / sqrtf(sq * (1.0f / DM) + LN_EPS);
    const int tb32 = token >> 5, rr = token & 31;
    u32x4 o0, o1;
#pragma unroll
    for (int j = 0; j < 4; ++j) {
      o0[j] = cvtpk((f0[2 * j] - mean) * rstd, (f0[2 * j + 1] - mean) * rstd);
      o1[j] = cvtpk((f1[2 * j] - mean) * rstd, (f1[2 * j + 1] - mean) * rstd);
    }
    *(u32x4*)(XN + ((size_t)((tb32 * 40 + (lane >> 1)) * 64 + (lane & 1) * 32 + rr)) * 8) = o0;
    if (has1) *(u32x4*)(XN + ((size_t)((tb32 * 40 + 32 + (lane >> 1)) * 64 + (lane & 1) * 32 + rr)) * 8) = o1;
  }
  STAMP();
  STAMP();
  STAMP_END(0, dbgp);
}

__global__ __launch_bounds__(512, 2) void k1_kvproj(const u16* __restrict__ x, const u16* __restrict__ ckv,
                                                    const u16* __restrict__ wuk, const u16* __restrict__ wuv,
                                                    u16* __restrict__ KF, u16* __restrict__ VF, u16* __restrict__ XN,
                                                    const u16* __restrict__ wq) {
  extern __shared__ __attribute__((aligned(16))) char smem[];
  LAS char* lds = (LAS char*)smem;
  const unsigned lds0 = (unsigned)(uintptr_t)lds;
  const int tid = threadIdx.x, lane = tid & 63;
  const int wid = RFL(tid >> 6);
  const int b = blockIdx.x, xcd = b & 7, idx = b >> 3;
  const int head = xcd * 4 + (idx & 3), tb = idx >> 2;
  const u16* wsrc = (wid & 1) ? wuv : wuk;
#define K1_EXTRA
  if ((wid & 3) >= 2) k1_body<true>(lds, lds0, wid, lane, head, tb, ckv, wsrc, x, KF, VF, XN, wq K1_EXTRA);
  else k1_body<false>(lds, lds0, wid, lane, head, tb, ckv, wsrc, x, KF, VF, XN, wq K1_EXTRA);
}

constexpr int K2_LDS = 163840;
constexpr unsigned L_KB1 = 0, L_VB0 = 16384, L_VA1 = 32768, L_KA0 = 49152, L_KB0 = 65536, L_KA1 = 81920, L_VA0 = 98304,
                   L_VB1 = 114688, L_STASH = 131072;
constexpr int NSTEP = 17;
#define SPRIO 2
#define MPRIO 0
#define TRAILPRIO SPRIO

template <int OFF>
DI void dma1o(__amdgpu_buffer_rsrc_t rsrc, unsigned voff, unsigned soff, unsigned ldsaddr) {
  asm volatile("s_mov_b32 m0, %0\n\ts_nop 0\n\tbuffer_load_dwordx4 %1, %2, %3 offen offset:%4 lds"
               ::"s"(ldsaddr), "v"(voff), "s"(rsrc), "s"(soff), "i"(OFF) : "memory", "m0");
}
#define XEXP(x) __builtin_amdgcn_exp2f(x)
#define MX3(a, b, c) __builtin_fmaxf(__builtin_fmaxf((a), (b)), (c))
#define PIN(x) asm volatile("" : "+v"(x))
DI float fmul_s(float a, float b) { float r; asm("v_mul_f32 %0, %1, %2" : "=v"(r) : "v"(a), "v"(b)); return r; }
DI float fadd_s(float a, float b) { float r; asm("v_add_f32 %0, %1, %2" : "=v"(r) : "v"(a), "v"(b)); return r; }
DI float fsub_s(float a, float b) { float r; asm("v_sub_f32 %0, %1, %2" : "=v"(r) : "v"(a), "v"(b)); return r; }
DI void add_in(f32x16& s, const f32x16& t) {
#pragma unroll
  for (int e = 0; e < 16; ++e) asm volatile("v_add_f32 %0, %0, %1" : "+v"(s[e]) : "v"(t[e]));
}
DI void add_in(f32x16& s, float t) {
#pragma unroll
  for (int e = 0; e < 16; ++e) asm volatile("v_add_f32 %0, %0, %1" : "+v"(s[e]) : "v"(t));
}
DI void sub_in(f32x16& s, float t) {
#pragma unroll
  for (int e = 0; e < 16; ++e) asm volatile("v_sub_f32 %0, %0, %1" : "+v"(s[e]) : "v"(t));
}
DI void set_in(f32x16& s, float t) {
#pragma unroll
  for (int e = 0; e < 16; ++e) asm volatile("v_mov_b32 %0, %1" : "+v"(s[e]) : "v"(t));
}
DI float ffma_s(float a, float b, float c) { float r; asm("v_fma_f32 %0, %1, %2, %3" : "=v"(r) : "v"(a), "v"(b), "v"(c)); return r; }
#define ACLOB "a8","a9","a10","a11","a12","a13","a14","a15","a16","a17","a18","a19","a20","a21","a22","a23","a24","a25","a26","a27","a28","a29","a30","a31","a32","a33","a34","a35","a36","a37","a38","a39","a40","a41","a42","a43","a44","a45","a46","a47","a48","a49","a50","a51","a52","a53","a54","a55","a56","a57","a58","a59","a60","a61","a62","a63","a64","a65","a66","a67","a68","a69","a70","a71","a72","a73","a74","a75","a76","a77","a78","a79","a80","a81","a82","a83","a84","a85","a86","a87","a88","a89","a90","a91","a92","a93","a94","a95","a96","a97","a98","a99","a100","a101","a102","a103","a104","a105","a106","a107","a108","a109","a110","a111","a112","a113","a114","a115","a116","a117","a118","a119","a120","a121","a122","a123","a124","a125","a126","a127"
constexpr int A_O = 8, A_Q = 72, A_R = 104, NRING = 6;
template <int SLOT, int OFF> DI void ring_read(unsigned addr) {
  asm volatile("ds_read_b128 a[%c1:%c2], %0 offset:%c3" ::"v"(addr), "i"(A_R + 4 * SLOT), "i"(A_R + 4 * SLOT + 3), "i"(OFF) : "memory");
}
template <int S, int OFF> DI void q_read(unsigned addr) {
  asm volatile("ds_read_b128 a[%c1:%c2], %0 offset:%c3" ::"v"(addr), "i"(A_Q + 4 * S), "i"(A_Q + 4 * S + 3), "i"(OFF) : "memory");
}
constexpr int V_NEGM = 64, V_S0 = 80, V_S1 = 96, V_PF = 112;
template <int W, int SLOT, int S, int DST> DI void qk_head() {
  asm volatile("s_waitcnt lgkmcnt(%c0)\n\tv_mfma_f32_32x32x16_bf16 v[%c1:%c2], a[%c3:%c4], a[%c5:%c6], v[%c7:%c8]"
               ::"i"(W), "i"(DST), "i"(DST + 15), "i"(A_R + 4 * SLOT), "i"(A_R + 4 * SLOT + 3), "i"(A_Q + 4 * S), "i"(A_Q + 4 * S + 3),
                 "i"(V_NEGM), "i"(V_NEGM + 15));
}
template <int W, int SLOT, int S, int DST> DI void qk_mfma() {
  asm volatile("s_waitcnt lgkmcnt(%c0)\n\tv_mfma_f32_32x32x16_bf16 v[%c1:%c2], a[%c3:%c4], a[%c5:%c6], v[%c1:%c2]"
               ::"i"(W), "i"(DST), "i"(DST + 15), "i"(A_R + 4 * SLOT), "i"(A_R + 4 * SLOT + 3), "i"(A_Q + 4 * S), "i"(A_Q + 4 * S + 3));
}
template <int W, int SLOT, int D, int P> DI void pv_mfma() {
  asm volatile("s_waitcnt lgkmcnt(%c0)\n\tv_mfma_f32_32x32x16_bf16 a[%c1:%c2], a[%c3:%c4], v[%c5:%c6], a[%c1:%c2]"
               ::"i"(W), "i"(A_O + 16 * D), "i"(A_O + 16 * D + 15), "i"(A_R + 4 * SLOT), "i"(A_R + 4 * SLOT + 3), "i"(V_PF + 4 * P),
                 "i"(V_PF + 4 * P + 3));
}
template <int W, int SLOT, int D, int P> DI void pv_zero() {
  asm volatile("s_waitcnt lgkmcnt(%c0)\n\tv_mfma_f32_32x32x16_bf16 a[%c1:%c2], a[%c3:%c4], v[%c5:%c6], 0"
               ::"i"(W), "i"(A_O + 16 * D), "i"(A_O + 16 * D + 15), "i"(A_R + 4 * SLOT), "i"(A_R + 4 * SLOT + 3), "i"(V_PF + 4 * P),
                 "i"(V_PF + 4 * P + 3));
}
template <int S, int P> DI void exps8(float& ps0, float& ps1, float& ps2, float& ps3) {
  float t0, t1, t2, t3, t4, t5, t6, t7;
  asm volatile("v_exp_f32 %4, v[%c12]\n\tv_exp_f32 %5, v[%c12+1]\n\tv_exp_f32 %6, v[%c12+2]\n\tv_exp_f32 %7, v[%c12+3]\n\t"
               "v_exp_f32 %8, v[%c12+4]\n\tv_exp_f32 %9, v[%c12+5]\n\tv_exp_f32 %10, v[%c12+6]\n\tv_exp_f32 %11, v[%c12+7]\n\t"
               "v_add_f32 %0, %0, %4\n\tv_add_f32 %1, %1, %5\n\tv_cvt_pk_bf16_f32 v[%c13], %4, %5\n\t"
               "v_add_f32 %2, %2, %6\n\tv_add_f32 %3, %3, %7\n\tv_cvt_pk_bf16_f32 v[%c13+1], %6, %7\n\t"
               "v_add_f32 %0, %0, %8\n\tv_add_f32 %1, %1, %9\n\tv_cvt_pk_bf16_f32 v[%c13+2], %8, %9\n\t"
               "v_add_f32 %2, %2, %10\n\tv_add_f32 %3, %3, %11\n\tv_cvt_pk_bf16_f32 v[%c13+3], %10, %11"
               : "+v"(ps0), "+v"(ps1), "+v"(ps2), "+v"(ps3), "=&v"(t0), "=&v"(t1), "=&v"(t2), "=&v"(t3), "=&v"(t4), "=&v"(t5),
                 "=&v"(t6), "=&v"(t7)
               : "i"(S), "i"(V_PF + 4 * P));
}
DI float exps32() {
  float ps0 = 0.f, ps1 = 0.f, ps2 = 0.f, ps3 = 0.f, r;
  exps8<V_S0, 0>(ps0, ps1, ps2, ps3); exps8<V_S0 + 8, 1>(ps0, ps1, ps2, ps3);
  exps8<V_S1, 2>(ps0, ps1, ps2, ps3); exps8<V_S1 + 8, 3>(ps0, ps1, ps2, ps3);
  asm volatile("v_add_f32 %1, %1, %2\n\tv_add_f32 %3, %3, %4\n\tv_add_f32 %0, %1, %3" : "=v"(r), "+v"(ps0), "+v"(ps1), "+v"(ps2), "+v"(ps3));
  return r;
}
DI float rowmax32() {
  float a, b;
  asm volatile("v_max3_f32 %0, v80, v81, v82\n\tv_max3_f32 %1, v83, v84, v85\n\tv_max3_f32 %0, %0, v86, v87\n\tv_max3_f32 %1, %1, v88, v89\n\t"
               "v_max3_f32 %0, %0, v90, v91\n\tv_max3_f32 %1, %1, v92, v93\n\tv_max3_f32 %0, %0, v94, v95\n\tv_max3_f32 %1, %1, v96, v97\n\t"
               "v_max3_f32 %0, %0, v98, v99\n\tv_max3_f32 %1, %1, v100, v101\n\tv_max3_f32 %0, %0, v102, v103\n\tv_max3_f32 %1, %1, v104, v105\n\t"
               "v_max3_f32 %0, %0, v106, v107\n\tv_max3_f32 %1, %1, v108, v109\n\tv_max3_f32 %0, %0, v110, v111\n\tv_max_f32 %0, %0, %1"
               : "=&v"(a), "=&v"(b));
  return a;
}
template <int R> DI void vreg_add(float t) { asm volatile("v_add_f32 v[%c1], v[%c1], %0" ::"v"(t), "i"(R)); }
template <int S, int... E> DI void mask_all_(float ninf, std::integer_sequence<int, E...>) { (vreg_add<S + E>(ninf), ...); }
template <int S, int... E> DI void mask_tri_(int dq, float ninf, std::integer_sequence<int, E...>) {
  (vreg_add<S + E>(((E & 3) + 8 * (E >> 2) > dq) ? ninf : 0.f), ...);
}
template <int I> DI float acc_rd() { float r; asm volatile("v_accvgpr_read_b32 %0, a[%c1]" : "=v"(r) : "i"(I)); return r; }
template <int I> DI void acc_wr(float v) { asm volatile("v_accvgpr_write_b32 a[%c1], %0" ::"v"(v), "i"(I)); }
template <int D, int... E> DI void o_read_(f32x16& t, std::integer_sequence<int, E...>) { ((t[E] = acc_rd<A_O + 16 * D + E>()), ...); }
template <int D> DI f32x16 o_read() { f32x16 t; o_read_<D>(t, std::make_integer_sequence<int, 16>{}); return t; }
template <int... E> DI void o_zero_(std::integer_sequence<int, E...>) { (acc_wr<A_O + E>(0.f), ...); }
template <int... E> DI void o_scale_(float f, std::integer_sequence<int, E...>) { (acc_wr<A_O + E>(fmul_s(acc_rd<A_O + E>(), f)), ...); }
template <int R> DI u32x4 acc_scale8(float f) {
  u32x4 o; float t0, t1, t2, t3, t4, t5, t6, t7;
  asm volatile("v_accvgpr_read_b32 %4, a[%c13]\n\tv_accvgpr_read_b32 %5, a[%c13+1]\n\tv_accvgpr_read_b32 %6, a[%c13+2]\n\t"
               "v_accvgpr_read_b32 %7, a[%c13+3]\n\tv_accvgpr_read_b32 %8, a[%c13+4]\n\tv_accvgpr_read_b32 %9, a[%c13+5]\n\t"
               "v_accvgpr_read_b32 %10, a[%c13+6]\n\tv_accvgpr_read_b32 %11, a[%c13+7]\n\t"
               "v_mul_f32 %4, %4, %12\n\tv_mul_f32 %5, %5, %12\n\tv_mul_f32 %6, %6, %12\n\tv_mul_f32 %7, %7, %12\n\t"
               "v_mul_f32 %8, %8, %12\n\tv_mul_f32 %9, %9, %12\n\tv_mul_f32 %10, %10, %12\n\tv_mul_f32 %11, %11, %12\n\t"
               "v_cvt_pk_bf16_f32 %0, %4, %5\n\tv_cvt_pk_bf16_f32 %1, %6, %7\n\tv_cvt_pk_bf16_f32 %2, %8, %9\n\tv_cvt_pk_bf16_f32 %3, %10, %11"
               : "=&v"(o[0]), "=&v"(o[1]), "=&v"(o[2]), "=&v"(o[3]), "=&v"(t0), "=&v"(t1), "=&v"(t2), "=&v"(t3), "=&v"(t4),
                 "=&v"(t5), "=&v"(t6), "=&v"(t7)
               : "v"(f), "i"(R));
  return o;
}
template <int R, int OFF> DI void acc_to_lds(unsigned addr) {
  asm volatile("ds_write_b128 %0, a[%c1:%c2] offset:%c3" ::"v"(addr), "i"(R), "i"(R + 3), "i"(OFF) : "memory");
}
template <int R> DI u32x4 acc_combine8f(const f32x4& w0, const f32x4& w1, float cw, float ca) {
  u32x4 o; float t0, t1, t2, t3, t4, t5, t6, t7;
  asm volatile("v_accvgpr_read_b32 %4, a[%c22]\n\tv_accvgpr_read_b32 %5, a[%c22+1]\n\tv_accvgpr_read_b32 %6, a[%c22+2]\n\t"
               "v_accvgpr_read_b32 %7, a[%c22+3]\n\tv_accvgpr_read_b32 %8, a[%c22+4]\n\tv_accvgpr_read_b32 %9, a[%c22+5]\n\t"
               "v_accvgpr_read_b32 %10, a[%c22+6]\n\tv_accvgpr_read_b32 %11, a[%c22+7]\n\t"
               "v_mul_f32 %4, %4, %21\n\tv_mul_f32 %5, %5, %21\n\tv_mul_f32 %6, %6, %21\n\tv_mul_f32 %7, %7, %21\n\t"
               "v_mul_f32 %8, %8, %21\n\tv_mul_f32 %9, %9, %21\n\tv_mul_f32 %10, %10, %21\n\tv_mul_f32 %11, %11, %21\n\t"
               "v_fma_f32 %4, %12, %20, %4\n\tv_fma_f32 %5, %13, %20, %5\n\tv_fma_f32 %6, %14, %20, %6\n\tv_fma_f32 %7, %15, %20, %7\n\t"
               "v_fma_f32 %8, %16, %20, %8\n\tv_fma_f32 %9, %17, %20, %9\n\tv_fma_f32 %10, %18, %20, %10\n\tv_fma_f32 %11, %19, %20, %11\n\t"
               "v_cvt_pk_bf16_f32 %0, %4, %5\n\tv_cvt_pk_bf16_f32 %1, %6, %7\n\tv_cvt_pk_bf16_f32 %2, %8, %9\n\tv_cvt_pk_bf16_f32 %3, %10, %11"
               : "=&v"(o[0]), "=&v"(o[1]), "=&v"(o[2]), "=&v"(o[3]), "=&v"(t0), "=&v"(t1), "=&v"(t2), "=&v"(t3), "=&v"(t4),
                 "=&v"(t5), "=&v"(t6), "=&v"(t7)
               : "v"(w0[0]), "v"(w0[1]), "v"(w0[2]), "v"(w0[3]), "v"(w1[0]), "v"(w1[1]), "v"(w1[2]), "v"(w1[3]), "v"(cw), "v"(ca),
                 "i"(R));
  return o;
}
DI void scores_add(float aone, float v) {
  asm volatile("s_nop 1\n\tv_mfma_f32_32x32x2_f32 v[80:95], %0, %1, v[80:95]\n\tv_mfma_f32_32x32x2_f32 v[96:111], %0, %1, v[96:111]" ::"v"(aone), "v"(v));
}
DI void negm_set(float aone, float v) { asm volatile("s_nop 1\n\tv_mfma_f32_32x32x2_f32 v[64:79], %0, %1, 0" ::"v"(aone), "v"(v)); }
#define ACC_FENCE() asm volatile("s_nop 15\n\ts_nop 3" ::: "memory")
#define BAR_A(WAITS) asm volatile("s_waitcnt " WAITS "\n\ts_barrier" ::: "memory", "v127", ACLOB)

template <int J> DI void frag_read(unsigned ka, unsigned va) {
  if constexpr (J < 16) ring_read<J % NRING, (J & 1) * 8192 + (J >> 1) * 1024>(ka);
  else { constexpr int i = J - 16; ring_read<J % NRING, (i >> 3) * 8192 + ((i & 3) * 2 + ((i >> 2) & 1)) * 1024>(va); }
}
constexpr bool XREAD = true;
constexpr bool XDMA = true;
#define PDX 4
constexpr int PD = PDX;

__global__ __launch_bounds__(512, 2) __attribute__((amdgpu_num_vgpr(64))) void k2_attn(const u16* __restrict__ XN, const u16* __restrict__ wq,
                                                  const u16* __restrict__ KF, const u16* __restrict__ VF,
                                                  u16* __restrict__ CF, float* __restrict__ LAM,
                                                  const u16* __restrict__ wo) {
  extern __shared__ __attribute__((aligned(16))) char smem[];
  LAS char* lds = (LAS char*)smem;
  const unsigned lds0 = (unsigned)(uintptr_t)lds;
  const int tid = threadIdx.x, lane = tid & 63;
  const int wid = RFL(tid >> 6);
  const int b = blockIdx.x, xcd = b & 7, idx = b >> 3;
  const int head = xcd * 4 + (idx >> 3), px = idx & 7;
  const int qbH = 15 - px, qbL = px;
  const int rr = wid & 3, c = wid >> 2;
  const int r = lane & 31, h = lane >> 5;
  const int g1 = (lane >> 4) & 1, q4 = (lane & 15) >> 2, p4 = lane & 3;
  const __amdgpu_buffer_rsrc_t rX = mk_rsrc(XN, SEQ * DM * 2);
  const __amdgpu_buffer_rsrc_t rW = mk_rsrc(wq, DM * NH * HD * 2);
  const __amdgpu_buffer_rsrc_t rK = mk_rsrc(KF, NH * SEQ * HD * 2);
  const __amdgpu_buffer_rsrc_t rV = mk_rsrc(VF, NH * SEQ * HD * 2);
  const unsigned vlin = lane * 16;
  const unsigned voffW = ((lane >> 2) & 7) * 8192 + (lane >> 5) * 64 + (lane & 3) * 16;
  const int p1 = 2 * (px + 1);
  auto kvoff = [&](int sg) -> unsigned {
    const int hidx = sg < p1 ? sg : 2 * sg - p1 + c;
    return (unsigned)((head * 64 + 2 * hidx) * 8192);
  };
  STAMP_DECL();
  STAMP();
  const int tbx = (wid < 4) ? (4 * qbH + wid) : (4 * qbL + wid - 4);
  auto q_load = [&](unsigned addr) {
    q_read<0, 0>(addr); q_read<1, 1024>(addr); q_read<2, 2048>(addr); q_read<3, 3072>(addr);
    q_read<4, 4096>(addr); q_read<5, 5120>(addr); q_read<6, 6144>(addr); q_read<7, 7168>(addr);
  };
  {
    auto q_piece = [&](int kt, int i) {
      const unsigned sb = lds0 + (unsigned)(kt % 3) * 49152;
      if (i < 2) dma1(rW, voffW, (unsigned)((kt * 64 + wid * 8) * 8192 + head * 256 + i * 128), sb + (wid * 2 + i) * 1024);
      else dma1(rX, vlin, (unsigned)((tbx * 40 + 4 * kt + (i - 2)) * 1024), sb + 16384 + wid * 4096 + (i - 2) * 1024);
    };
    auto kv_piece = [&](int i) {
      const unsigned part = wid * 2048 + i * 1024;
      dma1(rK, vlin, (unsigned)(head * 64 * 8192) + part, lds0 + L_KA0 + part);
    };
#pragma unroll
    for (int i = 0; i < 6; ++i) q_piece(0, i);
#pragma unroll
    for (int i = 0; i < 6; ++i) q_piece(1, i);
    f32x16 qa[2][2];
#pragma unroll
    for (int i = 0; i < 2; ++i)
#pragma unroll
      for (int n = 0; n < 2; ++n) qa[i][n] = f32x16{};
    const unsigned wbase = h * 2048 + c * 1024 + q4 * 64 + g1 * 32 + p4 * 8;
    const unsigned xbase = 16384 + rr * 4096 + lane * 16;
    bf16x8 wA[2], xA[2], wB[2], xB[2];
    auto rdq = [&](bf16x8 (&w)[2], bf16x8 (&x)[2], LAS const char* sb, int s) {
      w[0] = lds_tr2(sb + wbase + s * 4096, sb + wbase + s * 4096 + 256);
      x[0] = lds_b128(sb + xbase + s * 1024);
      w[1] = lds_tr2(sb + wbase + s * 4096 + 512, sb + wbase + s * 4096 + 768);
      x[1] = lds_b128(sb + xbase + 16384 + s * 1024);
    };
    auto mmq = [&](const bf16x8 (&w)[2], const bf16x8 (&x)[2]) {
      qa[0][0] = MFMA32(w[0], x[0], qa[0][0]);
      qa[1][0] = MFMA32(w[0], x[1], qa[1][0]);
      qa[0][1] = MFMA32(w[1], x[0], qa[0][1]);
      qa[1][1] = MFMA32(w[1], x[1], qa[1][1]);
    };
    for (int kt = 0; kt < 10; ++kt) {
      if (kt < 9) { WAIT_VL_BAR(6); } else { WAIT_VL_BAR(2); }
      LAS const char* sb = lds + (unsigned)(kt % 3) * 49152;
      auto piece = [&](int i) {
        if (kt < 8) q_piece(kt + 2, i);
        else if (kt == 8 && i < 2) kv_piece(i);
      };
      rdq(wA, xA, sb, 0);
      SBAR();
      if (kt > 0) mmq(wB, xB);
      piece(0); SBAR();
      rdq(wB, xB, sb, 1);
      SBAR();
      mmq(wA, xA);
      piece(1); piece(2); SBAR();
      rdq(wA, xA, sb, 2);
      SBAR();
      mmq(wB, xB);
      piece(3); piece(4); SBAR();
      rdq(wB, xB, sb, 3);
      SBAR();
      mmq(wA, xA);
      piece(5); SBAR();
    }
    mmq(wB, xB);
    STAMP();
    WAIT_L_BAR();
#pragma unroll
    for (int i = 0; i < 2; ++i) {
      LAS char* dst = lds + (i == 0 ? L_STASH : 0u) + rr * 8192 + lane * 16;
#pragma unroll
      for (int n = 0; n < 2; ++n) {
        const f32x16 t = qa[i][n] * QSCALE;
#pragma unroll
        for (int sg = 0; sg < 2; ++sg) *(LAS u32x4*)(dst + (4 * c + 2 * n + sg) * 1024) = pack8(t, 8 * sg);
      }
    }
    WAIT_L_BAR();
    q_load(lds0 + (c ? 0u : L_STASH) + rr * 8192 + lane * 16);
    BAR_A("vmcnt(0) lgkmcnt(0)");
  }
  STAMP();

  negm_set(0.f, 0.f);
  float mref = 0.f, lsum = 0.f, mfin = 0.f, lfin = 0.f;
  const unsigned kown0 = c ? L_KB0 : L_KA0, kown1 = c ? L_KB1 : L_KA1;
  const unsigned vown0 = c ? L_VB0 : L_VA0, vown1 = c ? L_VB1 : L_VA1;
  const unsigned lrd = lds0 + lane * 16;
  float* lamp = LAM + (size_t)b * 256 + rr * 64;
  f32x2 mlA = {0.f, 0.f};

  auto refix = [&](const bool first) __attribute__((always_inline)) {
    const float rm = swap_max(rowmax32());
    const float dl = first ? fmaxf(rm, NEG_BIG) : fmaxf(rm, 0.f);
    const float f = first ? 0.f : __builtin_amdgcn_exp2f(-dl);
    mref = first ? dl : mref + dl;
    lsum = first ? 0.f : lsum * f;
    int ln = lane;
    asm volatile("" : "+v"(ln));
    const float aone = ln < 32 ? 1.f : 0.f;
    scores_add(aone, -dl);
    negm_set(aone, -mref);
    ACC_FENCE();
    if (!first) { o_scale_(f, std::make_integer_sequence<int, 64>{}); ACC_FENCE(); }
  };
#define TRAIL() do { pv_mfma<0, 28 % NRING, 0, 3>(); pv_mfma<0, 29 % NRING, 1, 3>(); pv_mfma<0, 30 % NRING, 2, 3>(); pv_mfma<0, 31 % NRING, 3, 3>(); SBAR(); } while (0)
  auto splain = [&]() __attribute__((always_inline)) {
    __builtin_amdgcn_s_setprio(TRAILPRIO);
    TRAIL();
    if (TRAILPRIO != SPRIO) __builtin_amdgcn_s_setprio(SPRIO);
    float pst = exps32();
    if (__builtin_expect(__any(pst > P_LIMIT), 0)) { refix(false); pst = exps32(); }
    asm volatile("v_add_f32 %0, %0, %1" : "+v"(lsum) : "v"(pst));
    __builtin_amdgcn_s_setprio(MPRIO);
  };
  auto sgen = [&](const int tau) __attribute__((always_inline)) {
    __builtin_amdgcn_s_setprio(SPRIO);
    if (tau > 0) TRAIL();
    const bool lastL = c == 1 && tau == p1 - 1;
    const bool diag = tau == NSTEP - 1 || (c == 1 && (tau == p1 - 2 || tau == p1 - 1));
    const bool first = tau == 0 || (c == 1 && tau == p1);
    int ln = lane;
    asm volatile("" : "+v"(ln));
    if (diag) {
      const int hp = tau == NSTEP - 1 ? c : tau - (p1 - 2);
      float ninf = -INFINITY;
      asm volatile("" : "+v"(ninf));
      const int dq = (ln & 31) - 4 * (ln >> 5);
      if (2 * hp > rr) mask_all_<V_S0>(ninf, std::make_integer_sequence<int, 16>{});
      else if (2 * hp == rr) mask_tri_<V_S0>(dq, ninf, std::make_integer_sequence<int, 16>{});
      if (2 * hp + 1 > rr) mask_all_<V_S1>(ninf, std::make_integer_sequence<int, 16>{});
      else if (2 * hp + 1 == rr) mask_tri_<V_S1>(dq, ninf, std::make_integer_sequence<int, 16>{});
    }
    if (first && tau > 0) {
      ACC_FENCE();
      const float inv = 1.0f / lfin;
      u16* dstp = CF + ((size_t)(((4 * qbL + rr) * 32 + head) * 8) * 64 + ln) * 8;
      *(u32x4*)(dstp) = acc_scale8<A_O + 0>(inv);          *(u32x4*)(dstp + 512) = acc_scale8<A_O + 8>(inv);
      *(u32x4*)(dstp + 1024) = acc_scale8<A_O + 16>(inv);  *(u32x4*)(dstp + 1536) = acc_scale8<A_O + 24>(inv);
      *(u32x4*)(dstp + 2048) = acc_scale8<A_O + 32>(inv);  *(u32x4*)(dstp + 2560) = acc_scale8<A_O + 40>(inv);
      *(u32x4*)(dstp + 3072) = acc_scale8<A_O + 48>(inv);  *(u32x4*)(dstp + 3584) = acc_scale8<A_O + 56>(inv);
    }
    if (first) refix(true);
    float pst = exps32();
    if (!first && __any(pst > P_LIMIT)) { refix(false); pst = exps32(); }
    asm volatile("v_add_f32 %0, %0, %1" : "+v"(lsum) : "v"(pst));
    if (lastL || tau == NSTEP - 1) {
      mfin = mref; lfin = swap_add(lsum);
      if (lastL) {
        negm_set(0.f, 0.f);
        q_load(lds0 + L_STASH + rr * 8192 + ln * 16);
      } else {
        f32x2* lp = (f32x2*)lamp + (ln & 31);
        if (c == 0) { if (ln < 32) *lp = (f32x2){mfin, lfin}; }
        else mlA = *lp;
      }
    }
    __builtin_amdgcn_s_setprio(0);
  };
#define KVDMA(i) do { if ((i) == 0) dma1o<0>(rK, vlin, gk, lk); else if ((i) == 1) dma1o<1024>(rK, vlin, gk, lk); \
    else if ((i) == 2) dma1o<2048>(rK, vlin, gk, lk); else if ((i) == 3) dma1o<3072>(rK, vlin, gk, lk); \
    else if ((i) == 4) dma1o<0>(rV, vlin, gv, lv); else if ((i) == 5) dma1o<1024>(rV, vlin, gv, lv); \
    else if ((i) == 6) dma1o<2048>(rV, vlin, gv, lv); else dma1o<3072>(rV, vlin, gv, lv); } while (0)
#define MSTEP(J, FIRST, LAST, DMA, ZO) do { \
    constexpr int out_ = ((J) + PD - 1 < (LAST) ? (J) + PD - 1 : (LAST)) - (J);     \
    if constexpr ((J) < 2) qk_head<out_, (J) % NRING, 0, ((J) & 1) ? V_S1 : V_S0>(); \
    else if constexpr ((J) < 16) qk_mfma<out_, (J) % NRING, ((J) >> 1), ((J) & 1) ? V_S1 : V_S0>(); \
    else if constexpr ((J) < 20 && (ZO)) { if (zero_o) pv_zero<out_, (J) % NRING, (J) & 3, 0>(); else pv_mfma<out_, (J) % NRING, (J) & 3, 0>(); } \
    else pv_mfma<out_, (J) % NRING, (J) & 3, (((J) - 16) >> 2)>(); \
    if constexpr ((J) + PD <= (LAST)) frag_read<(J) + PD>(ka, va); \
    if constexpr ((DMA) && ((J) & 1) && (J) < 16) KVDMA((J) >> 1); \
    SBAR(); } while (0)
#define MSTEP4(J, FIRST, LAST, DMA, ZO) MSTEP(J, FIRST, LAST, DMA, ZO); MSTEP((J) + 1, FIRST, LAST, DMA, ZO); MSTEP((J) + 2, FIRST, LAST, DMA, ZO); MSTEP((J) + 3, FIRST, LAST, DMA, ZO)
#define KSLOT(t) (((t) < p1) ? (((t) & 1) ? L_KA1 : L_KA0) : (((t) & 1) ? kown1 : kown0))
#define VSLOT(t) (((t) < p1) ? (((t) & 1) ? L_VA1 : L_VA0) : (((t) & 1) ? vown1 : vown0))
#define STAGE_ADDR(t) \
      const unsigned gk = ((t) + 1 < NSTEP && (c == 0 || (t) + 1 >= p1)) ? kvoff((t) + 1) + rr * 4096 : 0x7ff00000u; \
      const unsigned gv = (c == 0 || (t) >= p1) ? kvoff(t) + rr * 4096 : 0x7ff00000u; \
      const unsigned lk = lds0 + ((((t) + 1) & 1) ? kown1 : kown0) + rr * 4096; \
      const unsigned lv = lds0 + (((t) & 1) ? vown1 : vown0) + rr * 4096;

  if (c == 1) asm volatile("s_barrier" ::: "memory");
  {
    const unsigned ka = lrd + L_KA0, va = 0;
    STAGE_ADDR(0)
    constexpr bool zero_o = false;
    frag_read<0>(ka, va); frag_read<1>(ka, va); frag_read<2>(ka, va); frag_read<3>(ka, va);
    SBAR();
    MSTEP4(0, 0, 15, true, false); MSTEP4(4, 0, 15, true, false); MSTEP4(8, 0, 15, true, false); MSTEP4(12, 0, 15, true, false);
    BAR_A("lgkmcnt(0)");
    ACC_FENCE();
    sgen(0);
    BAR_A("vmcnt(0) lgkmcnt(0)");
  }
#define MPHASE(tau, DMA, ZO) do { \
      const bool zero_o = (tau) == 1 || (c == 1 && (tau) == p1 + 1);     \
      (void)zero_o; \
      const unsigned ka = lrd + KSLOT(tau); \
      const unsigned va = lrd + VSLOT((tau) - 1); \
      STAGE_ADDR(tau) \
      (void)gk; (void)gv; (void)lk; (void)lv; \
      frag_read<0>(ka, va); frag_read<1>(ka, va); frag_read<2>(ka, va); frag_read<3>(ka, va); \
      SBAR(); \
      MSTEP4(0, 0, 31, DMA, ZO); MSTEP4(4, 0, 31, DMA, ZO); MSTEP4(8, 0, 31, DMA, ZO); MSTEP4(12, 0, 31, DMA, ZO); \
      MSTEP4(16, 0, 31, DMA, ZO); MSTEP4(20, 0, 31, DMA, ZO); MSTEP4(24, 0, 31, DMA, ZO); \
      BAR_A("lgkmcnt(0)"); } while (0)
  int tau = 1;
  while (tau < NSTEP) {
    const bool special = tau == 1 || tau == NSTEP - 1 || (c == 1 && tau >= p1 - 2 && tau <= p1 + 1);
    if (special) {
      STAMP();
      MPHASE(tau, true, true);
      STAMP();
      sgen(tau);
      BAR_A("vmcnt(0) lgkmcnt(0)");
      ++tau;
    } else if (c == 1 && tau < p1 - 2) {
      do {
        STAMP();
        MPHASE(tau, false, false);
        STAMP();
        splain();
        BAR_A("lgkmcnt(0)");
        ++tau;
      } while (tau < p1 - 2);
    } else {
      do {
        STAMP();
        MPHASE(tau, true, false);
        STAMP();
        splain();
        BAR_A("vmcnt(0) lgkmcnt(0)");
        ++tau;
      } while (tau < NSTEP - 1);
    }
  }
#undef MPHASE
#undef TRAIL
  {
    const unsigned v128 = lane * 128;
    const unsigned dead = lds0 + vown1 + rr * 256;
    const int u = idx * 8 + wid;
    if (u < 160) warm8k(mk_rsrc(wo, NH * HD * DM * 2), v128, (unsigned)((xcd >> 1) * 1024 * 1280 + u * 8192), dead);
  }
  {
    const unsigned ka = 0, va = lrd + VSLOT(NSTEP - 1);
    const unsigned gk = 0, gv = 0, lk = 0, lv = 0;
    (void)gk; (void)gv; (void)lk; (void)lv; (void)ka;
    const bool zero_o = c == 1 && p1 == NSTEP - 1;
    frag_read<16>(ka, va); frag_read<17>(ka, va); frag_read<18>(ka, va); frag_read<19>(ka, va);
    SBAR();
    MSTEP4(16, 16, 31, false, true); MSTEP4(20, 16, 31, false, true); MSTEP4(24, 16, 31, false, true); MSTEP4(28, 16, 31, false, true);
    BAR_A("lgkmcnt(0)");
    int ln = lane;
    asm volatile("" : "+v"(ln));
    const unsigned mine = lds0 + (c ? L_KB1 : L_KA0) + ((rr & 2) ? (c ? L_KB0 - L_KB1 : L_KA1 - L_KA0) : 0u) + (rr & 1) * 8192 + ln * 16;
    const unsigned theirs = (c ? L_KA0 : L_KB1) + ((rr & 2) ? (c ? L_KA1 - L_KA0 : L_KB0 - L_KB1) : 0u) + (rr & 1) * 8192 + ln * 16;
    ACC_FENCE();
    if (c == 0) {
      acc_to_lds<A_O + 32, 0>(mine); acc_to_lds<A_O + 36, 1024>(mine); acc_to_lds<A_O + 40, 2048>(mine); acc_to_lds<A_O + 44, 3072>(mine);
      acc_to_lds<A_O + 48, 4096>(mine); acc_to_lds<A_O + 52, 5120>(mine); acc_to_lds<A_O + 56, 6144>(mine); acc_to_lds<A_O + 60, 7168>(mine);
      BAR_A("lgkmcnt(0)");
    } else {
      acc_to_lds<A_O + 0, 0>(mine); acc_to_lds<A_O + 4, 1024>(mine); acc_to_lds<A_O + 8, 2048>(mine); acc_to_lds<A_O + 12, 3072>(mine);
      acc_to_lds<A_O + 16, 4096>(mine); acc_to_lds<A_O + 20, 5120>(mine); acc_to_lds<A_O + 24, 6144>(mine); acc_to_lds<A_O + 28, 7168>(mine);
      if (ln < 32) *(LAS f32x2*)(lds + L_STASH + rr * 256 + ln * 8) = (f32x2){mfin, lfin};
    }
    BAR_A("lgkmcnt(0)");
    {
      const f32x2 mo = c ? mlA : *(LAS const f32x2*)(lds + L_STASH + rr * 256 + (ln & 31) * 8);
      const float m = fmaxf(mo[0], mfin);
      const float eo = __builtin_amdgcn_exp2f(mo[0] - m), em = __builtin_amdgcn_exp2f(mfin - m);
      const float inv = 1.0f / (mo[1] * eo + lfin * em);
      const float co = eo * inv, cm = em * inv;
      f32x4 w[8];
#pragma unroll
      for (int i = 0; i < 8; ++i) w[i] = *(LAS const f32x4*)(lds + theirs + i * 1024);
      u16* dstp = CF + ((size_t)(((4 * qbH + rr) * 32 + head) * 8 + 4 * c) * 64 + ln) * 8;
      if (c == 0) {
        *(u32x4*)(dstp) = acc_combine8f<A_O + 0>(w[0], w[1], co, cm);         *(u32x4*)(dstp + 512) = acc_combine8f<A_O + 8>(w[2], w[3], co, cm);
        *(u32x4*)(dstp + 1024) = acc_combine8f<A_O + 16>(w[4], w[5], co, cm); *(u32x4*)(dstp + 1536) = acc_combine8f<A_O + 24>(w[6], w[7], co, cm);
      } else {
        *(u32x4*)(dstp) = acc_combine8f<A_O + 32>(w[0], w[1], co, cm);        *(u32x4*)(dstp + 512) = acc_combine8f<A_O + 40>(w[2], w[3], co, cm);
        *(u32x4*)(dstp + 1024) = acc_combine8f<A_O + 48>(w[4], w[5], co, cm); *(u32x4*)(dstp + 1536) = acc_combine8f<A_O + 56>(w[6], w[7], co, cm);
      }
    }
  }
#undef KSLOT
#undef VSLOT
#undef STAGE_ADDR
#undef KVDMA
#undef MSTEP
#undef MSTEP4
  STAMP();
  STAMP_END(1, DBGP(CF, WS_CF));
}

constexpr int K3_STAGE = 37888, K3_NST = 4, K3_LDS = K3_STAGE * K3_NST;

__global__ __launch_bounds__(512, 2) void k3_oproj(const u16* __restrict__ CF, const u16* __restrict__ wo,
                                                   u16* __restrict__ PT) {
  extern __shared__ __attribute__((aligned(16))) char smem[];
  LAS char* lds = (LAS char*)smem;
  const unsigned lds0 = (unsigned)(uintptr_t)lds;
  const int tid = threadIdx.x, lane = tid & 63;
  const int wid = RFL(tid >> 6);
  const int b = blockIdx.x, xcd = b & 7, idx = b >> 3;
  const int ks = xcd >> 1, panel = (xcd & 1) * 8 + (idx >> 2), nt = idx & 3;
  const int tbi = wid & 3, kp = wid >> 2;
  const int h = lane >> 5, g1 = (lane >> 4) & 1, q4 = (lane & 15) >> 2, p4 = lane & 3;
  const __amdgpu_buffer_rsrc_t rC = mk_rsrc(CF, NH * SEQ * HD * 2);
  const __amdgpu_buffer_rsrc_t rW = mk_rsrc(wo, NH * HD * DM * 2);
  const unsigned vlin = lane * 16;
  unsigned voffW[3];
#pragma unroll
  for (int i = 0; i < 3; ++i) {
    const int pc = wid + 8 * i;
    const int u = 2 * pc + (lane >> 5);
    const int kb = u / 5, nb = u % 5;
    voffW[i] = (unsigned)((kb * 8 + ((lane >> 2) & 7)) * 1280 + (nb * 32 + (lane & 3) * 8) * 2);
  }
  const bool lo = wid < 4;
  const unsigned sW0 = (unsigned)(ks * 1024 * 1280 + nt * 320);
  const unsigned sC0 = (unsigned)(((panel * 4 + tbi) * 256 + ks * 64) * 1024);
  const __amdgpu_buffer_rsrc_t r2 = lo ? rW : rC;
  const unsigned v2 = lo ? voffW[2] : vlin;
  const unsigned s2 = lo ? sW0 : sC0, st2 = lo ? 81920u : 4096u, l2 = lo ? (wid + 16) * 1024 : 20480 + tbi * 4096;
  const unsigned s3 = sC0 + (lo ? 3072u : 1024u), l3 = 20480 + tbi * 4096 + (lo ? 3072 : 1024);
  const unsigned s4 = lo ? 0x7ff00000u : sC0 + 2048u, l4 = lo ? 36864 : 20480 + tbi * 4096 + 2048;
  auto piece = [&](int j, int slot, int i) __attribute__((always_inline)) {
    const unsigned sb = lds0 + slot * K3_STAGE;
    if (i == 0) dma1(rW, voffW[0], sW0 + j * 81920, sb + wid * 1024);
    else if (i == 1) dma1(rW, voffW[1], sW0 + j * 81920, sb + (wid + 8) * 1024);
    else if (i == 2) dma1(r2, v2, s2 + j * st2, sb + l2);
    else if (i == 3) dma1(rC, vlin, s3 + j * 4096, sb + l3);
    else dma1(rC, vlin, s4 + j * 4096, sb + l4);
  };
  f32x16 acc[5];
#pragma unroll
  for (int n = 0; n < 5; ++n) acc[n] = f32x16{};
  const unsigned wbase = h * 256 + q4 * 64 + g1 * 32 + p4 * 8 + kp * 4 * 2560;
  const unsigned cbase = 20480 + tbi * 4096 + lane * 16 + kp * 2048;
  auto rd = [&](bf16x8& cf, bf16x8 (&w)[5], LAS const char* sb, int si) {
    cf = lds_b128(sb + cbase + si * 1024);
#pragma unroll
    for (int n = 0; n < 5; ++n)
      w[n] = lds_tr2(sb + wbase + (2 * si) * 2560 + n * 512, sb + wbase + (2 * si + 1) * 2560 + n * 512);
  };
  bf16x8 cA, cB, wA[5], wB[5];
  STAMP_DECL();
  STAMP();
#pragma unroll
  for (int j = 0; j < 3; ++j)
#pragma unroll
    for (int i = 0; i < 5; ++i) piece(j, j, i);
  STAMP();
  auto stage = [&](const int j, const int slot, const bool more, const bool first) __attribute__((always_inline)) {
    STAMP();
    LAS const char* sb = lds + slot * K3_STAGE;
    rd(cA, wA, sb, 0);
    SBAR();
    if (!first) {
      acc[0] = MFMA32(wB[0], cB, acc[0]); acc[1] = MFMA32(wB[1], cB, acc[1]);
      if (more) piece(j + 3, (slot + 3) & 3, 0);
      SBAR();
      acc[2] = MFMA32(wB[2], cB, acc[2]); acc[3] = MFMA32(wB[3], cB, acc[3]);
      if (more) piece(j + 3, (slot + 3) & 3, 1);
      SBAR();
      acc[4] = MFMA32(wB[4], cB, acc[4]);
    } else {
      piece(3, 3, 0); piece(3, 3, 1);
    }
    SBAR();
    rd(cB, wB, sb, 1);
    SBAR();
    acc[0] = MFMA32(wA[0], cA, acc[0]);
    if (more) piece(j + 3, (slot + 3) & 3, 2);
    SBAR();
    acc[1] = MFMA32(wA[1], cA, acc[1]); acc[2] = MFMA32(wA[2], cA, acc[2]);
    if (more) piece(j + 3, (slot + 3) & 3, 3);
    SBAR();
    acc[3] = MFMA32(wA[3], cA, acc[3]); acc[4] = MFMA32(wA[4], cA, acc[4]);
    if (more) piece(j + 3, (slot + 3) & 3, 4);
    SBAR();
  };
  WAIT_VL_BAR(10); stage(0, 0, true, true);
  for (int j4 = 1; j4 < 13; j4 += 4) {
    WAIT_VL_BAR(10); stage(j4, 1, true, false);
    WAIT_VL_BAR(10); stage(j4 + 1, 2, true, false);
    WAIT_VL_BAR(10); stage(j4 + 2, 3, true, false);
    WAIT_VL_BAR(10); stage(j4 + 3, 0, true, false);
  }
  WAIT_VL_BAR(10); stage(13, 1, false, false);
  WAIT_VL_BAR(5); stage(14, 2, false, false);
  WAIT_VL_BAR(0); stage(15, 3, false, false);
#pragma unroll
  for (int n = 0; n < 5; ++n) acc[n] = MFMA32(wB[n], cB, acc[n]);
  STAMP();
  WAIT_L_BAR();
  {
    LAS char* mine = lds + (tbi * 2 + kp) * 12288 + lane * 16;
    LAS const char* theirs = lds + (tbi * 2 + (kp ^ 1)) * 12288 + lane * 16;
    auto put = [&](const f32x16& a, int slot) {
#pragma unroll
      for (int gq = 0; gq < 4; ++gq)
        *(LAS f32x4*)(mine + (slot * 4 + gq) * 1024) = (f32x4){a[4 * gq], a[4 * gq + 1], a[4 * gq + 2], a[4 * gq + 3]};
    };
    auto fin = [&](const f32x16& a, int slot, int n) {
      f32x16 t;
#pragma unroll
      for (int gq = 0; gq < 4; ++gq) {
        const f32x4 v = *(LAS const f32x4*)(theirs + (slot * 4 + gq) * 1024);
#pragma unroll
        for (int e = 0; e < 4; ++e) t[4 * gq + e] = a[4 * gq + e] + v[e];
      }
      u16* dst = PT + ((size_t)((((ks * 64 + panel * 4 + tbi) * 20 + nt * 5 + n) * 2) * 64 + lane)) * 8;
      *(u32x4*)dst = pack8(t, 0);
      *(u32x4*)(dst + 512) = pack8(t, 8);
    };
    if (kp == 0) { put(acc[3], 0); put(acc[4], 1); } else { put(acc[0], 0); put(acc[1], 1); put(acc[2], 2); }
    WAIT_L_BAR();
    if (kp == 0) { fin(acc[0], 0, 0); fin(acc[1], 1, 1); fin(acc[2], 2, 2); } else { fin(acc[3], 0, 3); fin(acc[4], 1, 4); }
  }
  STAMP();
  STAMP();
  STAMP_END(2, DBGP(PT, WS_PT));
}

__global__ __launch_bounds__(320) void k4_ln(const u16* __restrict__ x, const u16* __restrict__ PT,
                                             float* __restrict__ H, u16* __restrict__ HN, const u16* __restrict__ w1) {
  __shared__ __attribute__((aligned(16))) u16 tile[8 * 648];
  __shared__ float red[2][5][8];
  __shared__ __attribute__((aligned(16))) char dead[5 * 256];
  const int tid = threadIdx.x, lane = tid & 63, wid = RFL(tid >> 6);
  const int b = blockIdx.x;
  const int r8 = lane & 7, hh = (lane >> 3) & 1, sg = (lane >> 4) & 1, ml = lane >> 5;
  const int token = b * 8 + r8, tb32 = token >> 5, rr = token & 31;
  STAMP_DECL();
  STAMP();
  u32x2 xv[2][2];
  u32x4 pv[2][4];
#pragma unroll
  for (int i = 0; i < 2; ++i) {
    const int mb = wid * 4 + 2 * i + ml;
    const int m0 = mb * 32 + sg * 16 + hh * 4;
    xv[i][0] = *(const u32x2*)(x + (size_t)token * DM + m0);
    xv[i][1] = *(const u32x2*)(x + (size_t)token * DM + m0 + 8);
#pragma unroll
    for (int ks = 0; ks < 4; ++ks)
      pv[i][ks] = *(const u32x4*)(PT + ((size_t)((((ks * 64 + tb32) * 20 + mb) * 2 + sg) * 64 + hh * 32 + rr)) * 8);
  }
  SBAR();
  STAMP();
  float v[2][8];
  float s = 0.f;
#pragma unroll
  for (int i = 0; i < 2; ++i) {
    const int m0 = (wid * 4 + 2 * i + ml) * 32 + sg * 16 + hh * 4;
#pragma unroll
    for (int q = 0; q < 4; ++q) {
      const unsigned xw = xv[i][q >> 1][q & 1];
      v[i][2 * q] = bflo(xw) + ((bflo(pv[i][0][q]) + bflo(pv[i][1][q])) + (bflo(pv[i][2][q]) + bflo(pv[i][3][q])));
      v[i][2 * q + 1] = bfhi(xw) + ((bfhi(pv[i][0][q]) + bfhi(pv[i][1][q])) + (bfhi(pv[i][2][q]) + bfhi(pv[i][3][q])));
    }
    *(f32x4*)(H + (size_t)token * DM + m0) = (f32x4){v[i][0], v[i][1], v[i][2], v[i][3]};
    *(f32x4*)(H + (size_t)token * DM + m0 + 8) = (f32x4){v[i][4], v[i][5], v[i][6], v[i][7]};
#pragma unroll
    for (int e = 0; e < 8; ++e) s += v[i][e];
  }
  STAMP();
  {
    const int u = (b >> 3) * 5 + wid;
    const int a4 = (b & 7) >> 1;
    const unsigned dd = (unsigned)(uintptr_t)(LAS char*)dead + wid * 256;
    const __amdgpu_buffer_rsrc_t r1 = mk_rsrc(w1, SEQ * FF * 2);
#pragma unroll
    for (int i = 0; i < 2; ++i) {
      const int line = (u + 160 * i) * 64 + lane;
      const unsigned off = line < 14336 ? (unsigned)((line / 7) * (FF * 2) + a4 * 896 + (line % 7) * 128) : 0x7ff00000u;
      warm8k(r1, off, 0u, dd);
    }
  }
  s += __shfl_xor(s, 8); s += __shfl_xor(s, 16); s += __shfl_xor(s, 32);
  if (lane < 8) red[0][wid][lane] = s;
  WAIT_L_BAR();
  STAMP();
  const float mean = ((red[0][0][r8] + red[0][1][r8]) + (red[0][2][r8] + red[0][3][r8]) + red[0][4][r8]) * (1.0f / DM);
  float sq = 0.f;
#pragma unroll
  for (int i = 0; i < 2; ++i)
#pragma unroll
    for (int e = 0; e < 8; ++e) { v[i][e] -= mean; sq += v[i][e] * v[i][e]; }
  sq += __shfl_xor(sq, 8); sq += __shfl_xor(sq, 16); sq += __shfl_xor(sq, 32);
  if (lane < 8) red[1][wid][lane] = sq;
  WAIT_L_BAR();
  const float var = ((red[1][0][r8] + red[1][1][r8]) + (red[1][2][r8] + red[1][3][r8]) + red[1][4][r8]) * (1.0f / DM);
  const float r1 = 1.0f / sqrtf(var + LN_EPS);
  const float r2 = 1.0f / sqrtf(var * r1 * r1 + LN_EPS);
  const float sc = r1 * r2;
#pragma unroll
  for (int i = 0; i < 2; ++i) {
    const int m0 = (wid * 4 + 2 * i + ml) * 32 + sg * 16 + hh * 4;
    *(u32x2*)(tile + r8 * 648 + m0) = (u32x2){cvtpk(v[i][0] * sc, v[i][1] * sc), cvtpk(v[i][2] * sc, v[i][3] * sc)};
    *(u32x2*)(tile + r8 * 648 + m0 + 8) = (u32x2){cvtpk(v[i][4] * sc, v[i][5] * sc), cvtpk(v[i][6] * sc, v[i][7] * sc)};
  }
  WAIT_L_BAR();
  STAMP();
  const int ks32 = b >> 2, g = b & 3;
#pragma unroll
  for (int i = 0; i < 2; ++i) {
    const int m = tid + 320 * i;
    u16 e[8];
#pragma unroll
    for (int j = 0; j < 8; ++j) e[j] = tile[j * 648 + m];
    u32x4 w = {(unsigned)e[0] | ((unsigned)e[1] << 16), (unsigned)e[2] | ((unsigned)e[3] << 16),
               (unsigned)e[4] | ((unsigned)e[5] << 16), (unsigned)e[6] | ((unsigned)e[7] << 16)};
    *(u32x4*)(HN + ((size_t)(((m >> 4) * 64 + ks32) * 64 + g * 16 + (m & 15))) * 8) = w;
  }
  STAMP();
  STAMP();
  STAMP_END(3, DBGP(HN, WS_HN));
}

constexpr int K56_LDS = 163840;

DI float gelu_tanh(float x) {
  const float u = 0.7978845608028654f * (x + 0.044715f * x * x * x);
  const float e = __builtin_amdgcn_exp2f(u * 2.8853900817779268f);
  const float t = 1.0f - 2.0f * __builtin_amdgcn_rcpf(e + 1.0f);
  return 0.5f * x * (1.0f + t);
}

#define ACLOB56 "a0","a1","a2","a3","a4","a5","a6","a7","a8","a9","a10","a11","a12","a13","a14","a15","a16","a17","a18","a19","a20","a21","a22","a23","a24","a25","a26","a27","a28","a29","a30","a31","a32","a33","a34","a35","a36","a37","a38","a39","a40","a41","a42","a43","a44","a45","a46","a47","a48","a49","a50","a51","a52","a53","a54","a55","a56","a57","a58","a59","a60","a61","a62","a63","a64","a65","a66","a67","a68","a69","a70","a71","a72","a73","a74","a75","a76","a77","a78","a79","a80","a81","a82","a83","a84","a85","a86","a87","a88","a89","a90","a91"
template <int R> DI void m_aload(__amdgpu_buffer_rsrc_t rsrc, unsigned voff, unsigned soff) {
  asm volatile("buffer_load_dwordx4 a[%c3:%c4], %0, %1, %2 offen" ::"v"(voff), "s"(rsrc), "s"(soff), "i"(R), "i"(R + 3) : "memory");
}
template <int R, int OFF> DI void m_trread(unsigned addr) {
  asm volatile("ds_read_b64_tr_b16 a[%c1:%c2], %0 offset:%c3" ::"v"(addr), "i"(R), "i"(R + 1), "i"(OFF) : "memory");
}
template <int RA, int RB> DI void m_mfma(f32x4& d) {
  asm volatile("v_mfma_f32_16x16x32_bf16 %0, a[%c1:%c2], a[%c3:%c4], %0" : "+v"(d) : "i"(RA), "i"(RA + 3), "i"(RB), "i"(RB + 3));
}

template <int MODE>
__global__ __launch_bounds__(512, 2) void k56_mix(const u16* __restrict__ W, const u16* __restrict__ AF,
                                                  const u16* __restrict__ bias, const float* __restrict__ H,
                                                  u16* __restrict__ outp, const u16* __restrict__ wnext) {
  constexpr int LDW = MODE == 0 ? FF : SEQ;
  constexpr int NKS = MODE == 0 ? 64 : 54;
  constexpr int NK = MODE == 0 ? 8 : 7;
  constexpr int KROWS = MODE == 0 ? SEQ : FF;
  extern __shared__ __attribute__((aligned(16))) char smem[];
  LAS char* lds = (LAS char*)smem;
  const unsigned lds0 = (unsigned)(uintptr_t)lds;
  const int tid = threadIdx.x, lane = tid & 63;
  const int wid = RFL(tid >> 6);
  const int b = blockIdx.x, xcd = b & 7, idx = b >> 3;
  int wt, dt;
  if (MODE == 0) {
    wt = (xcd >> 1) * 7 + (idx >> 2); dt = (xcd & 1) * 4 + (idx & 3);
    if (idx >= 28 || wt >= 27) {
      if (idx >= 28) {
        const __amdgpu_buffer_rsrc_t r2 = mk_rsrc(wnext, SEQ * FF * 2);
#pragma unroll
        for (int i = 0; i < 7; ++i) {
          const int line = (((idx - 28) * 8 + wid) * 7 + i) * 64 + lane;
          const unsigned off = line < 13824 ? (unsigned)((line >> 3) * 4096 + (xcd >> 1) * 1024 + (line & 7) * 128) : 0x7ff00000u;
          warm8k(r2, off, 0u, lds0 + wid * 256);
        }
      }
      return;
    }
  } else {
    wt = (xcd >> 1) * 8 + (idx >> 2); dt = (xcd & 1) * 4 + (idx & 3);
  }
  const __amdgpu_buffer_rsrc_t rW = mk_rsrc(W, KROWS * LDW * 2);
  const __amdgpu_buffer_rsrc_t rA = mk_rsrc(AF, 40 * NKS * 1024);
  const unsigned vlin = lane * 16;
  const int prow = (lane >> 2) & 7;
  const unsigned voffW0 = (unsigned)(prow * LDW * 2 + (lane >> 5) * 64 +
                                     (((lane & 3) ^ (MODE == 1 ? (((prow >> 2) & 1) << 1) : 0)) << 4));
  const unsigned voffW1 = MODE == 1 ? voffW0 : (voffW0 ^ 32u);
  const unsigned myl = lds0 + wid * 20480;
  const int g = lane >> 4, q4 = (lane & 15) >> 2, p4 = lane & 3;
  unsigned wb[2][2];
#pragma unroll
  for (int t = 0; t < 2; ++t)
#pragma unroll
    for (int hf = 0; hf < 2; ++hf) {
      const int kb = MODE == 0 ? g : 2 * t + (g >> 1);
      const int row = MODE == 0 ? 4 * t + q4 : 4 * (g & 1) + q4;
      const int sw = MODE == 0 ? (kb & 1) : ((row >> 2) & 1);
      wb[t][hf] = myl + kb * 1024 + row * 64 + ((hf ^ sw) * 32) + p4 * 8;
    }
  f32x4 acc[4][5];
#pragma unroll
  for (int i = 0; i < 4; ++i)
#pragma unroll
    for (int d = 0; d < 5; ++d) acc[i][d] = (f32x4){0.f, 0.f, 0.f, 0.f};
  STAMP_DECL();
  STAMP();
  u32x2 eb0[2], eb1[2];
  f32x4 eh[3]; u16 ebs[3];
  if (MODE == 0) {
#pragma unroll
    for (int i = 0; i < 2; ++i) {
      const int u = wid + 8 * i < 10 ? wid + 8 * i : wid;
      const int f0 = wt * 64 + (u / 5) * 32 + 4 * g;
      eb0[i] = *(const u32x2*)(bias + f0); eb1[i] = *(const u32x2*)(bias + f0 + 16);
    }
  } else {
#pragma unroll
    for (int i = 0; i < 3; ++i) {
      const int u = wid + 8 * i < 20 ? wid + 8 * i : wid;
      const int s = wt * 64 + (u / 5) * 16 + (lane & 15), d0 = dt * 80 + (u % 5) * 16 + 4 * g;
      ebs[i] = bias[s];
      eh[i] = *(const f32x4*)(H + (size_t)s * DM + d0);
    }
  }
  SBAR();
#define WDMA(IT, KB) dma1(rW, ((KB) & 1) ? voffW1 : voffW0, (unsigned)(((wid + 8 * (IT)) * 32 + (KB) * 8) * LDW * 2 + wt * 128), myl + ((IT) % 5) * 4096 + (KB) * 1024)
#define ALOAD(IT, D) m_aload<((IT) % 3) * 20 + 4 * (D)>(rA, vlin, (unsigned)(((dt * 5 + (D)) * NKS + wid + 8 * (IT)) * 1024))
#define WDMA4(IT) do { WDMA(IT, 0); WDMA(IT, 1); WDMA(IT, 2); WDMA(IT, 3); } while (0)
#define ALOAD5(IT) do { ALOAD(IT, 0); ALOAD(IT, 1); ALOAD(IT, 2); ALOAD(IT, 3); ALOAD(IT, 4); } while (0)
#define WREAD1(IT, I) do { m_trread<60 + 16 * ((IT) & 1) + 4 * (I), ((IT) % 5) * 4096 + ((I) >> 1) * 512>(wb[0][(I) & 1]); \
                           m_trread<60 + 16 * ((IT) & 1) + 4 * (I) + 2, ((IT) % 5) * 4096 + ((I) >> 1) * 512>(wb[1][(I) & 1]); } while (0)
#define WREAD(IT) do { WREAD1(IT, 0); WREAD1(IT, 1); WREAD1(IT, 2); WREAD1(IT, 3); } while (0)
#define MM(IT, I, D) do { if constexpr (MODE == 0) m_mfma<60 + 16 * ((IT) & 1) + 4 * (I), ((IT) % 3) * 20 + 4 * (D)>(acc[I][D]); \
                          else m_mfma<((IT) % 3) * 20 + 4 * (D), 60 + 16 * ((IT) & 1) + 4 * (I)>(acc[I][D]); } while (0)
#define MMROW(IT, I) do { MM(IT, I, 0); MM(IT, I, 1); MM(IT, I, 2); MM(IT, I, 3); MM(IT, I, 4); } while (0)
#define NV(IT) (((IT) + 4 < NK ? 4 : 0) + ((IT) + 2 < NK ? 5 : 0))
#define ALLOW(IT) ((IT) == 0 ? (5 + (NK > 2 ? 4 : 0) + (NK > 3 ? 4 : 0)) : (IT) == 1 ? ((NK > 3 ? 4 : 0) + NV(0)) : NV((IT) - 1))
#define STEP(IT) do { if constexpr ((IT) < NK) { \
    asm volatile("s_waitcnt vmcnt(%c0) lgkmcnt(0)" ::"i"(ALLOW(IT)) : "memory", ACLOB56); \
    if constexpr ((IT) + 1 < NK) WREAD((IT) + 1); \
    SBAR(); \
    MMROW(IT, 0); if constexpr ((IT) + 4 < NK) { WDMA((IT) + 4, 0); WDMA((IT) + 4, 1); } SBAR(); \
    MMROW(IT, 1); if constexpr ((IT) + 4 < NK) { WDMA((IT) + 4, 2); WDMA((IT) + 4, 3); } SBAR(); \
    MMROW(IT, 2); if constexpr ((IT) + 2 < NK) { ALOAD((IT) + 2, 0); ALOAD((IT) + 2, 1); ALOAD((IT) + 2, 2); } SBAR(); \
    MMROW(IT, 3); if constexpr ((IT) + 2 < NK) { ALOAD((IT) + 2, 3); ALOAD((IT) + 2, 4); } SBAR(); \
    } } while (0)
  asm volatile("" ::: "memory", ACLOB56);
  WDMA4(0); ALOAD5(0); asm volatile("s_barrier" ::: "memory");
  WDMA4(1); ALOAD5(1); asm volatile("s_barrier" ::: "memory");
  WDMA4(2); WDMA4(3);
  asm volatile("s_waitcnt vmcnt(22)" ::: "memory");
  WREAD(0);
  STAMP();
  STEP(0); STEP(1); STEP(2); STEP(3); STEP(4); STEP(5); STEP(6); STEP(7);
  asm volatile("s_nop 15" ::: "memory");
  STAMP();
#undef WDMA
#undef ALOAD
#undef WDMA4
#undef ALOAD5
#undef WREAD1
#undef WREAD
#undef MM
#undef MMROW
#undef NV
#undef ALLOW
#undef STEP
  WAIT_L0();
  STAMP();
  {
    LAS char* dst = lds + wid * 20480 + lane * 16;
#pragma unroll
    for (int i = 0; i < 4; ++i)
#pragma unroll
      for (int d = 0; d < 5; ++d) *(LAS f32x4*)(dst + (i * 5 + d) * 1024) = acc[i][d];
  }
  WAIT_L_BAR();
  if (MODE == 0) {
#pragma unroll
    for (int i = 0; i < 2; ++i) {
      const int u = wid + 8 * i;
      if (u < 10) {
        const int a = u / 5, d = u % 5;
        f32x4 t0 = {0.f, 0.f, 0.f, 0.f}, t1 = {0.f, 0.f, 0.f, 0.f};
#pragma unroll
        for (int w = 0; w < 8; ++w) {
          t0 += *(LAS const f32x4*)(lds + w * 20480 + ((2 * a) * 5 + d) * 1024 + lane * 16);
          t1 += *(LAS const f32x4*)(lds + w * 20480 + ((2 * a + 1) * 5 + d) * 1024 + lane * 16);
        }
        const u32x2 b0 = eb0[i], b1 = eb1[i];
        t0 += (f32x4){bflo(b0[0]), bfhi(b0[0]), bflo(b0[1]), bfhi(b0[1])};
        t1 += (f32x4){bflo(b1[0]), bfhi(b1[0]), bflo(b1[1]), bfhi(b1[1])};
        u32x4 w = {cvtpk(gelu_tanh(t0[0]), gelu_tanh(t0[1])), cvtpk(gelu_tanh(t0[2]), gelu_tanh(t0[3])),
                   cvtpk(gelu_tanh(t1[0]), gelu_tanh(t1[1])), cvtpk(gelu_tanh(t1[2]), gelu_tanh(t1[3]))};
        *(u32x4*)(outp + ((size_t)(((dt * 5 + d) * 54 + wt * 2 + a) * 64 + lane)) * 8) = w;
      }
    }
  } else {
#pragma unroll
    for (int i = 0; i < 3; ++i) {
      const int u = wid + 8 * i;
      if (u < 20) {
        f32x4 t0 = {0.f, 0.f, 0.f, 0.f};
#pragma unroll
        for (int w = 0; w < 8; ++w) t0 += *(LAS const f32x4*)(lds + w * 20480 + u * 1024 + lane * 16);
        const int s = wt * 64 + (u / 5) * 16 + (lane & 15);
        const int d0 = dt * 80 + (u % 5) * 16 + 4 * g;
        const float bb = bf2f(ebs[i]);
        const f32x4 hv = eh[i];
        u32x2 w = {cvtpk(t0[0] + bb + hv[0], t0[1] + bb + hv[1]), cvtpk(t0[2] + bb + hv[2], t0[3] + bb + hv[3])};
        *(u32x2*)(outp + (size_t)s * DM + d0) = w;
      }
    }
  }
  STAMP();
  STAMP_END(4 + MODE, DBGP(AF, MODE == 0 ? WS_HN : WS_UF));
}

}

extern "C" void kernel_launch(void* const* d_in, const int* in_sizes, int n_in, void* d_out, int out_size, void* d_ws,
                              size_t ws_size, hipStream_t stream) {
  (void)in_sizes; (void)n_in; (void)out_size; (void)ws_size;
  const u16* x = (const u16*)d_in[0];
  const u16* ckv = (const u16*)d_in[1];
  const u16* wq = (const u16*)d_in[2];
  const u16* wuk = (const u16*)d_in[3];
  const u16* wuv = (const u16*)d_in[4];
  const u16* wo = (const u16*)d_in[5];
  const u16* w1 = (const u16*)d_in[7];
  const u16* b1 = (const u16*)d_in[8];
  const u16* w2 = (const u16*)d_in[9];
  const u16* b2 = (const u16*)d_in[10];
  char* ws = (char*)d_ws;
  u16* XN = (u16*)(ws + WS_XN);
  u16* KF = (u16*)(ws + WS_KF);
  u16* VF = (u16*)(ws + WS_VF);
  u16* CF = (u16*)(ws + WS_CF);
  u16* PT = (u16*)(ws + WS_PT);
  float* H = (float*)(ws + WS_H);
  u16* HN = (u16*)(ws + WS_HN);
  u16* UF = (u16*)(ws + WS_UF);
  (void)hipFuncSetAttribute((const void*)k1_kvproj, hipFuncAttributeMaxDynamicSharedMemorySize, K1_LDS);
  (void)hipFuncSetAttribute((const void*)k2_attn, hipFuncAttributeMaxDynamicSharedMemorySize, K2_LDS);
  (void)hipFuncSetAttribute((const void*)k3_oproj, hipFuncAttributeMaxDynamicSharedMemorySize, K3_LDS);
  (void)hipFuncSetAttribute((const void*)k56_mix<0>, hipFuncAttributeMaxDynamicSharedMemorySize, K56_LDS);
  (void)hipFuncSetAttribute((const void*)k56_mix<1>, hipFuncAttributeMaxDynamicSharedMemorySize, K56_LDS);
  k1_kvproj<<<256, 512, K1_LDS, stream>>>(x, ckv, wuk, wuv, KF, VF, XN, wq);
  k2_attn<<<256, 512, K2_LDS, stream>>>(XN, wq, KF, VF, CF, (float*)(ws + WS_LAM), wo);
  k3_oproj<<<256, 512, K3_LDS, stream>>>(CF, wo, PT);
  k4_ln<<<256, 320, 0, stream>>>(x, PT, H, HN, w1);
  k56_mix<0><<<256, 512, K56_LDS, stream>>>(w1, HN, b1, nullptr, UF, w2);
  k56_mix<1><<<256, 512, K56_LDS, stream>>>(w2, UF, b2, H, (u16*)d_out, nullptr);
}
```

```cpp
#include <hip/hip_runtime.h>
#include <stdint.h>
#include <utility>

namespace {

typedef unsigned short u16;
typedef __bf16 bf16x8 __attribute__((ext_vector_type(8)));
typedef __bf16 bf16x2 __attribute__((ext_vector_type(2)));
typedef short s16x4 __attribute__((ext_vector_type(4)));
typedef short s16x8 __attribute__((ext_vector_type(8)));
typedef float f32x2 __attribute__((ext_vector_type(2)));
typedef float f32x4 __attribute__((ext_vector_type(4)));
typedef float f32x16 __attribute__((ext_vector_type(16)));
typedef unsigned u32x2 __attribute__((ext_vector_type(2)));
typedef unsigned u32x4 __attribute__((ext_vector_type(4)));
#define LAS __attribute__((address_space(3)))
#define DI __device__ __forceinline__
#define SBAR() __builtin_amdgcn_sched_barrier(0)
#define RFL(x) __builtin_amdgcn_readfirstlane(x)

constexpr int SEQ = 2048, DM = 640, DC = 512, NH = 32, HD = 128, FF = 1728;
constexpr float LN_EPS = 1e-5f;
constexpr float QSCALE = 0.08838834764831845f * 1.4426950408889634f;
constexpr float P_LIMIT = 1073741824.0f;
constexpr float NEG_BIG = -1e30f;

constexpr size_t WS_XN = 0;
constexpr size_t WS_KF = WS_XN + (size_t)SEQ * DM * 2;
constexpr size_t WS_VF = WS_KF + (size_t)NH * SEQ * HD * 2;
constexpr size_t WS_CF = WS_VF + (size_t)NH * SEQ * HD * 2;
constexpr size_t WS_PT = WS_CF + (size_t)NH * SEQ * HD * 2;
constexpr size_t WS_H = WS_PT + (size_t)4 * SEQ * DM * 2;
constexpr size_t WS_HN = WS_H + (size_t)SEQ * DM * 4;
constexpr size_t WS_UF = WS_HN + (size_t)SEQ * DM * 2;
constexpr size_t WS_LAM = WS_UF + (size_t)DM * FF * 2;
constexpr size_t WS_END = WS_LAM + 256 * 256 * 4;

DI __amdgpu_buffer_rsrc_t mk_rsrc(const void* p, unsigned bytes) {
  return __builtin_amdgcn_make_buffer_rsrc((void*)p, 0, bytes, 0x00020000);
}
DI void dma1(__amdgpu_buffer_rsrc_t rsrc, unsigned voff, unsigned soff, unsigned ldsaddr) {
  asm volatile("s_mov_b32 m0, %0\n\ts_nop 0\n\tbuffer_load_dwordx4 %1, %2, %3 offen lds"
               ::"s"(ldsaddr), "v"(voff), "s"(rsrc), "s"(soff) : "memory", "m0");
}
DI void warm8k(__amdgpu_buffer_rsrc_t rsrc, unsigned voff128, unsigned soff, unsigned ldsaddr) {
  asm volatile("s_mov_b32 m0, %0\n\ts_nop 0\n\tbuffer_load_dword %1, %2, %3 offen lds"
               ::"s"(ldsaddr), "v"(voff128), "s"(rsrc), "s"(soff) : "memory", "m0");
}
#define WAIT_VL_BAR(N) asm volatile("s_waitcnt vmcnt(" #N ") lgkmcnt(0)\n\ts_barrier" ::: "memory")
#define WAIT_L_BAR() asm volatile("s_waitcnt lgkmcnt(0)\n\ts_barrier" ::: "memory")
#define WAIT_L0() asm volatile("s_waitcnt lgkmcnt(0)" ::: "memory")

DI bf16x8 lds_b128(LAS const char* p) { return *(LAS const bf16x8*)p; }
DI bf16x8 lds_tr2(LAS const char* p0, LAS const char* p1) {
  s16x4 lo = __builtin_amdgcn_ds_read_tr16_b64_v4i16((LAS s16x4*)p0);
  s16x4 hi = __builtin_amdgcn_ds_read_tr16_b64_v4i16((LAS s16x4*)p1);
  s16x8 w = __builtin_shufflevector(lo, hi, 0, 1, 2, 3, 4, 5, 6, 7);
  return __builtin_bit_cast(bf16x8, w);
}
DI unsigned cvtpk(float lo, float hi) {
  f32x2 v = {lo, hi};
  bf16x2 b = __builtin_convertvector(v, bf16x2);
  return __builtin_bit_cast(unsigned, b);
}
DI u32x4 pack8(const f32x16& a, int o) {
  u32x4 w = {cvtpk(a[o], a[o + 1]), cvtpk(a[o + 2], a[o + 3]), cvtpk(a[o + 4], a[o + 5]), cvtpk(a[o + 6], a[o + 7])};
  return w;
}
DI float bf2f(unsigned short b) { return __uint_as_float(((unsigned)b) << 16); }
DI float bflo(unsigned w) { return __uint_as_float(w << 16); }
DI float bfhi(unsigned w) { return __uint_as_float(w & 0xffff0000u); }
#define MFMA32(a, b, c) __builtin_amdgcn_mfma_f32_32x32x16_bf16((a), (b), (c), 0, 0, 0)
#define MFMA16(a, b, c) __builtin_amdgcn_mfma_f32_16x16x32_bf16((a), (b), (c), 0, 0, 0)
#define STAMP_DECL()
#define STAMP()
#define STAMP_END(kid, dbg)
DI float swap_max(float v) {
  auto rr = __builtin_amdgcn_permlane32_swap(__float_as_uint(v), __float_as_uint(v), false, false);
  return fmaxf(__uint_as_float(rr[0]), __uint_as_float(rr[1]));
}
DI float swap_add(float v) {
  auto rr = __builtin_amdgcn_permlane32_swap(__float_as_uint(v), __float_as_uint(v), false, false);
  return __uint_as_float(rr[0]) + __uint_as_float(rr[1]);
}

constexpr int K1_LDS = 4 * 32768;

template <bool ISV>
DI void k1_body(LAS char* lds, unsigned lds0, int wid, int lane, int head, int tb, const u16* __restrict__ ckv,
                const u16* __restrict__ wsrc, const u16* __restrict__ x, u16* __restrict__ KF, u16* __restrict__ VF,
                u16* __restrict__ XN, const u16* __restrict__ wq
                ) {
  const int wm = wid >> 2, wn = wid & 3;
  const int r = lane & 31, h = lane >> 5;
  const int g1 = (lane >> 4) & 1, q = (lane & 15) >> 2, p = lane & 3;
  const __amdgpu_buffer_rsrc_t rA = mk_rsrc(ckv, SEQ * DC * 2);
  const __amdgpu_buffer_rsrc_t rWm = mk_rsrc(wsrc, DC * NH * HD * 2);
  const unsigned voffA = (lane >> 2) * 1024 + (((lane & 3) ^ ((lane >> 4) & 3)) << 4);
  const unsigned voffW = ((lane >> 2) & 7) * 8192 + (lane >> 5) * 64 + (lane & 3) * 16;
  auto piece = [&](int kt, int slot, int i) __attribute__((always_inline)) {
    const unsigned sb = lds0 + slot * 32768;
    if (i < 2) dma1(rA, voffA, (unsigned)((tb * 256 + (wid * 2 + i) * 16) * 1024 + kt * 64), sb + (wid * 2 + i) * 1024);
    else dma1(rWm, voffW, (unsigned)((kt * 32 + (wid >> 1) * 8) * 8192 + head * 256 + (i - 2) * 128),
              sb + 16384 + ((wid >> 1) * 4 + (wid & 1) * 2 + (i - 2)) * 1024);
  };
  unsigned abase[2];
#pragma unroll
  for (int s = 0; s < 2; ++s) abase[s] = wm * 8192 + r * 64 + (((2 * s + h) ^ ((r >> 2) & 3)) << 4);
  const unsigned wbase = 16384 + h * 4096 + wn * 1024 + q * 64 + g1 * 32 + p * 8;

  f32x16 acc[4][2];
#pragma unroll
  for (int i = 0; i < 4; ++i)
#pragma unroll
    for (int n = 0; n < 2; ++n) acc[i][n] = f32x16{};
  auto rd = [&](bf16x8 (&a)[4], bf16x8 (&w)[2], LAS const char* sb, int s) {
#pragma unroll
    for (int n = 0; n < 2; ++n) w[n] = lds_tr2(sb + wbase + s * 8192 + n * 512, sb + wbase + s * 8192 + n * 512 + 256);
#pragma unroll
    for (int i = 0; i < 4; ++i) a[i] = lds_b128(sb + abase[s] + i * 2048);
  };
  auto mm2 = [&](const bf16x8 (&a)[4], const bf16x8 (&w)[2], int i) {
#pragma unroll
    for (int n = 0; n < 2; ++n) {
      if (ISV) acc[i][n] = MFMA32(a[i], w[n], acc[i][n]);
      else acc[i][n] = MFMA32(w[n], a[i], acc[i][n]);
    }
  };
  bf16x8 aA[4], wA[2], aB[4], wB[2];
  STAMP_DECL();
  STAMP();
#pragma unroll
  for (int kt = 0; kt < 3; ++kt)
#pragma unroll
    for (int i = 0; i < 4; ++i) piece(kt, kt, i);
  auto stage = [&](const int kt, const int slot, const bool more, const bool first) __attribute__((always_inline)) {
    STAMP();
    LAS const char* sb = lds + slot * 32768;
    rd(aA, wA, sb, 0);
    SBAR();
    if (!first) {
      mm2(aB, wB, 0); mm2(aB, wB, 1);
      if (more) piece(kt + 3, (slot + 3) & 3, 0);
      SBAR();
      mm2(aB, wB, 2); mm2(aB, wB, 3);
      if (more) piece(kt + 3, (slot + 3) & 3, 1);
    } else {
      piece(3, 3, 0); piece(3, 3, 1);
    }
    SBAR();
    rd(aB, wB, sb, 1);
    SBAR();
    mm2(aA, wA, 0); mm2(aA, wA, 1);
    if (more) piece(kt + 3, (slot + 3) & 3, 2);
    SBAR();
    mm2(aA, wA, 2); mm2(aA, wA, 3);
    if (more) piece(kt + 3, (slot + 3) & 3, 3);
    SBAR();
  };
  WAIT_VL_BAR(8); stage(0, 0, true, true);
  for (int k4 = 1; k4 < 13; k4 += 4) {
    WAIT_VL_BAR(8); stage(k4, 1, true, false);
    WAIT_VL_BAR(8); stage(k4 + 1, 2, true, false);
    WAIT_VL_BAR(8); stage(k4 + 2, 3, true, false);
    WAIT_VL_BAR(8); stage(k4 + 3, 0, true, false);
  }
  WAIT_VL_BAR(8); stage(13, 1, false, false);
  WAIT_VL_BAR(4); stage(14, 2, false, false);
  WAIT_VL_BAR(0); stage(15, 3, false, false);
  mm2(aB, wB, 0); mm2(aB, wB, 1); mm2(aB, wB, 2); mm2(aB, wB, 3);
  STAMP();
  {
    const __amdgpu_buffer_rsrc_t rQ = mk_rsrc(wq, DM * NH * HD * 2);
    const int u = (blockIdx.x >> 3) * 8 + wid;
    if (u < 80) warm8k(rQ, (unsigned)((lane >> 3) * 8192 + (lane & 7) * 128), (unsigned)(u * 8 * 8192 + (blockIdx.x & 7) * 1024), lds0);
  }
  const int token = blockIdx.x * 8 + wid;
  const bool has1 = lane < 16;
  const u32x4 v0 = *(const u32x4*)(x + (size_t)token * DM + lane * 8);
  u32x4 v1 = {0u, 0u, 0u, 0u};
  if (has1) v1 = *(const u32x4*)(x + (size_t)token * DM + 512 + lane * 8);
  SBAR();
#pragma unroll
  for (int i = 0; i < 4; ++i) {
    const int kb32 = tb * 8 + wm * 4 + i;
#pragma unroll
    for (int n = 0; n < 2; ++n) {
#pragma unroll
      for (int sg = 0; sg < 2; ++sg) {
        const u32x4 w = pack8(acc[i][n], 8 * sg);
        if (ISV) {
          const int db = 2 * (wn - 2) + n;
          *(u32x4*)(VF + ((size_t)((((head * 64 + kb32) * 4 + db) * 2 + sg) * 64 + lane)) * 8) = w;
        } else {
          const int db = 2 * wn + n;
          *(u32x4*)(KF + ((size_t)(((head * 64 + kb32) * 8 + 2 * db + sg) * 64 + lane)) * 8) = w;
        }
      }
    }
  }
  STAMP();
  {
    float f0[8], f1[8];
#pragma unroll
    for (int j = 0; j < 4; ++j) {
      f0[2 * j] = bflo(v0[j]); f0[2 * j + 1] = bfhi(v0[j]);
      f1[2 * j] = bflo(v1[j]); f1[2 * j + 1] = bfhi(v1[j]);
    }
    float s = 0.f;
#pragma unroll
    for (int j = 0; j < 8; ++j) s += f0[j] + f1[j];
#pragma unroll
    for (int o = 1; o < 64; o <<= 1) s += __shfl_xor(s, o);
    const float mean = s * (1.0f / DM);
    float sq = 0.f;
#pragma unroll
    for (int j = 0; j < 8; ++j) {
      const float d0 = f0[j] - mean; sq += d0 * d0;
      const float d1 = has1 ? f1[j] - mean : 0.f; sq += d1 * d1;
    }
#pragma unroll
    for (int o = 1; o < 64; o <<= 1) sq += __shfl_xor(sq, o);
    const float rstd = 1.0f / sqrtf(sq * (1.0f / DM) + LN_EPS);
    const int tb32 = token >> 5, rr = token & 31;
    u32x4 o0, o1;
#pragma unroll
    for (int j = 0; j < 4; ++j) {
      o0[j] = cvtpk((f0[2 * j] - mean) * rstd, (f0[2 * j + 1] - mean) * rstd);
      o1[j] = cvtpk((f1[2 * j] - mean) * rstd, (f1[2 * j + 1] - mean) * rstd);
    }
    *(u32x4*)(XN + ((size_t)((tb32 * 40 + (lane >> 1)) * 64 + (lane & 1) * 32 + rr)) * 8) = o0;
    if (has1) *(u32x4*)(XN + ((size_t)((tb32 * 40 + 32 + (lane >> 1)) * 64 + (lane & 1) * 32 + rr)) * 8) = o1;
  }
  STAMP();
  STAMP();
  STAMP_END(0, dbgp);
}

__global__ __launch_bounds__(512, 2) void k1_kvproj(const u16* __restrict__ x, const u16* __restrict__ ckv,
                                                    const u16* __restrict__ wuk, const u16* __restrict__ wuv,
                                                    u16* __restrict__ KF, u16* __restrict__ VF, u16* __restrict__ XN,
                                                    const u16* __restrict__ wq) {
  extern __shared__ __attribute__((aligned(16))) char smem[];
  LAS char* lds = (LAS char*)smem;
  const unsigned lds0 = (unsigned)(uintptr_t)lds;
  const int tid = threadIdx.x, lane = tid & 63;
  const int wid = RFL(tid >> 6);
  const int b = blockIdx.x, xcd = b & 7, idx = b >> 3;
  const int head = xcd * 4 + (idx & 3), tb = idx >> 2;
  const u16* wsrc = (wid & 1) ? wuv : wuk;
#define K1_EXTRA
  if ((wid & 3) >= 2) k1_body<true>(lds, lds0, wid, lane, head, tb, ckv, wsrc, x, KF, VF, XN, wq K1_EXTRA);
  else k1_body<false>(lds, lds0, wid, lane, head, tb, ckv, wsrc, x, KF, VF, XN, wq K1_EXTRA);
}

constexpr int K2_LDS = 163840;
constexpr unsigned L_KB1 = 0, L_VB0 = 16384, L_VA1 = 32768, L_KA0 = 49152, L_KB0 = 65536, L_KA1 = 81920, L_VA0 = 98304,
                   L_VB1 = 114688, L_STASH = 131072;
constexpr int NSTEP = 17;
#define SPRIO 2
#define MPRIO 0
#define TRAILPRIO SPRIO

template <int OFF>
DI void dma1o(__amdgpu_buffer_rsrc_t rsrc, unsigned voff, unsigned soff, unsigned ldsaddr) {
  asm volatile("s_mov_b32 m0, %0\n\ts_nop 0\n\tbuffer_load_dwordx4 %1, %2, %3 offen offset:%4 lds"
               ::"s"(ldsaddr), "v"(voff), "s"(rsrc), "s"(soff), "i"(OFF) : "memory", "m0");
}
DI float fmul_s(float a, float b) { float r; asm("v_mul_f32 %0, %1, %2" : "=v"(r) : "v"(a), "v"(b)); return r; }
#define ACLOB "a8","a9","a10","a11","a12","a13","a14","a15","a16","a17","a18","a19","a20","a21","a22","a23","a24","a25","a26","a27","a28","a29","a30","a31","a32","a33","a34","a35","a36","a37","a38","a39","a40","a41","a42","a43","a44","a45","a46","a47","a48","a49","a50","a51","a52","a53","a54","a55","a56","a57","a58","a59","a60","a61","a62","a63","a64","a65","a66","a67","a68","a69","a70","a71","a72","a73","a74","a75","a76","a77","a78","a79","a80","a81","a82","a83","a84","a85","a86","a87","a88","a89","a90","a91","a92","a93","a94","a95","a96","a97","a98","a99","a100","a101","a102","a103","a104","a105","a106","a107","a108","a109","a110","a111","a112","a113","a114","a115","a116","a117","a118","a119","a120","a121","a122","a123","a124","a125","a126","a127"
constexpr int A_O = 8, A_Q = 72, A_R = 104, NRING = 6;
template <int SLOT, int OFF> DI void ring_read(unsigned addr) {
  asm volatile("ds_read_b128 a[%c1:%c2], %0 offset:%c3" ::"v"(addr), "i"(A_R + 4 * SLOT), "i"(A_R + 4 * SLOT + 3), "i"(OFF) : "memory");
}
template <int S, int OFF> DI void q_read(unsigned addr) {
  asm volatile("ds_read_b128 a[%c1:%c2], %0 offset:%c3" ::"v"(addr), "i"(A_Q + 4 * S), "i"(A_Q + 4 * S + 3), "i"(OFF) : "memory");
}
constexpr int V_NEGM = 64, V_S0 = 80, V_S1 = 96, V_PF = 112;
template <int W, int SLOT, int S, int DST> DI void qk_head() {
  asm volatile("s_waitcnt lgkmcnt(%c0)\n\tv_mfma_f32_32x32x16_bf16 v[%c1:%c2], a[%c3:%c4], a[%c5:%c6], v[%c7:%c8]"
               ::"i"(W), "i"(DST), "i"(DST + 15), "i"(A_R + 4 * SLOT), "i"(A_R + 4 * SLOT + 3), "i"(A_Q + 4 * S), "i"(A_Q + 4 * S + 3),
                 "i"(V_NEGM), "i"(V_NEGM + 15));
}
template <int W, int SLOT, int S, int DST> DI void qk_mfma() {
  asm volatile("s_waitcnt lgkmcnt(%c0)\n\tv_mfma_f32_32x32x16_bf16 v[%c1:%c2], a[%c3:%c4], a[%c5:%c6], v[%c1:%c2]"
               ::"i"(W), "i"(DST), "i"(DST + 15), "i"(A_R + 4 * SLOT), "i"(A_R + 4 * SLOT + 3), "i"(A_Q + 4 * S), "i"(A_Q + 4 * S + 3));
}
template <int W, int SLOT, int D, int P> DI void pv_mfma() {
  asm volatile("s_waitcnt lgkmcnt(%c0)\n\tv_mfma_f32_32x32x16_bf16 a[%c1:%c2], a[%c3:%c4], v[%c5:%c6], a[%c1:%c2]"
               ::"i"(W), "i"(A_O + 16 * D), "i"(A_O + 16 * D + 15), "i"(A_R + 4 * SLOT), "i"(A_R + 4 * SLOT + 3), "i"(V_PF + 4 * P),
                 "i"(V_PF + 4 * P + 3));
}
template <int W, int SLOT, int D, int P> DI void pv_zero() {
  asm volatile("s_waitcnt lgkmcnt(%c0)\n\tv_mfma_f32_32x32x16_bf16 a[%c1:%c2], a[%c3:%c4], v[%c5:%c6], 0"
               ::"i"(W), "i"(A_O + 16 * D), "i"(A_O + 16 * D + 15), "i"(A_R + 4 * SLOT), "i"(A_R + 4 * SLOT + 3), "i"(V_PF + 4 * P),
                 "i"(V_PF + 4 * P + 3));
}
template <int S, int P> DI void exps8(float& ps0, float& ps1, float& ps2, float& ps3) {
  float t0, t1, t2, t3, t4, t5, t6, t7;
  asm volatile("v_exp_f32 %4, v[%c12]\n\tv_exp_f32 %5, v[%c12+1]\n\tv_exp_f32 %6, v[%c12+2]\n\tv_exp_f32 %7, v[%c12+3]\n\t"
               "v_exp_f32 %8, v[%c12+4]\n\tv_exp_f32 %9, v[%c12+5]\n\tv_exp_f32 %10, v[%c12+6]\n\tv_exp_f32 %11, v[%c12+7]\n\t"
               "v_add_f32 %0, %0, %4\n\tv_add_f32 %1, %1, %5\n\tv_cvt_pk_bf16_f32 v[%c13], %4, %5\n\t"
               "v_add_f32 %2, %2, %6\n\tv_add_f32 %3, %3, %7\n\tv_cvt_pk_bf16_f32 v[%c13+1], %6, %7\n\t"
               "v_add_f32 %0, %0, %8\n\tv_add_f32 %1, %1, %9\n\tv_cvt_pk_bf16_f32 v[%c13+2], %8, %9\n\t"
               "v_add_f32 %2, %2, %10\n\tv_add_f32 %3, %3, %11\n\tv_cvt_pk_bf16_f32 v[%c13+3], %10, %11"
               : "+v"(ps0), "+v"(ps1), "+v"(ps2), "+v"(ps3), "=&v"(t0), "=&v"(t1), "=&v"(t2), "=&v"(t3), "=&v"(t4), "=&v"(t5),
                 "=&v"(t6), "=&v"(t7)
               : "i"(S), "i"(V_PF + 4 * P));
}
DI float exps32() {
  float ps0 = 0.f, ps1 = 0.f, ps2 = 0.f, ps3 = 0.f, r;
  exps8<V_S0, 0>(ps0, ps1, ps2, ps3); exps8<V_S0 + 8, 1>(ps0, ps1, ps2, ps3);
  exps8<V_S1, 2>(ps0, ps1, ps2, ps3); exps8<V_S1 + 8, 3>(ps0, ps1, ps2, ps3);
  asm volatile("v_add_f32 %1, %1, %2\n\tv_add_f32 %3, %3, %4\n\tv_add_f32 %0, %1, %3" : "=v"(r), "+v"(ps0), "+v"(ps1), "+v"(ps2), "+v"(ps3));
  return r;
}
DI float rowmax32() {
  float a, b;
  asm volatile("v_max3_f32 %0, v80, v81, v82\n\tv_max3_f32 %1, v83, v84, v85\n\tv_max3_f32 %0, %0, v86, v87\n\tv_max3_f32 %1, %1, v88, v89\n\t"
               "v_max3_f32 %0, %0, v90, v91\n\tv_max3_f32 %1, %1, v92, v93\n\tv_max3_f32 %0, %0, v94, v95\n\tv_max3_f32 %1, %1, v96, v97\n\t"
               "v_max3_f32 %0, %0, v98, v99\n\tv_max3_f32 %1, %1, v100, v101\n\tv_max3_f32 %0, %0, v102, v103\n\tv_max3_f32 %1, %1, v104, v105\n\t"
               "v_max3_f32 %0, %0, v106, v107\n\tv_max3_f32 %1, %1, v108, v109\n\tv_max3_f32 %0, %0, v110, v111\n\tv_max_f32 %0, %0, %1"
               : "=&v"(a), "=&v"(b));
  return a;
}
template <int R> DI void vreg_add(float t) { asm volatile("v_add_f32 v[%c1], v[%c1], %0" ::"v"(t), "i"(R)); }
template <int S, int... E> DI void mask_all_(float ninf, std::integer_sequence<int, E...>) { (vreg_add<S + E>(ninf), ...); }
template <int S, int... E> DI void mask_tri_(int dq, float ninf, std::integer_sequence<int, E...>) {
  (vreg_add<S + E>(((E & 3) + 8 * (E >> 2) > dq) ? ninf : 0.f), ...);
}
template <int I> DI float acc_rd() { float r; asm volatile("v_accvgpr_read_b32 %0, a[%c1]" : "=v"(r) : "i"(I)); return r; }
template <int I> DI void acc_wr(float v) { asm volatile("v_accvgpr_write_b32 a[%c1], %0" ::"v"(v), "i"(I)); }
template <int... E> DI void o_scale_(float f, std::integer_sequence<int, E...>) { (acc_wr<A_O + E>(fmul_s(acc_rd<A_O + E>(), f)), ...); }
template <int R> DI u32x4 acc_scale8(float f) {
  u32x4 o; float t0, t1, t2, t3, t4, t5, t6, t7;
  asm volatile("v_accvgpr_read_b32 %4, a[%c13]\n\tv_accvgpr_read_b32 %5, a[%c13+1]\n\tv_accvgpr_read_b32 %6, a[%c13+2]\n\t"
               "v_accvgpr_read_b32 %7, a[%c13+3]\n\tv_accvgpr_read_b32 %8, a[%c13+4]\n\tv_accvgpr_read_b32 %9, a[%c13+5]\n\t"
               "v_accvgpr_read_b32 %10, a[%c13+6]\n\tv_accvgpr_read_b32 %11, a[%c13+7]\n\t"
               "v_mul_f32 %4, %4, %12\n\tv_mul_f32 %5, %5, %12\n\tv_mul_f32 %6, %6, %12\n\tv_mul_f32 %7, %7, %12\n\t"
               "v_mul_f32 %8, %8, %12\n\tv_mul_f32 %9, %9, %12\n\tv_mul_f32 %10, %10, %12\n\tv_mul_f32 %11, %11, %12\n\t"
               "v_cvt_pk_bf16_f32 %0, %4, %5\n\tv_cvt_pk_bf16_f32 %1, %6, %7\n\tv_cvt_pk_bf16_f32 %2, %8, %9\n\tv_cvt_pk_bf16_f32 %3, %10, %11"
               : "=&v"(o[0]), "=&v"(o[1]), "=&v"(o[2]), "=&v"(o[3]), "=&v"(t0), "=&v"(t1), "=&v"(t2), "=&v"(t3), "=&v"(t4),
                 "=&v"(t5), "=&v"(t6), "=&v"(t7)
               : "v"(f), "i"(R));
  return o;
}
template <int R, int OFF> DI void acc_to_lds(unsigned addr) {
  asm volatile("ds_write_b128 %0, a[%c1:%c2] offset:%c3" ::"v"(addr), "i"(R), "i"(R + 3), "i"(OFF) : "memory");
}
template <int R> DI u32x4 acc_combine8f(const f32x4& w0, const f32x4& w1, float cw, float ca) {
  u32x4 o; float t0, t1, t2, t3, t4, t5, t6, t7;
  asm volatile("v_accvgpr_read_b32 %4, a[%c22]\n\tv_accvgpr_read_b32 %5, a[%c22+1]\n\tv_accvgpr_read_b32 %6, a[%c22+2]\n\t"
               "v_accvgpr_read_b32 %7, a[%c22+3]\n\tv_accvgpr_read_b32 %8, a[%c22+4]\n\tv_accvgpr_read_b32 %9, a[%c22+5]\n\t"
               "v_accvgpr_read_b32 %10, a[%c22+6]\n\tv_accvgpr_read_b32 %11, a[%c22+7]\n\t"
               "v_mul_f32 %4, %4, %21\n\tv_mul_f32 %5, %5, %21\n\tv_mul_f32 %6, %6, %21\n\tv_mul_f32 %7, %7, %21\n\t"
               "v_mul_f32 %8, %8, %21\n\tv_mul_f32 %9, %9, %21\n\tv_mul_f32 %10, %10, %21\n\tv_mul_f32 %11, %11, %21\n\t"
               "v_fma_f32 %4, %12, %20, %4\n\tv_fma_f32 %5, %13, %20, %5\n\tv_fma_f32 %6, %14, %20, %6\n\tv_fma_f32 %7, %15, %20, %7\n\t"
               "v_fma_f32 %8, %16, %20, %8\n\tv_fma_f32 %9, %17, %20, %9\n\tv_fma_f32 %10, %18, %20, %10\n\tv_fma_f32 %11, %19, %20, %11\n\t"
               "v_cvt_pk_bf16_f32 %0, %4, %5\n\tv_cvt_pk_bf16_f32 %1, %6, %7\n\tv_cvt_pk_bf16_f32 %2, %8, %9\n\tv_cvt_pk_bf16_f32 %3, %10, %11"
               : "=&v"(o[0]), "=&v"(o[1]), "=&v"(o[2]), "=&v"(o[3]), "=&v"(t0), "=&v"(t1), "=&v"(t2), "=&v"(t3), "=&v"(t4),
                 "=&v"(t5), "=&v"(t6), "=&v"(t7)
               : "v"(w0[0]), "v"(w0[1]), "v"(w0[2]), "v"(w0[3]), "v"(w1[0]), "v"(w1[1]), "v"(w1[2]), "v"(w1[3]), "v"(cw), "v"(ca),
                 "i"(R));
  return o;
}
DI void scores_add(float aone, float v) {
  asm volatile("s_nop 1\n\tv_mfma_f32_32x32x2_f32 v[80:95], %0, %1, v[80:95]\n\tv_mfma_f32_32x32x2_f32 v[96:111], %0, %1, v[96:111]" ::"v"(aone), "v"(v));
}
DI void negm_set(float aone, float v) { asm volatile("s_nop 1\n\tv_mfma_f32_32x32x2_f32 v[64:79], %0, %1, 0" ::"v"(aone), "v"(v)); }
#define ACC_FENCE() asm volatile("s_nop 15\n\ts_nop 3" ::: "memory")
#define BAR_A(WAITS) asm volatile("s_waitcnt " WAITS "\n\ts_barrier" ::: "memory", "v127", ACLOB)

template <int J> DI void frag_read(unsigned ka, unsigned va) {
  if constexpr (J < 16) ring_read<J % NRING, (J & 1) * 8192 + (J >> 1) * 1024>(ka);
  else { constexpr int i = J - 16; ring_read<J % NRING, (i >> 3) * 8192 + ((i & 3) * 2 + ((i >> 2) & 1)) * 1024>(va); }
}
#define PDX 4
constexpr int PD = PDX;

__global__ __launch_bounds__(512, 2) __attribute__((amdgpu_num_vgpr(64))) void k2_attn(const u16* __restrict__ XN, const u16* __restrict__ wq,
                                                  const u16* __restrict__ KF, const u16* __restrict__ VF,
                                                  u16* __restrict__ CF, float* __restrict__ LAM,
                                                  const u16* __restrict__ wo) {
  extern __shared__ __attribute__((aligned(16))) char smem[];
  LAS char* lds = (LAS char*)smem;
  const unsigned lds0 = (unsigned)(uintptr_t)lds;
  const int tid = threadIdx.x, lane = tid & 63;
  const int wid = RFL(tid >> 6);
  const int b = blockIdx.x, xcd = b & 7, idx = b >> 3;
  const int head = xcd * 4 + (idx >> 3), px = idx & 7;
  const int qbH = 15 - px, qbL = px;
  const int rr = wid & 3, c = wid >> 2;
  const int r = lane & 31, h = lane >> 5;
  const int g1 = (lane >> 4) & 1, q4 = (lane & 15) >> 2, p4 = lane & 3;
  const __amdgpu_buffer_rsrc_t rX = mk_rsrc(XN, SEQ * DM * 2);
  const __amdgpu_buffer_rsrc_t rW = mk_rsrc(wq, DM * NH * HD * 2);
  const __amdgpu_buffer_rsrc_t rK = mk_rsrc(KF, NH * SEQ * HD * 2);
  const __amdgpu_buffer_rsrc_t rV = mk_rsrc(VF, NH * SEQ * HD * 2);
  const unsigned vlin = lane * 16;
  const unsigned voffW = ((lane >> 2) & 7) * 8192 + (lane >> 5) * 64 + (lane & 3) * 16;
  const int p1 = 2 * (px + 1);
  auto kvoff = [&](int sg) -> unsigned {
    const int hidx = sg < p1 ? sg : 2 * sg - p1 + c;
    return (unsigned)((head * 64 + 2 * hidx) * 8192);
  };
  STAMP_DECL();
  STAMP();
  const int tbx = (wid < 4) ? (4 * qbH + wid) : (4 * qbL + wid - 4);
  auto q_load = [&](unsigned addr) {
    q_read<0, 0>(addr); q_read<1, 1024>(addr); q_read<2, 2048>(addr); q_read<3, 3072>(addr);
    q_read<4, 4096>(addr); q_read<5, 5120>(addr); q_read<6, 6144>(addr); q_read<7, 7168>(addr);
  };
  {
    auto q_piece = [&](int kt, int i) {
      const unsigned sb = lds0 + (unsigned)(kt % 3) * 49152;
      if (i < 2) dma1(rW, voffW, (unsigned)((kt * 64 + wid * 8) * 8192 + head * 256 + i * 128), sb + (wid * 2 + i) * 1024);
      else dma1(rX, vlin, (unsigned)((tbx * 40 + 4 * kt + (i - 2)) * 1024), sb + 16384 + wid * 4096 + (i - 2) * 1024);
    };
    auto kv_piece = [&](int i) {
      const unsigned part = wid * 2048 + i * 1024;
      dma1(rK, vlin, (unsigned)(head * 64 * 8192) + part, lds0 + L_KA0 + part);
    };
#pragma unroll
    for (int i = 0; i < 6; ++i) q_piece(0, i);
#pragma unroll
    for (int i = 0; i < 6; ++i) q_piece(1, i);
    f32x16 qa[2][2];
#pragma unroll
    for (int i = 0; i < 2; ++i)
#pragma unroll
      for (int n = 0; n < 2; ++n) qa[i][n] = f32x16{};
    const unsigned wbase = h * 2048 + c * 1024 + q4 * 64 + g1 * 32 + p4 * 8;
    const unsigned xbase = 16384 + rr * 4096 + lane * 16;
    bf16x8 wA[2], xA[2], wB[2], xB[2];
    auto rdq = [&](bf16x8 (&w)[2], bf16x8 (&x)[2], LAS const char* sb, int s) {
      w[0] = lds_tr2(sb + wbase + s * 4096, sb + wbase + s * 4096 + 256);
      x[0] = lds_b128(sb + xbase + s * 1024);
      w[1] = lds_tr2(sb + wbase + s * 4096 + 512, sb + wbase + s * 4096 + 768);
      x[1] = lds_b128(sb + xbase + 16384 + s * 1024);
    };
    auto mmq = [&](const bf16x8 (&w)[2], const bf16x8 (&x)[2]) {
      qa[0][0] = MFMA32(w[0], x[0], qa[0][0]);
      qa[1][0] = MFMA32(w[0], x[1], qa[1][0]);
      qa[0][1] = MFMA32(w[1], x[0], qa[0][1]);
      qa[1][1] = MFMA32(w[1], x[1], qa[1][1]);
    };
    for (int kt = 0; kt < 10; ++kt) {
      if (kt < 9) { WAIT_VL_BAR(6); } else { WAIT_VL_BAR(2); }
      LAS const char* sb = lds + (unsigned)(kt % 3) * 49152;
      auto piece = [&](int i) {
        if (kt < 8) q_piece(kt + 2, i);
        else if (kt == 8 && i < 2) kv_piece(i);
      };
      rdq(wA, xA, sb, 0);
      SBAR();
      if (kt > 0) mmq(wB, xB);
      piece(0); SBAR();
      rdq(wB, xB, sb, 1);
      SBAR();
      mmq(wA, xA);
      piece(1); piece(2); SBAR();
      rdq(wA, xA, sb, 2);
      SBAR();
      mmq(wB, xB);
      piece(3); piece(4); SBAR();
      rdq(wB, xB, sb, 3);
      SBAR();
      mmq(wA, xA);
      piece(5); SBAR();
    }
    mmq(wB, xB);
    STAMP();
    WAIT_L_BAR();
#pragma unroll
    for (int i = 0; i < 2; ++i) {
      LAS char* dst = lds + (i == 0 ? L_STASH : 0u) + rr * 8192 + lane * 16;
#pragma unroll
      for (int n = 0; n < 2; ++n) {
        const f32x16 t = qa[i][n] * QSCALE;
#pragma unroll
        for (int sg = 0; sg < 2; ++sg) *(LAS u32x4*)(dst + (4 * c + 2 * n + sg) * 1024) = pack8(t, 8 * sg);
      }
    }
    WAIT_L_BAR();
    q_load(lds0 + (c ? 0u : L_STASH) + rr * 8192 + lane * 16);
    BAR_A("vmcnt(0) lgkmcnt(0)");
  }
  STAMP();

  negm_set(0.f, 0.f);
  float mref = 0.f, lsum = 0.f, mfin = 0.f, lfin = 0.f;
  const unsigned kown0 = c ? L_KB0 : L_KA0, kown1 = c ? L_KB1 : L_KA1;
  const unsigned vown0 = c ? L_VB0 : L_VA0, vown1 = c ? L_VB1 : L_VA1;
  const unsigned lrd = lds0 + lane * 16;
  float* lamp = LAM + (size_t)b * 256 + rr * 64;
  f32x2 mlA = {0.f, 0.f};

  auto refix = [&](const bool first) __attribute__((always_inline)) {
    const float rm = swap_max(rowmax32());
    const float dl = first ? fmaxf(rm, NEG_BIG) : fmaxf(rm, 0.f);
    const float f = first ? 0.f : __builtin_amdgcn_exp2f(-dl);
    mref = first ? dl : mref + dl;
    lsum = first ? 0.f : lsum * f;
    int ln = lane;
    asm volatile("" : "+v"(ln));
    const float aone = ln < 32 ? 1.f : 0.f;
    scores_add(aone, -dl);
    negm_set(aone, -mref);
    ACC_FENCE();
    if (!first) { o_scale_(f, std::make_integer_sequence<int, 64>{}); ACC_FENCE(); }
  };
#define TRAIL() do { pv_mfma<0, 28 % NRING, 0, 3>(); pv_mfma<0, 29 % NRING, 1, 3>(); pv_mfma<0, 30 % NRING, 2, 3>(); pv_mfma<0, 31 % NRING, 3, 3>(); SBAR(); } while (0)
  auto splain = [&]() __attribute__((always_inline)) {
    __builtin_amdgcn_s_setprio(TRAILPRIO);
    TRAIL();
    if (TRAILPRIO != SPRIO) __builtin_amdgcn_s_setprio(SPRIO);
    float pst = exps32();
    if (__builtin_expect(__any(pst > P_LIMIT), 0)) { refix(false); pst = exps32(); }
    asm volatile("v_add_f32 %0, %0, %1" : "+v"(lsum) : "v"(pst));
    __builtin_amdgcn_s_setprio(MPRIO);
  };
  auto sgen = [&](const int tau) __attribute__((always_inline)) {
    __builtin_amdgcn_s_setprio(SPRIO);
    if (tau > 0) TRAIL();
    const bool lastL = c == 1 && tau == p1 - 1;
    const bool diag = tau == NSTEP - 1 || (c == 1 && (tau == p1 - 2 || tau == p1 - 1));
    const bool first = tau == 0 || (c == 1 && tau == p1);
    int ln = lane;
    asm volatile("" : "+v"(ln));
    if (diag) {
      const int hp = tau == NSTEP - 1 ? c : tau - (p1 - 2);
      float ninf = -INFINITY;
      asm volatile("" : "+v"(ninf));
      const int dq = (ln & 31) - 4 * (ln >> 5);
      if (2 * hp > rr) mask_all_<V_S0>(ninf, std::make_integer_sequence<int, 16>{});
      else if (2 * hp == rr) mask_tri_<V_S0>(dq, ninf, std::make_integer_sequence<int, 16>{});
      if (2 * hp + 1 > rr) mask_all_<V_S1>(ninf, std::make_integer_sequence<int, 16>{});
      else if (2 * hp + 1 == rr) mask_tri_<V_S1>(dq, ninf, std::make_integer_sequence<int, 16>{});
    }
    if (first && tau > 0) {
      ACC_FENCE();
      const float inv = 1.0f / lfin;
      u16* dstp = CF + ((size_t)(((4 * qbL + rr) * 32 + head) * 8) * 64 + ln) * 8;
      *(u32x4*)(dstp) = acc_scale8<A_O + 0>(inv);          *(u32x4*)(dstp + 512) = acc_scale8<A_O + 8>(inv);
      *(u32x4*)(dstp + 1024) = acc_scale8<A_O + 16>(inv);  *(u32x4*)(dstp + 1536) = acc_scale8<A_O + 24>(inv);
      *(u32x4*)(dstp + 2048) = acc_scale8<A_O + 32>(inv);  *(u32x4*)(dstp + 2560) = acc_scale8<A_O + 40>(inv);
      *(u32x4*)(dstp + 3072) = acc_scale8<A_O + 48>(inv);  *(u32x4*)(dstp + 3584) = acc_scale8<A_O + 56>(inv);
    }
    if (first) refix(true);
    float pst = exps32();
    if (!first && __any(pst > P_LIMIT)) { refix(false); pst = exps32(); }
    asm volatile("v_add_f32 %0, %0, %1" : "+v"(lsum) : "v"(pst));
    if (lastL || tau == NSTEP - 1) {
      mfin = mref; lfin = swap_add(lsum);
      if (lastL) {
        negm_set(0.f, 0.f);
        q_load(lds0 + L_STASH + rr * 8192 + ln * 16);
      } else {
        f32x2* lp = (f32x2*)lamp + (ln & 31);
        if (c == 0) { if (ln < 32) *lp = (f32x2){mfin, lfin}; }
        else mlA = *lp;
      }
    }
    __builtin_amdgcn_s_setprio(0);
  };
#define KVDMA(i) do { if ((i) == 0) dma1o<0>(rK, vlin, gk, lk); else if ((i) == 1) dma1o<1024>(rK, vlin, gk, lk); \
    else if ((i) == 2) dma1o<2048>(rK, vlin, gk, lk); else if ((i) == 3) dma1o<3072>(rK, vlin, gk, lk); \
    else if ((i) == 4) dma1o<0>(rV, vlin, gv, lv); else if ((i) == 5) dma1o<1024>(rV, vlin, gv, lv); \
    else if ((i) == 6) dma1o<2048>(rV, vlin, gv, lv); else dma1o<3072>(rV, vlin, gv, lv); } while (0)
#define MSTEP(J, FIRST, LAST, DMA, ZO) do { \
    constexpr int out_ = ((J) + PD - 1 < (LAST) ? (J) + PD - 1 : (LAST)) - (J);     \
    if constexpr ((J) < 2) qk_head<out_, (J) % NRING, 0, ((J) & 1) ? V_S1 : V_S0>(); \
    else if constexpr ((J) < 16) qk_mfma<out_, (J) % NRING, ((J) >> 1), ((J) & 1) ? V_S1 : V_S0>(); \
    else if constexpr ((J) < 20 && (ZO)) { if (zero_o) pv_zero<out_, (J) % NRING, (J) & 3, 0>(); else pv_mfma<out_, (J) % NRING, (J) & 3, 0>(); } \
    else pv_mfma<out_, (J) % NRING, (J) & 3, (((J) - 16) >> 2)>(); \
    if constexpr ((J) + PD <= (LAST)) frag_read<(J) + PD>(ka, va); \
    if constexpr ((DMA) && ((J) & 1) && (J) < 16) KVDMA((J) >> 1); \
    SBAR(); } while (0)
#define MSTEP4(J, FIRST, LAST, DMA, ZO) MSTEP(J, FIRST, LAST, DMA, ZO); MSTEP((J) + 1, FIRST, LAST, DMA, ZO); MSTEP((J) + 2, FIRST, LAST, DMA, ZO); MSTEP((J) + 3, FIRST, LAST, DMA, ZO)
#define KSLOT(t) (((t) < p1) ? (((t) & 1) ? L_KA1 : L_KA0) : (((t) & 1) ? kown1 : kown0))
#define VSLOT(t) (((t) < p1) ? (((t) & 1) ? L_VA1 : L_VA0) : (((t) & 1) ? vown1 : vown0))
#define STAGE_ADDR(t) \
      const unsigned gk = ((t) + 1 < NSTEP && (c == 0 || (t) + 1 >= p1)) ? kvoff((t) + 1) + rr * 4096 : 0x7ff00000u; \
      const unsigned gv = (c == 0 || (t) >= p1) ? kvoff(t) + rr * 4096 : 0x7ff00000u; \
      const unsigned lk = lds0 + ((((t) + 1) & 1) ? kown1 : kown0) + rr * 4096; \
      const unsigned lv = lds0 + (((t) & 1) ? vown1 : vown0) + rr * 4096;

  if (c == 1) asm volatile("s_barrier" ::: "memory");
  {
    const unsigned ka = lrd + L_KA0, va = 0;
    STAGE_ADDR(0)
    constexpr bool zero_o = false;
    frag_read<0>(ka, va); frag_read<1>(ka, va); frag_read<2>(ka, va); frag_read<3>(ka, va);
    SBAR();
    MSTEP4(0, 0, 15, true, false); MSTEP4(4, 0, 15, true, false); MSTEP4(8, 0, 15, true, false); MSTEP4(12, 0, 15, true, false);
    BAR_A("lgkmcnt(0)");
    ACC_FENCE();
    sgen(0);
    BAR_A("vmcnt(0) lgkmcnt(0)");
  }
#define MPHASE(tau, DMA, ZO) do { \
      const bool zero_o = (tau) == 1 || (c == 1 && (tau) == p1 + 1);     \
      (void)zero_o; \
      const unsigned ka = lrd + KSLOT(tau); \
      const unsigned va = lrd + VSLOT((tau) - 1); \
      STAGE_ADDR(tau) \
      (void)gk; (void)gv; (void)lk; (void)lv; \
      frag_read<0>(ka, va); frag_read<1>(ka, va); frag_read<2>(ka, va); frag_read<3>(ka, va); \
      SBAR(); \
      MSTEP4(0, 0, 31, DMA, ZO); MSTEP4(4, 0, 31, DMA, ZO); MSTEP4(8, 0, 31, DMA, ZO); MSTEP4(12, 0, 31, DMA, ZO); \
      MSTEP4(16, 0, 31, DMA, ZO); MSTEP4(20, 0, 31, DMA, ZO); MSTEP4(24, 0, 31, DMA, ZO); \
      BAR_A("lgkmcnt(0)"); } while (0)
  int tau = 1;
  while (tau < NSTEP) {
    const bool special = tau == 1 || tau == NSTEP - 1 || (c == 1 && tau >= p1 - 2 && tau <= p1 + 1);
    if (special) {
      STAMP();
      MPHASE(tau, true, true);
      STAMP();
      sgen(tau);
      BAR_A("vmcnt(0) lgkmcnt(0)");
      ++tau;
    } else if (c == 1 && tau < p1 - 2) {
      do {
        STAMP();
        MPHASE(tau, false, false);
        STAMP();
        splain();
        BAR_A("lgkmcnt(0)");
        ++tau;
      } while (tau < p1 - 2);
    } else {
      do {
        STAMP();
        MPHASE(tau, true, false);
        STAMP();
        splain();
        BAR_A("vmcnt(0) lgkmcnt(0)");
        ++tau;
      } while (tau < NSTEP - 1);
    }
  }
#undef MPHASE
#undef TRAIL
  {
    const unsigned v128 = lane * 128;
    const unsigned dead = lds0 + vown1 + rr * 256;
    const int u = idx * 8 + wid;
    if (u < 160) warm8k(mk_rsrc(wo, NH * HD * DM * 2), v128, (unsigned)((xcd >> 1) * 1024 * 1280 + u * 8192), dead);
  }
  {
    const unsigned ka = 0, va = lrd + VSLOT(NSTEP - 1);
    const unsigned gk = 0, gv = 0, lk = 0, lv = 0;
    (void)gk; (void)gv; (void)lk; (void)lv; (void)ka;
    const bool zero_o = c == 1 && p1 == NSTEP - 1;
    frag_read<16>(ka, va); frag_read<17>(ka, va); frag_read<18>(ka, va); frag_read<19>(ka, va);
    SBAR();
    MSTEP4(16, 16, 31, false, true); MSTEP4(20, 16, 31, false, true); MSTEP4(24, 16, 31, false, true); MSTEP4(28, 16, 31, false, true);
    BAR_A("lgkmcnt(0)");
    int ln = lane;
    asm volatile("" : "+v"(ln));
    const unsigned mine = lds0 + (c ? L_KB1 : L_KA0) + ((rr & 2) ? (c ? L_KB0 - L_KB1 : L_KA1 - L_KA0) : 0u) + (rr & 1) * 8192 + ln * 16;
    const unsigned theirs = (c ? L_KA0 : L_KB1) + ((rr & 2) ? (c ? L_KA1 - L_KA0 : L_KB0 - L_KB1) : 0u) + (rr & 1) * 8192 + ln * 16;
    ACC_FENCE();
    if (c == 0) {
      acc_to_lds<A_O + 32, 0>(mine); acc_to_lds<A_O + 36, 1024>(mine); acc_to_lds<A_O + 40, 2048>(mine); acc_to_lds<A_O + 44, 3072>(mine);
      acc_to_lds<A_O + 48, 4096>(mine); acc_to_lds<A_O + 52, 5120>(mine); acc_to_lds<A_O + 56, 6144>(mine); acc_to_lds<A_O + 60, 7168>(mine);
      BAR_A("lgkmcnt(0)");
    } else {
      acc_to_lds<A_O + 0, 0>(mine); acc_to_lds<A_O + 4, 1024>(mine); acc_to_lds<A_O + 8, 2048>(mine); acc_to_lds<A_O + 12, 3072>(mine);
      acc_to_lds<A_O + 16, 4096>(mine); acc_to_lds<A_O + 20, 5120>(mine); acc_to_lds<A_O + 24, 6144>(mine); acc_to_lds<A_O + 28, 7168>(mine);
      if (ln < 32) *(LAS f32x2*)(lds + L_STASH + rr * 256 + ln * 8) = (f32x2){mfin, lfin};
    }
    BAR_A("lgkmcnt(0)");
    {
      const f32x2 mo = c ? mlA : *(LAS const f32x2*)(lds + L_STASH + rr * 256 + (ln & 31) * 8);
      const float m = fmaxf(mo[0], mfin);
      const float eo = __builtin_amdgcn_exp2f(mo[0] - m), em = __builtin_amdgcn_exp2f(mfin - m);
      const float inv = 1.0f / (mo[1] * eo + lfin * em);
      const float co = eo * inv, cm = em * inv;
      f32x4 w[8];
#pragma unroll
      for (int i = 0; i < 8; ++i) w[i] = *(LAS const f32x4*)(lds + theirs + i * 1024);
      u16* dstp = CF + ((size_t)(((4 * qbH + rr) * 32 + head) * 8 + 4 * c) * 64 + ln) * 8;
      if (c == 0) {
        *(u32x4*)(dstp) = acc_combine8f<A_O + 0>(w[0], w[1], co, cm);         *(u32x4*)(dstp + 512) = acc_combine8f<A_O + 8>(w[2], w[3], co, cm);
        *(u32x4*)(dstp + 1024) = acc_combine8f<A_O + 16>(w[4], w[5], co, cm); *(u32x4*)(dstp + 1536) = acc_combine8f<A_O + 24>(w[6], w[7], co, cm);
      } else {
        *(u32x4*)(dstp) = acc_combine8f<A_O + 32>(w[0], w[1], co, cm);        *(u32x4*)(dstp + 512) = acc_combine8f<A_O + 40>(w[2], w[3], co, cm);
        *(u32x4*)(dstp + 1024) = acc_combine8f<A_O + 48>(w[4], w[5], co, cm); *(u32x4*)(dstp + 1536) = acc_combine8f<A_O + 56>(w[6], w[7], co, cm);
      }
    }
  }
#undef KSLOT
#undef VSLOT
#undef STAGE_ADDR
#undef KVDMA
#undef MSTEP
#undef MSTEP4
  STAMP();
  STAMP_END(1, DBGP(CF, WS_CF));
}

constexpr int K3_STAGE = 37888, K3_NST = 4, K3_LDS = K3_STAGE * K3_NST;

__global__ __launch_bounds__(512, 2) void k3_oproj(const u16* __restrict__ CF, const u16* __restrict__ wo,
                                                   u16* __restrict__ PT) {
  extern __shared__ __attribute__((aligned(16))) char smem[];
  LAS char* lds = (LAS char*)smem;
  const unsigned lds0 = (unsigned)(uintptr_t)lds;
  const int tid = threadIdx.x, lane = tid & 63;
  const int wid = RFL(tid >> 6);
  const int b = blockIdx.x, xcd = b & 7, idx = b >> 3;
  const int ks = xcd >> 1, panel = (xcd & 1) * 8 + (idx >> 2), nt = idx & 3;
  const int tbi = wid & 3, kp = wid >> 2;
  const int h = lane >> 5, g1 = (lane >> 4) & 1, q4 = (lane & 15) >> 2, p4 = lane & 3;
  const __amdgpu_buffer_rsrc_t rC = mk_rsrc(CF, NH * SEQ * HD * 2);
  const __amdgpu_buffer_rsrc_t rW = mk_rsrc(wo, NH * HD * DM * 2);
  const unsigned vlin = lane * 16;
  unsigned voffW[3];
#pragma unroll
  for (int i = 0; i < 3; ++i) {
    const int pc = wid + 8 * i;
    const int u = 2 * pc + (lane >> 5);
    const int kb = u / 5, nb = u % 5;
    voffW[i] = (unsigned)((kb * 8 + ((lane >> 2) & 7)) * 1280 + (nb * 32 + (lane & 3) * 8) * 2);
  }
  const bool lo = wid < 4;
  const unsigned sW0 = (unsigned)(ks * 1024 * 1280 + nt * 320);
  const unsigned sC0 = (unsigned)(((panel * 4 + tbi) * 256 + ks * 64) * 1024);
  const __amdgpu_buffer_rsrc_t r2 = lo ? rW : rC;
  const unsigned v2 = lo ? voffW[2] : vlin;
  const unsigned s2 = lo ? sW0 : sC0, st2 = lo ? 81920u : 4096u, l2 = lo ? (wid + 16) * 1024 : 20480 + tbi * 4096;
  const unsigned s3 = sC0 + (lo ? 3072u : 1024u), l3 = 20480 + tbi * 4096 + (lo ? 3072 : 1024);
  const unsigned s4 = lo ? 0x7ff00000u : sC0 + 2048u, l4 = lo ? 36864 : 20480 + tbi * 4096 + 2048;
  auto piece = [&](int j, int slot, int i) __attribute__((always_inline)) {
    const unsigned sb = lds0 + slot * K3_STAGE;
    if (i == 0) dma1(rW, voffW[0], sW0 + j * 81920, sb + wid * 1024);
    else if (i == 1) dma1(rW, voffW[1], sW0 + j * 81920, sb + (wid + 8) * 1024);
    else if (i == 2) dma1(r2, v2, s2 + j * st2, sb + l2);
    else if (i == 3) dma1(rC, vlin, s3 + j * 4096, sb + l3);
    else dma1(rC, vlin, s4 + j * 4096, sb + l4);
  };
  f32x16 acc[5];
#pragma unroll
  for (int n = 0; n < 5; ++n) acc[n] = f32x16{};
  const unsigned wbase = h * 256 + q4 * 64 + g1 * 32 + p4 * 8 + kp * 4 * 2560;
  const unsigned cbase = 20480 + tbi * 4096 + lane * 16 + kp * 2048;
  auto rd = [&](bf16x8& cf, bf16x8 (&w)[5], LAS const char* sb, int si) {
    cf = lds_b128(sb + cbase + si * 1024);
#pragma unroll
    for (int n = 0; n < 5; ++n)
      w[n] = lds_tr2(sb + wbase + (2 * si) * 2560 + n * 512, sb + wbase + (2 * si + 1) * 2560 + n * 512);
  };
  bf16x8 cA, cB, wA[5], wB[5];
  STAMP_DECL();
  STAMP();
#pragma unroll
  for (int j = 0; j < 3; ++j)
#pragma unroll
    for (int i = 0; i < 5; ++i) piece(j, j, i);
  STAMP();
  auto stage = [&](const int j, const int slot, const bool more, const bool first) __attribute__((always_inline)) {
    STAMP();
    LAS const char* sb = lds + slot * K3_STAGE;
    rd(cA, wA, sb, 0);
    SBAR();
    if (!first) {
      acc[0] = MFMA32(wB[0], cB, acc[0]); acc[1] = MFMA32(wB[1], cB, acc[1]);
      if (more) piece(j + 3, (slot + 3) & 3, 0);
      SBAR();
      acc[2] = MFMA32(wB[2], cB, acc[2]); acc[3] = MFMA32(wB[3], cB, acc[3]);
      if (more) piece(j + 3, (slot + 3) & 3, 1);
      SBAR();
      acc[4] = MFMA32(wB[4], cB, acc[4]);
    } else {
      piece(3, 3, 0); piece(3, 3, 1);
    }
    SBAR();
    rd(cB, wB, sb, 1);
    SBAR();
    acc[0] = MFMA32(wA[0], cA, acc[0]);
    if (more) piece(j + 3, (slot + 3) & 3, 2);
    SBAR();
    acc[1] = MFMA32(wA[1], cA, acc[1]); acc[2] = MFMA32(wA[2], cA, acc[2]);
    if (more) piece(j + 3, (slot + 3) & 3, 3);
    SBAR();
    acc[3] = MFMA32(wA[3], cA, acc[3]); acc[4] = MFMA32(wA[4], cA, acc[4]);
    if (more) piece(j + 3, (slot + 3) & 3, 4);
    SBAR();
  };
  WAIT_VL_BAR(10); stage(0, 0, true, true);
  for (int j4 = 1; j4 < 13; j4 += 4) {
    WAIT_VL_BAR(10); stage(j4, 1, true, false);
    WAIT_VL_BAR(10); stage(j4 + 1, 2, true, false);
    WAIT_VL_BAR(10); stage(j4 + 2, 3, true, false);
    WAIT_VL_BAR(10); stage(j4 + 3, 0, true, false);
  }
  WAIT_VL_BAR(10); stage(13, 1, false, false);
  WAIT_VL_BAR(5); stage(14, 2, false, false);
  WAIT_VL_BAR(0); stage(15, 3, false, false);
#pragma unroll
  for (int n = 0; n < 5; ++n) acc[n] = MFMA32(wB[n], cB, acc[n]);
  STAMP();
  WAIT_L_BAR();
  {
    LAS char* mine = lds + (tbi * 2 + kp) * 12288 + lane * 16;
    LAS const char* theirs = lds + (tbi * 2 + (kp ^ 1)) * 12288 + lane * 16;
    auto put = [&](const f32x16& a, int slot) {
#pragma unroll
      for (int gq = 0; gq < 4; ++gq)
        *(LAS f32x4*)(mine + (slot * 4 + gq) * 1024) = (f32x4){a[4 * gq], a[4 * gq + 1], a[4 * gq + 2], a[4 * gq + 3]};
    };
    auto fin = [&](const f32x16& a, int slot, int n) {
      f32x16 t;
#pragma unroll
      for (int gq = 0; gq < 4; ++gq) {
        const f32x4 v = *(LAS const f32x4*)(theirs + (slot * 4 + gq) * 1024);
#pragma unroll
        for (int e = 0; e < 4; ++e) t[4 * gq + e] = a[4 * gq + e] + v[e];
      }
      u16* dst = PT + ((size_t)((((ks * 64 + panel * 4 + tbi) * 20 + nt * 5 + n) * 2) * 64 + lane)) * 8;
      *(u32x4*)dst = pack8(t, 0);
      *(u32x4*)(dst + 512) = pack8(t, 8);
    };
    if (kp == 0) { put(acc[3], 0); put(acc[4], 1); } else { put(acc[0], 0); put(acc[1], 1); put(acc[2], 2); }
    WAIT_L_BAR();
    if (kp == 0) { fin(acc[0], 0, 0); fin(acc[1], 1, 1); fin(acc[2], 2, 2); } else { fin(acc[3], 0, 3); fin(acc[4], 1, 4); }
  }
  STAMP();
  STAMP();
  STAMP_END(2, DBGP(PT, WS_PT));
}

__global__ __launch_bounds__(320) void k4_ln(const u16* __restrict__ x, const u16* __restrict__ PT,
                                             float* __restrict__ H, u16* __restrict__ HN, const u16* __restrict__ w1) {
  __shared__ __attribute__((aligned(16))) u16 tile[8 * 648];
  __shared__ float red[2][5][8];
  __shared__ __attribute__((aligned(16))) char dead[5 * 256];
  const int tid = threadIdx.x, lane = tid & 63, wid = RFL(tid >> 6);
  const int b = blockIdx.x;
  const int r8 = lane & 7, hh = (lane >> 3) & 1, sg = (lane >> 4) & 1, ml = lane >> 5;
  const int token = b * 8 + r8, tb32 = token >> 5, rr = token & 31;
  STAMP_DECL();
  STAMP();
  u32x2 xv[2][2];
  u32x4 pv[2][4];
#pragma unroll
  for (int i = 0; i < 2; ++i) {
    const int mb = wid * 4 + 2 * i + ml;
    const int m0 = mb * 32 + sg * 16 + hh * 4;
    xv[i][0] = *(const u32x2*)(x + (size_t)token * DM + m0);
    xv[i][1] = *(const u32x2*)(x + (size_t)token * DM + m0 + 8);
#pragma unroll
    for (int ks = 0; ks < 4; ++ks)
      pv[i][ks] = *(const u32x4*)(PT + ((size_t)((((ks * 64 + tb32) * 20 + mb) * 2 + sg) * 64 + hh * 32 + rr)) * 8);
  }
  SBAR();
  STAMP();
  float v[2][8];
  float s = 0.f;
#pragma unroll
  for (int i = 0; i < 2; ++i) {
    const int m0 = (wid * 4 + 2 * i + ml) * 32 + sg * 16 + hh * 4;
#pragma unroll
    for (int q = 0; q < 4; ++q) {
      const unsigned xw = xv[i][q >> 1][q & 1];
      v[i][2 * q] = bflo(xw) + ((bflo(pv[i][0][q]) + bflo(pv[i][1][q])) + (bflo(pv[i][2][q]) + bflo(pv[i][3][q])));
      v[i][2 * q + 1] = bfhi(xw) + ((bfhi(pv[i][0][q]) + bfhi(pv[i][1][q])) + (bfhi(pv[i][2][q]) + bfhi(pv[i][3][q])));
    }
    *(f32x4*)(H + (size_t)token * DM + m0) = (f32x4){v[i][0], v[i][1], v[i][2], v[i][3]};
    *(f32x4*)(H + (size_t)token * DM + m0 + 8) = (f32x4){v[i][4], v[i][5], v[i][6], v[i][7]};
#pragma unroll
    for (int e = 0; e < 8; ++e) s += v[i][e];
  }
  STAMP();
  {
    const int u = (b >> 3) * 5 + wid;
    const int a4 = (b & 7) >> 1;
    const unsigned dd = (unsigned)(uintptr_t)(LAS char*)dead + wid * 256;
    const __amdgpu_buffer_rsrc_t r1 = mk_rsrc(w1, SEQ * FF * 2);
#pragma unroll
    for (int i = 0; i < 2; ++i) {
      const int line = (u + 160 * i) * 64 + lane;
      const unsigned off = line < 14336 ? (unsigned)((line / 7) * (FF * 2) + a4 * 896 + (line % 7) * 128) : 0x7ff00000u;
      warm8k(r1, off, 0u, dd);
    }
  }
  s += __shfl_xor(s, 8); s += __shfl_xor(s, 16); s += __shfl_xor(s, 32);
  if (lane < 8) red[0][wid][lane] = s;
  WAIT_L_BAR();
  STAMP();
  const float mean = ((red[0][0][r8] + red[0][1][r8]) + (red[0][2][r8] + red[0][3][r8]) + red[0][4][r8]) * (1.0f / DM);
  float sq = 0.f;
#pragma unroll
  for (int i = 0; i < 2; ++i)
#pragma unroll
    for (int e = 0; e < 8; ++e) { v[i][e] -= mean; sq += v[i][e] * v[i][e]; }
  sq += __shfl_xor(sq, 8); sq += __shfl_xor(sq, 16); sq += __shfl_xor(sq, 32);
  if (lane < 8) red[1][wid][lane] = sq;
  WAIT_L_BAR();
  const float var = ((red[1][0][r8] + red[1][1][r8]) + (red[1][2][r8] + red[1][3][r8]) + red[1][4][r8]) * (1.0f / DM);
  const float r1 = 1.0f / sqrtf(var + LN_EPS);
  const float r2 = 1.0f / sqrtf(var * r1 * r1 + LN_EPS);
  const float sc = r1 * r2;
#pragma unroll
  for (int i = 0; i < 2; ++i) {
    const int m0 = (wid * 4 + 2 * i + ml) * 32 + sg * 16 + hh * 4;
    *(u32x2*)(tile + r8 * 648 + m0) = (u32x2){cvtpk(v[i][0] * sc, v[i][1] * sc), cvtpk(v[i][2] * sc, v[i][3] * sc)};
    *(u32x2*)(tile + r8 * 648 + m0 + 8) = (u32x2){cvtpk(v[i][4] * sc, v[i][5] * sc), cvtpk(v[i][6] * sc, v[i][7] * sc)};
  }
  WAIT_L_BAR();
  STAMP();
  const int ks32 = b >> 2, g = b & 3;
#pragma unroll
  for (int i = 0; i < 2; ++i) {
    const int m = tid + 320 * i;
    u16 e[8];
#pragma unroll
    for (int j = 0; j < 8; ++j) e[j] = tile[j * 648 + m];
    u32x4 w = {(unsigned)e[0] | ((unsigned)e[1] << 16), (unsigned)e[2] | ((unsigned)e[3] << 16),
               (unsigned)e[4] | ((unsigned)e[5] << 16), (unsigned)e[6] | ((unsigned)e[7] << 16)};
    *(u32x4*)(HN + ((size_t)(((m >> 4) * 64 + ks32) * 64 + g * 16 + (m & 15))) * 8) = w;
  }
  STAMP();
  STAMP();
  STAMP_END(3, DBGP(HN, WS_HN));
}

constexpr int K56_LDS = 163840;

DI float gelu_tanh(float x) {
  const float u = 0.7978845608028654f * (x + 0.044715f * x * x * x);
  const float e = __builtin_amdgcn_exp2f(u * 2.8853900817779268f);
  const float t = 1.0f - 2.0f * __builtin_amdgcn_rcpf(e + 1.0f);
  return 0.5f * x * (1.0f + t);
}

#define ACLOB56 "a0","a1","a2","a3","a4","a5","a6","a7","a8","a9","a10","a11","a12","a13","a14","a15","a16","a17","a18","a19","a20","a21","a22","a23","a24","a25","a26","a27","a28","a29","a30","a31","a32","a33","a34","a35","a36","a37","a38","a39","a40","a41","a42","a43","a44","a45","a46","a47","a48","a49","a50","a51","a52","a53","a54","a55","a56","a57","a58","a59","a60","a61","a62","a63","a64","a65","a66","a67","a68","a69","a70","a71","a72","a73","a74","a75","a76","a77","a78","a79","a80","a81","a82","a83","a84","a85","a86","a87","a88","a89","a90","a91"
template <int R> DI void m_aload(__amdgpu_buffer_rsrc_t rsrc, unsigned voff, unsigned soff) {
  asm volatile("buffer_load_dwordx4 a[%c3:%c4], %0, %1, %2 offen" ::"v"(voff), "s"(rsrc), "s"(soff), "i"(R), "i"(R + 3) : "memory");
}
template <int R, int OFF> DI void m_trread(unsigned addr) {
  asm volatile("ds_read_b64_tr_b16 a[%c1:%c2], %0 offset:%c3" ::"v"(addr), "i"(R), "i"(R + 1), "i"(OFF) : "memory");
}
template <int RA, int RB> DI void m_mfma(f32x4& d) {
  asm volatile("v_mfma_f32_16x16x32_bf16 %0, a[%c1:%c2], a[%c3:%c4], %0" : "+v"(d) : "i"(RA), "i"(RA + 3), "i"(RB), "i"(RB + 3));
}

template <int MODE>
__global__ __launch_bounds__(512, 2) void k56_mix(const u16* __restrict__ W, const u16* __restrict__ AF,
                                                  const u16* __restrict__ bias, const float* __restrict__ H,
                                                  u16* __restrict__ outp, const u16* __restrict__ wnext) {
  constexpr int LDW = MODE == 0 ? FF : SEQ;
  constexpr int NKS = MODE == 0 ? 64 : 54;
  constexpr int NK = MODE == 0 ? 8 : 7;
  constexpr int KROWS = MODE == 0 ? SEQ : FF;
  extern __shared__ __attribute__((aligned(16))) char smem[];
  LAS char* lds = (LAS char*)smem;
  const unsigned lds0 = (unsigned)(uintptr_t)lds;
  const int tid = threadIdx.x, lane = tid & 63;
  const int wid = RFL(tid >> 6);
  const int b = blockIdx.x, xcd = b & 7, idx = b >> 3;
  int wt, dt;
  if (MODE == 0) {
    wt = (xcd >> 1) * 7 + (idx >> 2); dt = (xcd & 1) * 4 + (idx & 3);
    if (idx >= 28 || wt >= 27) {
      if (idx >= 28) {
        const __amdgpu_buffer_rsrc_t r2 = mk_rsrc(wnext, SEQ * FF * 2);
#pragma unroll
        for (int i = 0; i < 7; ++i) {
          const int line = (((idx - 28) * 8 + wid) * 7 + i) * 64 + lane;
          const unsigned off = line < 13824 ? (unsigned)((line >> 3) * 4096 + (xcd >> 1) * 1024 + (line & 7) * 128) : 0x7ff00000u;
          warm8k(r2, off, 0u, lds0 + wid * 256);
        }
      }
      return;
    }
  } else {
    wt = (xcd >> 1) * 8 + (idx >> 2); dt = (xcd & 1) * 4 + (idx & 3);
  }
  const __amdgpu_buffer_rsrc_t rW = mk_rsrc(W, KROWS * LDW * 2);
  const __amdgpu_buffer_rsrc_t rA = mk_rsrc(AF, 40 * NKS * 1024);
  const unsigned vlin = lane * 16;
  const int prow = (lane >> 2) & 7;
  const unsigned voffW0 = (unsigned)(prow * LDW * 2 + (lane >> 5) * 64 +
                                     (((lane & 3) ^ (MODE == 1 ? (((prow >> 2) & 1) << 1) : 0)) << 4));
  const unsigned voffW1 = MODE == 1 ? voffW0 : (voffW0 ^ 32u);
  const unsigned myl = lds0 + wid * 20480;
  const int g = lane >> 4, q4 = (lane & 15) >> 2, p4 = lane & 3;
  unsigned wb[2][2];
#pragma unroll
  for (int t = 0; t < 2; ++t)
#pragma unroll
    for (int hf = 0; hf < 2; ++hf) {
      const int kb = MODE == 0 ? g : 2 * t + (g >> 1);
      const int row = MODE == 0 ? 4 * t + q4 : 4 * (g & 1) + q4;
      const int sw = MODE == 0 ? (kb & 1) : ((row >> 2) & 1);
      wb[t][hf] = myl + kb * 1024 + row * 64 + ((hf ^ sw) * 32) + p4 * 8;
    }
  f32x4 acc[4][5];
#pragma unroll
  for (int i = 0; i < 4; ++i)
#pragma unroll
    for (int d = 0; d < 5; ++d) acc[i][d] = (f32x4){0.f, 0.f, 0.f, 0.f};
  STAMP_DECL();
  STAMP();
  u32x2 eb0[2], eb1[2];
  f32x4 eh[3]; u16 ebs[3];
  if (MODE == 0) {
#pragma unroll
    for (int i = 0; i < 2; ++i) {
      const int u = wid + 8 * i < 10 ? wid + 8 * i : wid;
      const int f0 = wt * 64 + (u / 5) * 32 + 4 * g;
      eb0[i] = *(const u32x2*)(bias + f0); eb1[i] = *(const u32x2*)(bias + f0 + 16);
    }
  } else {
#pragma unroll
    for (int i = 0; i < 3; ++i) {
      const int u = wid + 8 * i < 20 ? wid + 8 * i : wid;
      const int s = wt * 64 + (u / 5) * 16 + (lane & 15), d0 = dt * 80 + (u % 5) * 16 + 4 * g;
      ebs[i] = bias[s];
      eh[i] = *(const f32x4*)(H + (size_t)s * DM + d0);
    }
  }
  SBAR();
#define KSTEP(IT) (wid + 8 * (IT))
#define WDMA(IT, KB) dma1(rW, ((KB) & 1) ? voffW1 : voffW0, (unsigned)((KSTEP(IT) * 32 + (KB) * 8) * LDW * 2 + wt * 128), myl + ((IT) % 5) * 4096 + (KB) * 1024)
#define ALOAD(IT, D) m_aload<((IT) % 3) * 20 + 4 * (D)>(rA, vlin, (unsigned)(((dt * 5 + (D)) * NKS + KSTEP(IT)) * 1024))
#define WDMA4(IT) do { WDMA(IT, 0); WDMA(IT, 1); WDMA(IT, 2); WDMA(IT, 3); } while (0)
#define ALOAD5(IT) do { ALOAD(IT, 0); ALOAD(IT, 1); ALOAD(IT, 2); ALOAD(IT, 3); ALOAD(IT, 4); } while (0)
#define WREAD1(IT, I) do { m_trread<60 + 16 * ((IT) & 1) + 4 * (I), ((IT) % 5) * 4096 + ((I) >> 1) * 512>(wb[0][(I) & 1]); \
                           m_trread<60 + 16 * ((IT) & 1) + 4 * (I) + 2, ((IT) % 5) * 4096 + ((I) >> 1) * 512>(wb[1][(I) & 1]); } while (0)
#define WREAD(IT) do { WREAD1(IT, 0); WREAD1(IT, 1); WREAD1(IT, 2); WREAD1(IT, 3); } while (0)
#define MM(IT, I, D) do { if constexpr (MODE == 0) m_mfma<60 + 16 * ((IT) & 1) + 4 * (I), ((IT) % 3) * 20 + 4 * (D)>(acc[I][D]); \
                          else m_mfma<((IT) % 3) * 20 + 4 * (D), 60 + 16 * ((IT) & 1) + 4 * (I)>(acc[I][D]); } while (0)
#define MMROW(IT, I) do { MM(IT, I, 0); MM(IT, I, 1); MM(IT, I, 2); MM(IT, I, 3); MM(IT, I, 4); } while (0)
#define NV(IT) (((IT) + 4 < NK ? 4 : 0) + ((IT) + 2 < NK ? 5 : 0))
#define ALLOW(IT) ((IT) == 0 ? (5 + (NK > 2 ? 4 : 0) + (NK > 3 ? 4 : 0)) : (IT) == 1 ? ((NK > 3 ? 4 : 0) + NV(0)) : NV((IT) - 1))
#define STEP(IT) do { if constexpr ((IT) < NK) { \
    asm volatile("s_waitcnt vmcnt(%c0) lgkmcnt(0)" ::"i"(ALLOW(IT)) : "memory", ACLOB56); \
    if constexpr ((IT) + 1 < NK) WREAD((IT) + 1); \
    SBAR(); \
    MMROW(IT, 0); if constexpr ((IT) + 4 < NK) { WDMA((IT) + 4, 0); WDMA((IT) + 4, 1); } SBAR(); \
    MMROW(IT, 1); if constexpr ((IT) + 4 < NK) { WDMA((IT) + 4, 2); WDMA((IT) + 4, 3); } SBAR(); \
    MMROW(IT, 2); if constexpr ((IT) + 2 < NK) { ALOAD((IT) + 2, 0); ALOAD((IT) + 2, 1); ALOAD((IT) + 2, 2); } SBAR(); \
    MMROW(IT, 3); if constexpr ((IT) + 2 < NK) { ALOAD((IT) + 2, 3); ALOAD((IT) + 2, 4); } SBAR(); \
    } } while (0)
  asm volatile("" ::: "memory", ACLOB56);
  WDMA4(0); ALOAD5(0); asm volatile("s_barrier" ::: "memory");
  WDMA4(1); ALOAD5(1); asm volatile("s_barrier" ::: "memory");
  WDMA4(2); WDMA4(3);
  asm volatile("s_waitcnt vmcnt(22)" ::: "memory");
  WREAD(0);
  STAMP();
  STEP(0); STEP(1); STEP(2); STEP(3); STEP(4); STEP(5); STEP(6); STEP(7);
  asm volatile("s_nop 15" ::: "memory");
  STAMP();
#undef KSTEP
#undef WDMA
#undef ALOAD
#undef WDMA4
#undef ALOAD5
#undef WREAD1
#undef WREAD
#undef MM
#undef MMROW
#undef NV
#undef ALLOW
#undef STEP
  WAIT_L0();
  STAMP();
  {
    LAS char* dst = lds + wid * 20480 + lane * 16;
#pragma unroll
    for (int i = 0; i < 4; ++i)
#pragma unroll
      for (int d = 0; d < 5; ++d) *(LAS f32x4*)(dst + (i * 5 + d) * 1024) = acc[i][d];
  }
  WAIT_L_BAR();
  if (MODE == 0) {
#pragma unroll
    for (int i = 0; i < 2; ++i) {
      const int u = wid + 8 * i;
      if (u < 10) {
        const int a = u / 5, d = u % 5;
        f32x4 t0 = {0.f, 0.f, 0.f, 0.f}, t1 = {0.f, 0.f, 0.f, 0.f};
#pragma unroll
        for (int w = 0; w < 8; ++w) {
          t0 += *(LAS const f32x4*)(lds + w * 20480 + ((2 * a) * 5 + d) * 1024 + lane * 16);
          t1 += *(LAS const f32x4*)(lds + w * 20480 + ((2 * a + 1) * 5 + d) * 1024 + lane * 16);
        }
        const u32x2 b0 = eb0[i], b1 = eb1[i];
        t0 += (f32x4){bflo(b0[0]), bfhi(b0[0]), bflo(b0[1]), bfhi(b0[1])};
        t1 += (f32x4){bflo(b1[0]), bfhi(b1[0]), bflo(b1[1]), bfhi(b1[1])};
        u32x4 w = {cvtpk(gelu_tanh(t0[0]), gelu_tanh(t0[1])), cvtpk(gelu_tanh(t0[2]), gelu_tanh(t0[3])),
                   cvtpk(gelu_tanh(t1[0]), gelu_tanh(t1[1])), cvtpk(gelu_tanh(t1[2]), gelu_tanh(t1[3]))};
        *(u32x4*)(outp + ((size_t)(((dt * 5 + d) * 54 + wt * 2 + a) * 64 + lane)) * 8) = w;
      }
    }
  } else {
#pragma unroll
    for (int i = 0; i < 3; ++i) {
      const int u = wid + 8 * i;
      if (u < 20) {
        f32x4 t0 = {0.f, 0.f, 0.f, 0.f};
#pragma unroll
        for (int w = 0; w < 8; ++w) t0 += *(LAS const f32x4*)(lds + w * 20480 + u * 1024 + lane * 16);
        const int s = wt * 64 + (u / 5) * 16 + (lane & 15);
        const int d0 = dt * 80 + (u % 5) * 16 + 4 * g;
        const float bb = bf2f(ebs[i]);
        const f32x4 hv = eh[i];
        u32x2 w = {cvtpk(t0[0] + bb + hv[0], t0[1] + bb + hv[1]), cvtpk(t0[2] + bb + hv[2], t0[3] + bb + hv[3])};
        *(u32x2*)(outp + (size_t)s * DM + d0) = w;
      }
    }
  }
  STAMP();
  STAMP_END(4 + MODE, DBGP(AF, MODE == 0 ? WS_HN : WS_UF));
}

}

extern "C" void kernel_launch(void* const* d_in, const int* in_sizes, int n_in, void* d_out, int out_size, void* d_ws,
                              size_t ws_size, hipStream_t stream) {
  (void)in_sizes; (void)n_in; (void)out_size; (void)ws_size;
  const u16* x = (const u16*)d_in[0];
  const u16* ckv = (const u16*)d_in[1];
  const u16* wq = (const u16*)d_in[2];
  const u16* wuk = (const u16*)d_in[3];
  const u16* wuv = (const u16*)d_in[4];
  const u16* wo = (const u16*)d_in[5];
  const u16* w1 = (const u16*)d_in[7];
  const u16* b1 = (const u16*)d_in[8];
  const u16* w2 = (const u16*)d_in[9];
  const u16* b2 = (const u16*)d_in[10];
  char* ws = (char*)d_ws;
  u16* XN = (u16*)(ws + WS_XN);
  u16* KF = (u16*)(ws + WS_KF);
  u16* VF = (u16*)(ws + WS_VF);
  u16* CF = (u16*)(ws + WS_CF);
  u16* PT = (u16*)(ws + WS_PT);
  float* H = (float*)(ws + WS_H);
  u16* HN = (u16*)(ws + WS_HN);
  u16* UF = (u16*)(ws + WS_UF);
  (void)hipFuncSetAttribute((const void*)k1_kvproj, hipFuncAttributeMaxDynamicSharedMemorySize, K1_LDS);
  (void)hipFuncSetAttribute((const void*)k2_attn, hipFuncAttributeMaxDynamicSharedMemorySize, K2_LDS);
  (void)hipFuncSetAttribute((const void*)k3_oproj, hipFuncAttributeMaxDynamicSharedMemorySize, K3_LDS);
  (void)hipFuncSetAttribute((const void*)k56_mix<0>, hipFuncAttributeMaxDynamicSharedMemorySize, K56_LDS);
  (void)hipFuncSetAttribute((const void*)k56_mix<1>, hipFuncAttributeMaxDynamicSharedMemorySize, K56_LDS);
  k1_kvproj<<<256, 512, K1_LDS, stream>>>(x, ckv, wuk, wuv, KF, VF, XN, wq);
  k2_attn<<<256, 512, K2_LDS, stream>>>(XN, wq, KF, VF, CF, (float*)(ws + WS_LAM), wo);
  k3_oproj<<<256, 512, K3_LDS, stream>>>(CF, wo, PT);
  k4_ln<<<256, 320, 0, stream>>>(x, PT, H, HN, w1);
  k56_mix<0><<<256, 512, K56_LDS, stream>>>(w1, HN, b1, nullptr, UF, w2);
  k56_mix<1><<<256, 512, K56_LDS, stream>>>(w2, UF, b2, H, (u16*)d_out, nullptr);
}
```

```cpp
#include <hip/hip_runtime.h>
#include <stdint.h>
#include <utility>

namespace {

typedef unsigned short u16;
typedef __bf16 bf16x8 __attribute__((ext_vector_type(8)));
typedef __bf16 bf16x2 __attribute__((ext_vector_type(2)));
typedef short s16x4 __attribute__((ext_vector_type(4)));
typedef short s16x8 __attribute__((ext_vector_type(8)));
typedef float f32x2 __attribute__((ext_vector_type(2)));
typedef float f32x4 __attribute__((ext_vector_type(4)));
typedef float f32x16 __attribute__((ext_vector_type(16)));
typedef unsigned u32x2 __attribute__((ext_vector_type(2)));
typedef unsigned u32x4 __attribute__((ext_vector_type(4)));
#define LAS __attribute__((address_space(3)))
#define DI __device__ __forceinline__
#define SBAR() __builtin_amdgcn_sched_barrier(0)
#define RFL(x) __builtin_amdgcn_readfirstlane(x)

constexpr int SEQ = 2048, DM = 640, DC = 512, NH = 32, HD = 128, FF = 1728;
constexpr float LN_EPS = 1e-5f;
constexpr float QSCALE = 0.08838834764831845f * 1.4426950408889634f;
constexpr float P_LIMIT = 1073741824.0f;
constexpr float NEG_BIG = -1e30f;

constexpr size_t WS_XN = 0;
constexpr size_t WS_KF = WS_XN + (size_t)SEQ * DM * 2;
constexpr size_t WS_VF = WS_KF + (size_t)NH * SEQ * HD * 2;
constexpr size_t WS_CF = WS_VF + (size_t)NH * SEQ * HD * 2;
constexpr size_t WS_PT = WS_CF + (size_t)NH * SEQ * HD * 2;
constexpr size_t WS_H = WS_PT + (size_t)4 * SEQ * DM * 2;
constexpr size_t WS_HN = WS_H + (size_t)SEQ * DM * 4;
constexpr size_t WS_UF = WS_HN + (size_t)SEQ * DM * 2;
constexpr size_t WS_LAM = WS_UF + (size_t)DM * FF * 2;
constexpr size_t WS_END = WS_LAM + 256 * 256 * 4;

DI __amdgpu_buffer_rsrc_t mk_rsrc(const void* p, unsigned bytes) {
  return __builtin_amdgcn_make_buffer_rsrc((void*)p, 0, bytes, 0x00020000);
}
DI void dma1(__amdgpu_buffer_rsrc_t rsrc, unsigned voff, unsigned soff, unsigned ldsaddr) {
  asm volatile("s_mov_b32 m0, %0\n\ts_nop 0\n\tbuffer_load_dwordx4 %1, %2, %3 offen lds"
               ::"s"(ldsaddr), "v"(voff), "s"(rsrc), "s"(soff) : "memory", "m0");
}
DI void warm8k(__amdgpu_buffer_rsrc_t rsrc, unsigned voff128, unsigned soff, unsigned ldsaddr) {
  asm volatile("s_mov_b32 m0, %0\n\ts_nop 0\n\tbuffer_load_dword %1, %2, %3 offen lds"
               ::"s"(ldsaddr), "v"(voff128), "s"(rsrc), "s"(soff) : "memory", "m0");
}
#define WAIT_VL_BAR(N) asm volatile("s_waitcnt vmcnt(" #N ") lgkmcnt(0)\n\ts_barrier" ::: "memory")
#define WAIT_L_BAR() asm volatile("s_waitcnt lgkmcnt(0)\n\ts_barrier" ::: "memory")
#define WAIT_L0() asm volatile("s_waitcnt lgkmcnt(0)" ::: "memory")

DI bf16x8 lds_b128(LAS const char* p) { return *(LAS const bf16x8*)p; }
DI bf16x8 lds_tr2(LAS const char* p0, LAS const char* p1) {
  s16x4 lo = __builtin_amdgcn_ds_read_tr16_b64_v4i16((LAS s16x4*)p0);
  s16x4 hi = __builtin_amdgcn_ds_read_tr16_b64_v4i16((LAS s16x4*)p1);
  s16x8 w = __builtin_shufflevector(lo, hi, 0, 1, 2, 3, 4, 5, 6, 7);
  return __builtin_bit_cast(bf16x8, w);
}
DI unsigned cvtpk(float lo, float hi) {
  f32x2 v = {lo, hi};
  bf16x2 b = __builtin_convertvector(v, bf16x2);
  return __builtin_bit_cast(unsigned, b);
}
DI u32x4 pack8(const f32x16& a, int o) {
  u32x4 w = {cvtpk(a[o], a[o + 1]), cvtpk(a[o + 2], a[o + 3]), cvtpk(a[o + 4], a[o + 5]), cvtpk(a[o + 6], a[o + 7])};
  return w;
}
DI float bf2f(unsigned short b) { return __uint_as_float(((unsigned)b) << 16); }
DI float bflo(unsigned w) { return __uint_as_float(w << 16); }
DI float bfhi(unsigned w) { return __uint_as_float(w & 0xffff0000u); }
#define MFMA32(a, b, c) __builtin_amdgcn_mfma_f32_32x32x16_bf16((a), (b), (c), 0, 0, 0)
#define MFMA16(a, b, c) __builtin_amdgcn_mfma_f32_16x16x32_bf16((a), (b), (c), 0, 0, 0)
#define STAMP_DECL()
#define STAMP()
#define STAMP_END(kid, dbg)
DI float swap_max(float v) {
  auto rr = __builtin_amdgcn_permlane32_swap(__float_as_uint(v), __float_as_uint(v), false, false);
  return fmaxf(__uint_as_float(rr[0]), __uint_as_float(rr[1]));
}
DI float swap_add(float v) {
  auto rr = __builtin_amdgcn_permlane32_swap(__float_as_uint(v), __float_as_uint(v), false, false);
  return __uint_as_float(rr[0]) + __uint_as_float(rr[1]);
}

constexpr int K1_LDS = 4 * 32768;

template <bool ISV>
DI void k1_body(LAS char* lds, unsigned lds0, int wid, int lane, int head, int tb, const u16* __restrict__ ckv,
                const u16* __restrict__ wsrc, const u16* __restrict__ x, u16* __restrict__ KF, u16* __restrict__ VF,
                u16* __restrict__ XN, const u16* __restrict__ wq
                ) {
  const int wm = wid >> 2, wn = wid & 3;
  const int r = lane & 31, h = lane >> 5;
  const int g1 = (lane >> 4) & 1, q = (lane & 15) >> 2, p = lane & 3;
  const __amdgpu_buffer_rsrc_t rA = mk_rsrc(ckv, SEQ * DC * 2);
  const __amdgpu_buffer_rsrc_t rWm = mk_rsrc(wsrc, DC * NH * HD * 2);
  const unsigned voffA = (lane >> 2) * 1024 + (((lane & 3) ^ ((lane >> 4) & 3)) << 4);
  const unsigned voffW = ((lane >> 2) & 7) * 8192 + (lane >> 5) * 64 + (lane & 3) * 16;
  auto piece = [&](int kt, int slot, int i) __attribute__((always_inline)) {
    const unsigned sb = lds0 + slot * 32768;
    if (i < 2) dma1(rA, voffA, (unsigned)((tb * 256 + (wid * 2 + i) * 16) * 1024 + kt * 64), sb + (wid * 2 + i) * 1024);
    else dma1(rWm, voffW, (unsigned)((kt * 32 + (wid >> 1) * 8) * 8192 + head * 256 + (i - 2) * 128),
              sb + 16384 + ((wid >> 1) * 4 + (wid & 1) * 2 + (i - 2)) * 1024);
  };
  unsigned abase[2];
#pragma unroll
  for (int s = 0; s < 2; ++s) abase[s] = wm * 8192 + r * 64 + (((2 * s + h) ^ ((r >> 2) & 3)) << 4);
  const unsigned wbase = 16384 + h * 4096 + wn * 1024 + q * 64 + g1 * 32 + p * 8;

  f32x16 acc[4][2];
#pragma unroll
  for (int i = 0; i < 4; ++i)
#pragma unroll
    for (int n = 0; n < 2; ++n) acc[i][n] = f32x16{};
  auto rd = [&](bf16x8 (&a)[4], bf16x8 (&w)[2], LAS const char* sb, int s) {
#pragma unroll
    for (int n = 0; n < 2; ++n) w[n] = lds_tr2(sb + wbase + s * 8192 + n * 512, sb + wbase + s * 8192 + n * 512 + 256);
#pragma unroll
    for (int i = 0; i < 4; ++i) a[i] = lds_b128(sb + abase[s] + i * 2048);
  };
  auto mm2 = [&](const bf16x8 (&a)[4], const bf16x8 (&w)[2], int i) {
#pragma unroll
    for (int n = 0; n < 2; ++n) {
      if (ISV) acc[i][n] = MFMA32(a[i], w[n], acc[i][n]);
      else acc[i][n] = MFMA32(w[n], a[i], acc[i][n]);
    }
  };
  bf16x8 aA[4], wA[2], aB[4], wB[2];
  STAMP_DECL();
  STAMP();
#pragma unroll
  for (int kt = 0; kt < 3; ++kt)
#pragma unroll
    for (int i = 0; i < 4; ++i) piece(kt, kt, i);
  auto stage = [&](const int kt, const int slot, const bool more, const bool first) __attribute__((always_inline)) {
    STAMP();
    LAS const char* sb = lds + slot * 32768;
    rd(aA, wA, sb, 0);
    SBAR();
    if (!first) {
      mm2(aB, wB, 0); mm2(aB, wB, 1);
      if (more) piece(kt + 3, (slot + 3) & 3, 0);
      SBAR();
      mm2(aB, wB, 2); mm2(aB, wB, 3);
      if (more) piece(kt + 3, (slot + 3) & 3, 1);
    } else {
      piece(3, 3, 0); piece(3, 3, 1);
    }
    SBAR();
    rd(aB, wB, sb, 1);
    SBAR();
    mm2(aA, wA, 0); mm2(aA, wA, 1);
    if (more) piece(kt + 3, (slot + 3) & 3, 2);
    SBAR();
    mm2(aA, wA, 2); mm2(aA, wA, 3);
    if (more) piece(kt + 3, (slot + 3) & 3, 3);
    SBAR();
  };
  WAIT_VL_BAR(8); stage(0, 0, true, true);
  for (int k4 = 1; k4 < 13; k4 += 4) {
    WAIT_VL_BAR(8); stage(k4, 1, true, false);
    WAIT_VL_BAR(8); stage(k4 + 1, 2, true, false);
    WAIT_VL_BAR(8); stage(k4 + 2, 3, true, false);
    WAIT_VL_BAR(8); stage(k4 + 3, 0, true, false);
  }
  WAIT_VL_BAR(8); stage(13, 1, false, false);
  WAIT_VL_BAR(4); stage(14, 2, false, false);
  WAIT_VL_BAR(0); stage(15, 3, false, false);
  mm2(aB, wB, 0); mm2(aB, wB, 1); mm2(aB, wB, 2); mm2(aB, wB, 3);
  STAMP();
  {
    const __amdgpu_buffer_rsrc_t rQ = mk_rsrc(wq, DM * NH * HD * 2);
    const int u = (blockIdx.x >> 3) * 8 + wid;
    if (u < 80) warm8k(rQ, (unsigned)((lane >> 3) * 8192 + (lane & 7) * 128), (unsigned)(u * 8 * 8192 + (blockIdx.x & 7) * 1024), lds0);
  }
  const int token = blockIdx.x * 8 + wid;
  const bool has1 = lane < 16;
  const u32x4 v0 = *(const u32x4*)(x + (size_t)token * DM + lane * 8);
  u32x4 v1 = {0u, 0u, 0u, 0u};
  if (has1) v1 = *(const u32x4*)(x + (size_t)token * DM + 512 + lane * 8);
  SBAR();
#pragma unroll
  for (int i = 0; i < 4; ++i) {
    const int kb32 = tb * 8 + wm * 4 + i;
#pragma unroll
    for (int n = 0; n < 2; ++n) {
#pragma unroll
      for (int sg = 0; sg < 2; ++sg) {
        const u32x4 w = pack8(acc[i][n], 8 * sg);
        if (ISV) {
          const int db = 2 * (wn - 2) + n;
          *(u32x4*)(VF + ((size_t)((((head * 64 + kb32) * 4 + db) * 2 + sg) * 64 + lane)) * 8) = w;
        } else {
          const int db = 2 * wn + n;
          *(u32x4*)(KF + ((size_t)(((head * 64 + kb32) * 8 + 2 * db + sg) * 64 + lane)) * 8) = w;
        }
      }
    }
  }
  STAMP();
  {
    float f0[8], f1[8];
#pragma unroll
    for (int j = 0; j < 4; ++j) {
      f0[2 * j] = bflo(v0[j]); f0[2 * j + 1] = bfhi(v0[j]);
      f1[2 * j] = bflo(v1[j]); f1[2 * j + 1] = bfhi(v1[j]);
    }
    float s = 0.f;
#pragma unroll
    for (int j = 0; j < 8; ++j) s += f0[j] + f1[j];
#pragma unroll
    for (int o = 1; o < 64; o <<= 1) s += __shfl_xor(s, o);
    const float mean = s * (1.0f / DM);
    float sq = 0.f;
#pragma unroll
    for (int j = 0; j < 8; ++j) {
      const float d0 = f0[j] - mean; sq += d0 * d0;
      const float d1 = has1 ? f1[j] - mean : 0.f; sq += d1 * d1;
    }
#pragma unroll
    for (int o = 1; o < 64; o <<= 1) sq += __shfl_xor(sq, o);
    const float rstd = 1.0f / sqrtf(sq * (1.0f / DM) + LN_EPS);
    const int tb32 = token >> 5, rr = token & 31;
    u32x4 o0, o1;
#pragma unroll
    for (int j = 0; j < 4; ++j) {
      o0[j] = cvtpk((f0[2 * j] - mean) * rstd, (f0[2 * j + 1] - mean) * rstd);
      o1[j] = cvtpk((f1[2 * j] - mean) * rstd, (f1[2 * j + 1] - mean) * rstd);
    }
    *(u32x4*)(XN + ((size_t)((tb32 * 40 + (lane >> 1)) * 64 + (lane & 1) * 32 + rr)) * 8) = o0;
    if (has1) *(u32x4*)(XN + ((size_t)((tb32 * 40 + 32 + (lane >> 1)) * 64 + (lane & 1) * 32 + rr)) * 8) = o1;
  }
  STAMP();
  STAMP();
  STAMP_END(0, dbgp);
}

__global__ __launch_bounds__(512, 2) void k1_kvproj(const u16* __restrict__ x, const u16* __restrict__ ckv,
                                                    const u16* __restrict__ wuk, const u16* __restrict__ wuv,
                                                    u16* __restrict__ KF, u16* __restrict__ VF, u16* __restrict__ XN,
                                                    const u16* __restrict__ wq) {
  extern __shared__ __attribute__((aligned(16))) char smem[];
  LAS char* lds = (LAS char*)smem;
  const unsigned lds0 = (unsigned)(uintptr_t)lds;
  const int tid = threadIdx.x, lane = tid & 63;
  const int wid = RFL(tid >> 6);
  const int b = blockIdx.x, xcd = b & 7, idx = b >> 3;
  const int head = xcd * 4 + (idx & 3), tb = idx >> 2;
  const u16* wsrc = (wid & 1) ? wuv : wuk;
#define K1_EXTRA
  if ((wid & 3) >= 2) k1_body<true>(lds, lds0, wid, lane, head, tb, ckv, wsrc, x, KF, VF, XN, wq K1_EXTRA);
  else k1_body<false>(lds, lds0, wid, lane, head, tb, ckv, wsrc, x, KF, VF, XN, wq K1_EXTRA);
}

constexpr int K2_LDS = 163840;
constexpr unsigned L_KB1 = 0, L_VB0 = 16384, L_VA1 = 32768, L_KA0 = 49152, L_KB0 = 65536, L_KA1 = 81920, L_VA0 = 98304,
                   L_VB1 = 114688, L_STASH = 131072;
constexpr int NSTEP = 17;
#define SPRIO 2
#define MPRIO 0
#define TRAILPRIO SPRIO

template <int OFF>
DI void dma1o(__amdgpu_buffer_rsrc_t rsrc, unsigned voff, unsigned soff, unsigned ldsaddr) {
  asm volatile("s_mov_b32 m0, %0\n\ts_nop 0\n\tbuffer_load_dwordx4 %1, %2, %3 offen offset:%4 lds"
               ::"s"(ldsaddr), "v"(voff), "s"(rsrc), "s"(soff), "i"(OFF) : "memory", "m0");
}
DI float fmul_s(float a, float b) { float r; asm("v_mul_f32 %0, %1, %2" : "=v"(r) : "v"(a), "v"(b)); return r; }
#define ACLOB "a8","a9","a10","a11","a12","a13","a14","a15","a16","a17","a18","a19","a20","a21","a22","a23","a24","a25","a26","a27","a28","a29","a30","a31","a32","a33","a34","a35","a36","a37","a38","a39","a40","a41","a42","a43","a44","a45","a46","a47","a48","a49","a50","a51","a52","a53","a54","a55","a56","a57","a58","a59","a60","a61","a62","a63","a64","a65","a66","a67","a68","a69","a70","a71","a72","a73","a74","a75","a76","a77","a78","a79","a80","a81","a82","a83","a84","a85","a86","a87","a88","a89","a90","a91","a92","a93","a94","a95","a96","a97","a98","a99","a100","a101","a102","a103","a104","a105","a106","a107","a108","a109","a110","a111","a112","a113","a114","a115","a116","a117","a118","a119","a120","a121","a122","a123","a124","a125","a126","a127"
constexpr int A_O = 8, A_Q = 72, A_R = 104, NRING = 6;
template <int SLOT, int OFF> DI void ring_read(unsigned addr) {
  asm volatile("ds_read_b128 a[%c1:%c2], %0 offset:%c3" ::"v"(addr), "i"(A_R + 4 * SLOT), "i"(A_R + 4 * SLOT + 3), "i"(OFF) : "memory");
}
template <int S, int OFF> DI void q_read(unsigned addr) {
  asm volatile("ds_read_b128 a[%c1:%c2], %0 offset:%c3" ::"v"(addr), "i"(A_Q + 4 * S), "i"(A_Q + 4 * S + 3), "i"(OFF) : "memory");
}
constexpr int V_NEGM = 64, V_S0 = 80, V_S1 = 96, V_PF = 112;
template <int W, int SLOT, int S, int DST> DI void qk_head() {
  asm volatile("s_waitcnt lgkmcnt(%c0)\n\tv_mfma_f32_32x32x16_bf16 v[%c1:%c2], a[%c3:%c4], a[%c5:%c6], v[%c7:%c8]"
               ::"i"(W), "i"(DST), "i"(DST + 15), "i"(A_R + 4 * SLOT), "i"(A_R + 4 * SLOT + 3), "i"(A_Q + 4 * S), "i"(A_Q + 4 * S + 3),
                 "i"(V_NEGM), "i"(V_NEGM + 15));
}
template <int W, int SLOT, int S, int DST> DI void qk_mfma() {
  asm volatile("s_waitcnt lgkmcnt(%c0)\n\tv_mfma_f32_32x32x16_bf16 v[%c1:%c2], a[%c3:%c4], a[%c5:%c6], v[%c1:%c2]"
               ::"i"(W), "i"(DST), "i"(DST + 15), "i"(A_R + 4 * SLOT), "i"(A_R + 4 * SLOT + 3), "i"(A_Q + 4 * S), "i"(A_Q + 4 * S + 3));
}
template <int W, int SLOT, int D, int P> DI void pv_mfma() {
  asm volatile("s_waitcnt lgkmcnt(%c0)\n\tv_mfma_f32_32x32x16_bf16 a[%c1:%c2], a[%c3:%c4], v[%c5:%c6], a[%c1:%c2]"
               ::"i"(W), "i"(A_O + 16 * D), "i"(A_O + 16 * D + 15), "i"(A_R + 4 * SLOT), "i"(A_R + 4 * SLOT + 3), "i"(V_PF + 4 * P),
                 "i"(V_PF + 4 * P + 3));
}
template <int W, int SLOT, int D, int P> DI void pv_zero() {
  asm volatile("s_waitcnt lgkmcnt(%c0)\n\tv_mfma_f32_32x32x16_bf16 a[%c1:%c2], a[%c3:%c4], v[%c5:%c6], 0"
               ::"i"(W), "i"(A_O + 16 * D), "i"(A_O + 16 * D + 15), "i"(A_R + 4 * SLOT), "i"(A_R + 4 * SLOT + 3), "i"(V_PF + 4 * P),
                 "i"(V_PF + 4 * P + 3));
}
template <int S, int P> DI void exps8(float& ps0, float& ps1, float& ps2, float& ps3) {
  float t0, t1, t2, t3, t4, t5, t6, t7;
  asm volatile("v_exp_f32 %4, v[%c12]\n\tv_exp_f32 %5, v[%c12+1]\n\tv_exp_f32 %6, v[%c12+2]\n\tv_exp_f32 %7, v[%c12+3]\n\t"
               "v_exp_f32 %8, v[%c12+4]\n\tv_exp_f32 %9, v[%c12+5]\n\tv_exp_f32 %10, v[%c12+6]\n\tv_exp_f32 %11, v[%c12+7]\n\t"
               "v_add_f32 %0, %0, %4\n\tv_add_f32 %1, %1, %5\n\tv_cvt_pk_bf16_f32 v[%c13], %4, %5\n\t"
               "v_add_f32 %2, %2, %6\n\tv_add_f32 %3, %3, %7\n\tv_cvt_pk_bf16_f32 v[%c13+1], %6, %7\n\t"
               "v_add_f32 %0, %0, %8\n\tv_add_f32 %1, %1, %9\n\tv_cvt_pk_bf16_f32 v[%c13+2], %8, %9\n\t"
               "v_add_f32 %2, %2, %10\n\tv_add_f32 %3, %3, %11\n\tv_cvt_pk_bf16_f32 v[%c13+3], %10, %11"
               : "+v"(ps0), "+v"(ps1), "+v"(ps2), "+v"(ps3), "=&v"(t0), "=&v"(t1), "=&v"(t2), "=&v"(t3), "=&v"(t4), "=&v"(t5),
                 "=&v"(t6), "=&v"(t7)
               : "i"(S), "i"(V_PF + 4 * P));
}
DI float exps32() {
  float ps0 = 0.f, ps1 = 0.f, ps2 = 0.f, ps3 = 0.f, r;
  exps8<V_S0, 0>(ps0, ps1, ps2, ps3); exps8<V_S0 + 8, 1>(ps0, ps1, ps2, ps3);
  exps8<V_S1, 2>(ps0, ps1, ps2, ps3); exps8<V_S1 + 8, 3>(ps0, ps1, ps2, ps3);
  asm volatile("v_add_f32 %1, %1, %2\n\tv_add_f32 %3, %3, %4\n\tv_add_f32 %0, %1, %3" : "=v"(r), "+v"(ps0), "+v"(ps1), "+v"(ps2), "+v"(ps3));
  return r;
}
DI float rowmax32() {
  float a, b;
  asm volatile("v_max3_f32 %0, v80, v81, v82\n\tv_max3_f32 %1, v83, v84, v85\n\tv_max3_f32 %0, %0, v86, v87\n\tv_max3_f32 %1, %1, v88, v89\n\t"
               "v_max3_f32 %0, %0, v90, v91\n\tv_max3_f32 %1, %1, v92, v93\n\tv_max3_f32 %0, %0, v94, v95\n\tv_max3_f32 %1, %1, v96, v97\n\t"
               "v_max3_f32 %0, %0, v98, v99\n\tv_max3_f32 %1, %1, v100, v101\n\tv_max3_f32 %0, %0, v102, v103\n\tv_max3_f32 %1, %1, v104, v105\n\t"
               "v_max3_f32 %0, %0, v106, v107\n\tv_max3_f32 %1, %1, v108, v109\n\tv_max3_f32 %0, %0, v110, v111\n\tv_max_f32 %0, %0, %1"
               : "=&v"(a), "=&v"(b));
  return a;
}
template <int R> DI void vreg_add(float t) { asm volatile("v_add_f32 v[%c1], v[%c1], %0" ::"v"(t), "i"(R)); }
template <int S, int... E> DI void mask_all_(float ninf, std::integer_sequence<int, E...>) { (vreg_add<S + E>(ninf), ...); }
template <int S, int... E> DI void mask_tri_(int dq, float ninf, std::integer_sequence<int, E...>) {
  (vreg_add<S + E>(((E & 3) + 8 * (E >> 2) > dq) ? ninf : 0.f), ...);
}
template <int I> DI float acc_rd() { float r; asm volatile("v_accvgpr_read_b32 %0, a[%c1]" : "=v"(r) : "i"(I)); return r; }
template <int I> DI void acc_wr(float v) { asm volatile("v_accvgpr_write_b32 a[%c1], %0" ::"v"(v), "i"(I)); }
template <int... E> DI void o_scale_(float f, std::integer_sequence<int, E...>) { (acc_wr<A_O + E>(fmul_s(acc_rd<A_O + E>(), f)), ...); }
template <int R> DI u32x4 acc_scale8(float f) {
  u32x4 o; float t0, t1, t2, t3, t4, t5, t6, t7;
  asm volatile("v_accvgpr_read_b32 %4, a[%c13]\n\tv_accvgpr_read_b32 %5, a[%c13+1]\n\tv_accvgpr_read_b32 %6, a[%c13+2]\n\t"
               "v_accvgpr_read_b32 %7, a[%c13+3]\n\tv_accvgpr_read_b32 %8, a[%c13+4]\n\tv_accvgpr_read_b32 %9, a[%c13+5]\n\t"
               "v_accvgpr_read_b32 %10, a[%c13+6]\n\tv_accvgpr_read_b32 %11, a[%c13+7]\n\t"
               "v_mul_f32 %4, %4, %12\n\tv_mul_f32 %5, %5, %12\n\tv_mul_f32 %6, %6, %12\n\tv_mul_f32 %7, %7, %12\n\t"
               "v_mul_f32 %8, %8, %12\n\tv_mul_f32 %9, %9, %12\n\tv_mul_f32 %10, %10, %12\n\tv_mul_f32 %11, %11, %12\n\t"
               "v_cvt_pk_bf16_f32 %0, %4, %5\n\tv_cvt_pk_bf16_f32 %1, %6, %7\n\tv_cvt_pk_bf16_f32 %2, %8, %9\n\tv_cvt_pk_bf16_f32 %3, %10, %11"
               : "=&v"(o[0]), "=&v"(o[1]), "=&v"(o[2]), "=&v"(o[3]), "=&v"(t0), "=&v"(t1), "=&v"(t2), "=&v"(t3), "=&v"(t4),
                 "=&v"(t5), "=&v"(t6), "=&v"(t7)
               : "v"(f), "i"(R));
  return o;
}
template <int R, int OFF> DI void acc_to_lds(unsigned addr) {
  asm volatile("ds_write_b128 %0, a[%c1:%c2] offset:%c3" ::"v"(addr), "i"(R), "i"(R + 3), "i"(OFF) : "memory");
}
template <int R> DI u32x4 acc_combine8f(const f32x4& w0, const f32x4& w1, float cw, float ca) {
  u32x4 o; float t0, t1, t2, t3, t4, t5, t6, t7;
  asm volatile("v_accvgpr_read_b32 %4, a[%c22]\n\tv_accvgpr_read_b32 %5, a[%c22+1]\n\tv_accvgpr_read_b32 %6, a[%c22+2]\n\t"
               "v_accvgpr_read_b32 %7, a[%c22+3]\n\tv_accvgpr_read_b32 %8, a[%c22+4]\n\tv_accvgpr_read_b32 %9, a[%c22+5]\n\t"
               "v_accvgpr_read_b32 %10, a[%c22+6]\n\tv_accvgpr_read_b32 %11, a[%c22+7]\n\t"
               "v_mul_f32 %4, %4, %21\n\tv_mul_f32 %5, %5, %21\n\tv_mul_f32 %6, %6, %21\n\tv_mul_f32 %7, %7, %21\n\t"
               "v_mul_f32 %8, %8, %21\n\tv_mul_f32 %9, %9, %21\n\tv_mul_f32 %10, %10, %21\n\tv_mul_f32 %11, %11, %21\n\t"
               "v_fma_f32 %4, %12, %20, %4\n\tv_fma_f32 %5, %13, %20, %5\n\tv_fma_f32 %6, %14, %20, %6\n\tv_fma_f32 %7, %15, %20, %7\n\t"
               "v_fma_f32 %8, %16, %20, %8\n\tv_fma_f32 %9, %17, %20, %9\n\tv_fma_f32 %10, %18, %20, %10\n\tv_fma_f32 %11, %19, %20, %11\n\t"
               "v_cvt_pk_bf16_f32 %0, %4, %5\n\tv_cvt_pk_bf16_f32 %1, %6, %7\n\tv_cvt_pk_bf16_f32 %2, %8, %9\n\tv_cvt_pk_bf16_f32 %3, %10, %11"
               : "=&v"(o[0]), "=&v"(o[1]), "=&v"(o[2]), "=&v"(o[3]), "=&v"(t0), "=&v"(t1), "=&v"(t2), "=&v"(t3), "=&v"(t4),
                 "=&v"(t5), "=&v"(t6), "=&v"(t7)
               : "v"(w0[0]), "v"(w0[1]), "v"(w0[2]), "v"(w0[3]), "v"(w1[0]), "v"(w1[1]), "v"(w1[2]), "v"(w1[3]), "v"(cw), "v"(ca),
                 "i"(R));
  return o;
}
DI void scores_add(float aone, float v) {
  asm volatile("s_nop 1\n\tv_mfma_f32_32x32x2_f32 v[80:95], %0, %1, v[80:95]\n\tv_mfma_f32_32x32x2_f32 v[96:111], %0, %1, v[96:111]" ::"v"(aone), "v"(v));
}
DI void negm_set(float aone, float v) { asm volatile("s_nop 1\n\tv_mfma_f32_32x32x2_f32 v[64:79], %0, %1, 0" ::"v"(aone), "v"(v)); }
#define ACC_FENCE() asm volatile("s_nop 15\n\ts_nop 3" ::: "memory")
#define BAR_A(WAITS) asm volatile("s_waitcnt " WAITS "\n\ts_barrier" ::: "memory", "v127", ACLOB)

template <int J> DI void frag_read(unsigned ka, unsigned va) {
  if constexpr (J < 16) ring_read<J % NRING, (J & 1) * 8192 + (J >> 1) * 1024>(ka);
  else { constexpr int i = J - 16; ring_read<J % NRING, (i >> 3) * 8192 + ((i & 3) * 2 + ((i >> 2) & 1)) * 1024>(va); }
}
#define PDX 4
constexpr int PD = PDX;

__global__ __launch_bounds__(512, 2) __attribute__((amdgpu_num_vgpr(64))) void k2_attn(const u16* __restrict__ XN, const u16* __restrict__ wq,
                                                  const u16* __restrict__ KF, const u16* __restrict__ VF,
                                                  u16* __restrict__ CF, float* __restrict__ LAM,
                                                  const u16* __restrict__ wo, const u16* __restrict__ w1,
                                                  const u16* __restrict__ w2) {
  extern __shared__ __attribute__((aligned(16))) char smem[];
  LAS char* lds = (LAS char*)smem;
  const unsigned lds0 = (unsigned)(uintptr_t)lds;
  const int tid = threadIdx.x, lane = tid & 63;
  const int wid = RFL(tid >> 6);
  const int b = blockIdx.x, xcd = b & 7, idx = b >> 3;
  const int head = xcd * 4 + (idx >> 3), px = idx & 7;
  const int qbH = 15 - px, qbL = px;
  const int rr = wid & 3, c = wid >> 2;
  const int r = lane & 31, h = lane >> 5;
  const int g1 = (lane >> 4) & 1, q4 = (lane & 15) >> 2, p4 = lane & 3;
  const __amdgpu_buffer_rsrc_t rX = mk_rsrc(XN, SEQ * DM * 2);
  const __amdgpu_buffer_rsrc_t rW = mk_rsrc(wq, DM * NH * HD * 2);
  const __amdgpu_buffer_rsrc_t rK = mk_rsrc(KF, NH * SEQ * HD * 2);
  const __amdgpu_buffer_rsrc_t rV = mk_rsrc(VF, NH * SEQ * HD * 2);
  const unsigned vlin = lane * 16;
  const unsigned voffW = ((lane >> 2) & 7) * 8192 + (lane >> 5) * 64 + (lane & 3) * 16;
  const int p1 = 2 * (px + 1);
  auto kvoff = [&](int sg) -> unsigned {
    const int hidx = sg < p1 ? sg : 2 * sg - p1 + c;
    return (unsigned)((head * 64 + 2 * hidx) * 8192);
  };
  STAMP_DECL();
  STAMP();
  const int tbx = (wid < 4) ? (4 * qbH + wid) : (4 * qbL + wid - 4);
  auto q_load = [&](unsigned addr) {
    q_read<0, 0>(addr); q_read<1, 1024>(addr); q_read<2, 2048>(addr); q_read<3, 3072>(addr);
    q_read<4, 4096>(addr); q_read<5, 5120>(addr); q_read<6, 6144>(addr); q_read<7, 7168>(addr);
  };
  {
    auto q_piece = [&](int kt, int i) {
      const unsigned sb = lds0 + (unsigned)(kt % 3) * 49152;
      if (i < 2) dma1(rW, voffW, (unsigned)((kt * 64 + wid * 8) * 8192 + head * 256 + i * 128), sb + (wid * 2 + i) * 1024);
      else dma1(rX, vlin, (unsigned)((tbx * 40 + 4 * kt + (i - 2)) * 1024), sb + 16384 + wid * 4096 + (i - 2) * 1024);
    };
    auto kv_piece = [&](int i) {
      const unsigned part = wid * 2048 + i * 1024;
      dma1(rK, vlin, (unsigned)(head * 64 * 8192) + part, lds0 + L_KA0 + part);
    };
#pragma unroll
    for (int i = 0; i < 6; ++i) q_piece(0, i);
#pragma unroll
    for (int i = 0; i < 6; ++i) q_piece(1, i);
    f32x16 qa[2][2];
#pragma unroll
    for (int i = 0; i < 2; ++i)
#pragma unroll
      for (int n = 0; n < 2; ++n) qa[i][n] = f32x16{};
    const unsigned wbase = h * 2048 + c * 1024 + q4 * 64 + g1 * 32 + p4 * 8;
    const unsigned xbase = 16384 + rr * 4096 + lane * 16;
    bf16x8 wA[2], xA[2], wB[2], xB[2];
    auto rdq = [&](bf16x8 (&w)[2], bf16x8 (&x)[2], LAS const char* sb, int s) {
      w[0] = lds_tr2(sb + wbase + s * 4096, sb + wbase + s * 4096 + 256);
      x[0] = lds_b128(sb + xbase + s * 1024);
      w[1] = lds_tr2(sb + wbase + s * 4096 + 512, sb + wbase + s * 4096 + 768);
      x[1] = lds_b128(sb + xbase + 16384 + s * 1024);
    };
    auto mmq = [&](const bf16x8 (&w)[2], const bf16x8 (&x)[2]) {
      qa[0][0] = MFMA32(w[0], x[0], qa[0][0]);
      qa[1][0] = MFMA32(w[0], x[1], qa[1][0]);
      qa[0][1] = MFMA32(w[1], x[0], qa[0][1]);
      qa[1][1] = MFMA32(w[1], x[1], qa[1][1]);
    };
    for (int kt = 0; kt < 10; ++kt) {
      if (kt < 9) { WAIT_VL_BAR(6); } else { WAIT_VL_BAR(2); }
      LAS const char* sb = lds + (unsigned)(kt % 3) * 49152;
      auto piece = [&](int i) {
        if (kt < 8) q_piece(kt + 2, i);
        else if (kt == 8 && i < 2) kv_piece(i);
      };
      rdq(wA, xA, sb, 0);
      SBAR();
      if (kt > 0) mmq(wB, xB);
      piece(0); SBAR();
      rdq(wB, xB, sb, 1);
      SBAR();
      mmq(wA, xA);
      piece(1); piece(2); SBAR();
      rdq(wA, xA, sb, 2);
      SBAR();
      mmq(wB, xB);
      piece(3); piece(4); SBAR();
      rdq(wB, xB, sb, 3);
      SBAR();
      mmq(wA, xA);
      piece(5); SBAR();
    }
    mmq(wB, xB);
    STAMP();
    WAIT_L_BAR();
#pragma unroll
    for (int i = 0; i < 2; ++i) {
      LAS char* dst = lds + (i == 0 ? L_STASH : 0u) + rr * 8192 + lane * 16;
#pragma unroll
      for (int n = 0; n < 2; ++n) {
        const f32x16 t = qa[i][n] * QSCALE;
#pragma unroll
        for (int sg = 0; sg < 2; ++sg) *(LAS u32x4*)(dst + (4 * c + 2 * n + sg) * 1024) = pack8(t, 8 * sg);
      }
    }
    WAIT_L_BAR();
    q_load(lds0 + (c ? 0u : L_STASH) + rr * 8192 + lane * 16);
    BAR_A("vmcnt(0) lgkmcnt(0)");
  }
  STAMP();

  negm_set(0.f, 0.f);
  float mref = 0.f, lsum = 0.f, mfin = 0.f, lfin = 0.f;
  const unsigned kown0 = c ? L_KB0 : L_KA0, kown1 = c ? L_KB1 : L_KA1;
  const unsigned vown0 = c ? L_VB0 : L_VA0, vown1 = c ? L_VB1 : L_VA1;
  const unsigned lrd = lds0 + lane * 16;
  float* lamp = LAM + (size_t)b * 256 + rr * 64;
  f32x2 mlA = {0.f, 0.f};

  auto refix = [&](const bool first) __attribute__((always_inline)) {
    const float rm = swap_max(rowmax32());
    const float dl = first ? fmaxf(rm, NEG_BIG) : fmaxf(rm, 0.f);
    const float f = first ? 0.f : __builtin_amdgcn_exp2f(-dl);
    mref = first ? dl : mref + dl;
    lsum = first ? 0.f : lsum * f;
    int ln = lane;
    asm volatile("" : "+v"(ln));
    const float aone = ln < 32 ? 1.f : 0.f;
    scores_add(aone, -dl);
    negm_set(aone, -mref);
    ACC_FENCE();
    if (!first) { o_scale_(f, std::make_integer_sequence<int, 64>{}); ACC_FENCE(); }
  };
#define TRAIL() do { pv_mfma<0, 28 % NRING, 0, 3>(); pv_mfma<0, 29 % NRING, 1, 3>(); pv_mfma<0, 30 % NRING, 2, 3>(); pv_mfma<0, 31 % NRING, 3, 3>(); SBAR(); } while (0)
  auto splain = [&]() __attribute__((always_inline)) {
    __builtin_amdgcn_s_setprio(TRAILPRIO);
    TRAIL();
    if (TRAILPRIO != SPRIO) __builtin_amdgcn_s_setprio(SPRIO);
    float pst = exps32();
    if (__builtin_expect(__any(pst > P_LIMIT), 0)) { refix(false); pst = exps32(); }
    asm volatile("v_add_f32 %0, %0, %1" : "+v"(lsum) : "v"(pst));
    __builtin_amdgcn_s_setprio(MPRIO);
  };
  auto sgen = [&](const int tau) __attribute__((always_inline)) {
    __builtin_amdgcn_s_setprio(SPRIO);
    if (tau > 0) TRAIL();
    const bool lastL = c == 1 && tau == p1 - 1;
    const bool diag = tau == NSTEP - 1 || (c == 1 && (tau == p1 - 2 || tau == p1 - 1));
    const bool first = tau == 0 || (c == 1 && tau == p1);
    int ln = lane;
    asm volatile("" : "+v"(ln));
    if (diag) {
      const int hp = tau == NSTEP - 1 ? c : tau - (p1 - 2);
      float ninf = -INFINITY;
      asm volatile("" : "+v"(ninf));
      const int dq = (ln & 31) - 4 * (ln >> 5);
      if (2 * hp > rr) mask_all_<V_S0>(ninf, std::make_integer_sequence<int, 16>{});
      else if (2 * hp == rr) mask_tri_<V_S0>(dq, ninf, std::make_integer_sequence<int, 16>{});
      if (2 * hp + 1 > rr) mask_all_<V_S1>(ninf, std::make_integer_sequence<int, 16>{});
      else if (2 * hp + 1 == rr) mask_tri_<V_S1>(dq, ninf, std::make_integer_sequence<int, 16>{});
    }
    if (first && tau > 0) {
      ACC_FENCE();
      const float inv = 1.0f / lfin;
      u16* dstp = CF + ((size_t)(((4 * qbL + rr) * 32 + head) * 8) * 64 + ln) * 8;
      *(u32x4*)(dstp) = acc_scale8<A_O + 0>(inv);          *(u32x4*)(dstp + 512) = acc_scale8<A_O + 8>(inv);
      *(u32x4*)(dstp + 1024) = acc_scale8<A_O + 16>(inv);  *(u32x4*)(dstp + 1536) = acc_scale8<A_O + 24>(inv);
      *(u32x4*)(dstp + 2048) = acc_scale8<A_O + 32>(inv);  *(u32x4*)(dstp + 2560) = acc_scale8<A_O + 40>(inv);
      *(u32x4*)(dstp + 3072) = acc_scale8<A_O + 48>(inv);  *(u32x4*)(dstp + 3584) = acc_scale8<A_O + 56>(inv);
    }
    if (first) refix(true);
    float pst = exps32();
    if (!first && __any(pst > P_LIMIT)) { refix(false); pst = exps32(); }
    asm volatile("v_add_f32 %0, %0, %1" : "+v"(lsum) : "v"(pst));
    if (lastL || tau == NSTEP - 1) {
      mfin = mref; lfin = swap_add(lsum);
      if (lastL) {
        negm_set(0.f, 0.f);
        q_load(lds0 + L_STASH + rr * 8192 + ln * 16);
      } else {
        f32x2* lp = (f32x2*)lamp + (ln & 31);
        if (c == 0) { if (ln < 32) *lp = (f32x2){mfin, lfin}; }
        else mlA = *lp;
      }
    }
    __builtin_amdgcn_s_setprio(0);
  };
#define KVDMA(i) do { if ((i) == 0) dma1o<0>(rk_, vlin, gk, lk); else if ((i) == 1) dma1o<1024>(rk_, vlin, gk, lk); \
    else if ((i) == 2) dma1o<2048>(rk_, vlin, gk, lk); else if ((i) == 3) dma1o<3072>(rk_, vlin, gk, lk); \
    else if ((i) == 4) dma1o<0>(rv_, vlin, gv, lv); else if ((i) == 5) dma1o<1024>(rv_, vlin, gv, lv); \
    else if ((i) == 6) dma1o<2048>(rv_, vlin, gv, lv); else dma1o<3072>(rv_, vlin, gv, lv); } while (0)
#define MSTEP(J, FIRST, LAST, DMA, ZO) do { \
    constexpr int out_ = ((J) + PD - 1 < (LAST) ? (J) + PD - 1 : (LAST)) - (J);     \
    if constexpr ((J) < 2) qk_head<out_, (J) % NRING, 0, ((J) & 1) ? V_S1 : V_S0>(); \
    else if constexpr ((J) < 16) qk_mfma<out_, (J) % NRING, ((J) >> 1), ((J) & 1) ? V_S1 : V_S0>(); \
    else if constexpr ((J) < 20 && (ZO)) { if (zero_o) pv_zero<out_, (J) % NRING, (J) & 3, 0>(); else pv_mfma<out_, (J) % NRING, (J) & 3, 0>(); } \
    else pv_mfma<out_, (J) % NRING, (J) & 3, (((J) - 16) >> 2)>(); \
    if constexpr ((J) + PD <= (LAST)) frag_read<(J) + PD>(ka, va); \
    if constexpr ((DMA) && ((J) & 1) && (J) < 16) KVDMA((J) >> 1); \
    SBAR(); } while (0)
#define MSTEP4(J, FIRST, LAST, DMA, ZO) MSTEP(J, FIRST, LAST, DMA, ZO); MSTEP((J) + 1, FIRST, LAST, DMA, ZO); MSTEP((J) + 2, FIRST, LAST, DMA, ZO); MSTEP((J) + 3, FIRST, LAST, DMA, ZO)
#define KSLOT(t) (((t) < p1) ? (((t) & 1) ? L_KA1 : L_KA0) : (((t) & 1) ? kown1 : kown0))
#define VSLOT(t) (((t) < p1) ? (((t) & 1) ? L_VA1 : L_VA0) : (((t) & 1) ? vown1 : vown0))
#define STAGE_ADDR(t) \
      const __amdgpu_buffer_rsrc_t rk_ = rK, rv_ = rV; \
      const unsigned gk = ((t) + 1 < NSTEP && (c == 0 || (t) + 1 >= p1)) ? kvoff((t) + 1) + rr * 4096 : 0x7ff00000u; \
      const unsigned gv = (c == 0 || (t) >= p1) ? kvoff(t) + rr * 4096 : 0x7ff00000u; \
      const unsigned lk = lds0 + ((((t) + 1) & 1) ? kown1 : kown0) + rr * 4096; \
      const unsigned lv = lds0 + (((t) & 1) ? vown1 : vown0) + rr * 4096;

  if (c == 1) asm volatile("s_barrier" ::: "memory");
  {
    const unsigned ka = lrd + L_KA0, va = 0;
    STAGE_ADDR(0)
    constexpr bool zero_o = false;
    frag_read<0>(ka, va); frag_read<1>(ka, va); frag_read<2>(ka, va); frag_read<3>(ka, va);
    SBAR();
    MSTEP4(0, 0, 15, true, false); MSTEP4(4, 0, 15, true, false); MSTEP4(8, 0, 15, true, false); MSTEP4(12, 0, 15, true, false);
    BAR_A("lgkmcnt(0)");
    ACC_FENCE();
    sgen(0);
    BAR_A("vmcnt(0) lgkmcnt(0)");
  }
#define MPHASE(tau, DMA, ZO) do { \
      const bool zero_o = (tau) == 1 || (c == 1 && (tau) == p1 + 1);     \
      (void)zero_o; \
      const unsigned ka = lrd + KSLOT(tau); \
      const unsigned va = lrd + VSLOT((tau) - 1); \
      STAGE_ADDR(tau) \
      (void)gk; (void)gv; (void)lk; (void)lv; (void)rk_; (void)rv_; \
      frag_read<0>(ka, va); frag_read<1>(ka, va); frag_read<2>(ka, va); frag_read<3>(ka, va); \
      SBAR(); \
      MSTEP4(0, 0, 31, DMA, ZO); MSTEP4(4, 0, 31, DMA, ZO); MSTEP4(8, 0, 31, DMA, ZO); MSTEP4(12, 0, 31, DMA, ZO); \
      MSTEP4(16, 0, 31, DMA, ZO); MSTEP4(20, 0, 31, DMA, ZO); MSTEP4(24, 0, 31, DMA, ZO); \
      BAR_A("lgkmcnt(0)"); } while (0)
#define MPHASE_WARM(tau) do { \
      constexpr bool zero_o = false; (void)zero_o; \
      const unsigned ka = lrd + KSLOT(tau); \
      const unsigned va = lrd + VSLOT((tau) - 1); \
      const int ch_ = ((tau) < 4 ? ((tau) - 2) * 768 + (px - 2) * 128 : 1536 + ((tau) - 4) * 640 + (px - 3) * 128) + head * 4 + rr; \
      const __amdgpu_buffer_rsrc_t rk_ = ch_ < 640 ? rWo : ch_ < 1504 ? rW1 : rW2, rv_ = rk_; \
      const unsigned gk = (WARM_ON && (tau) <= 5) ? (unsigned)(ch_ - (ch_ < 640 ? 0 : ch_ < 1504 ? 640 : 1504)) * 8192u : 0x7ff00000u; \
      const unsigned gv = gk + 4096u; \
      const unsigned lk = lds0 + kown0 + rr * 4096, lv = lds0 + vown0 + rr * 4096; \
      frag_read<0>(ka, va); frag_read<1>(ka, va); frag_read<2>(ka, va); frag_read<3>(ka, va); \
      SBAR(); \
      MSTEP4(0, 0, 31, true, false); MSTEP4(4, 0, 31, true, false); MSTEP4(8, 0, 31, true, false); MSTEP4(12, 0, 31, true, false); \
      MSTEP4(16, 0, 31, true, false); MSTEP4(20, 0, 31, true, false); MSTEP4(24, 0, 31, true, false); \
      BAR_A("lgkmcnt(0)"); } while (0)
  constexpr bool WARM_ON = true;
  const __amdgpu_buffer_rsrc_t rWo = mk_rsrc(wo, NH * HD * DM * 2), rW1 = mk_rsrc(w1, SEQ * FF * 2), rW2 = mk_rsrc(w2, SEQ * FF * 2);
  int tau = 1;
  while (tau < NSTEP) {
    const bool special = tau == 1 || tau == NSTEP - 1 || (c == 1 && tau >= p1 - 2 && tau <= p1 + 1);
    if (special) {
      STAMP();
      MPHASE(tau, true, true);
      STAMP();
      sgen(tau);
      BAR_A("vmcnt(0) lgkmcnt(0)");
      ++tau;
    } else if (c == 1 && tau < p1 - 2) {
      do {
        STAMP();
        if (tau <= 5) MPHASE_WARM(tau); else MPHASE(tau, false, false);
        STAMP();
        splain();
        BAR_A("lgkmcnt(0)");
        ++tau;
      } while (tau < p1 - 2);
    } else {
      do {
        STAMP();
        MPHASE(tau, true, false);
        STAMP();
        splain();
        BAR_A("vmcnt(0) lgkmcnt(0)");
        ++tau;
      } while (tau < NSTEP - 1);
    }
  }
#undef MPHASE
#undef MPHASE_WARM
#undef TRAIL
  {
    const unsigned ka = 0, va = lrd + VSLOT(NSTEP - 1);
    const unsigned gk = 0, gv = 0, lk = 0, lv = 0;
    const __amdgpu_buffer_rsrc_t rk_ = rK, rv_ = rV;
    (void)gk; (void)gv; (void)lk; (void)lv; (void)ka; (void)rk_; (void)rv_;
    const bool zero_o = c == 1 && p1 == NSTEP - 1;
    frag_read<16>(ka, va); frag_read<17>(ka, va); frag_read<18>(ka, va); frag_read<19>(ka, va);
    SBAR();
    MSTEP4(16, 16, 31, false, true); MSTEP4(20, 16, 31, false, true); MSTEP4(24, 16, 31, false, true); MSTEP4(28, 16, 31, false, true);
    BAR_A("lgkmcnt(0)");
    int ln = lane;
    asm volatile("" : "+v"(ln));
    const unsigned mine = lds0 + (c ? L_KB1 : L_KA0) + ((rr & 2) ? (c ? L_KB0 - L_KB1 : L_KA1 - L_KA0) : 0u) + (rr & 1) * 8192 + ln * 16;
    const unsigned theirs = (c ? L_KA0 : L_KB1) + ((rr & 2) ? (c ? L_KA1 - L_KA0 : L_KB0 - L_KB1) : 0u) + (rr & 1) * 8192 + ln * 16;
    ACC_FENCE();
    if (c == 0) {
      acc_to_lds<A_O + 32, 0>(mine); acc_to_lds<A_O + 36, 1024>(mine); acc_to_lds<A_O + 40, 2048>(mine); acc_to_lds<A_O + 44, 3072>(mine);
      acc_to_lds<A_O + 48, 4096>(mine); acc_to_lds<A_O + 52, 5120>(mine); acc_to_lds<A_O + 56, 6144>(mine); acc_to_lds<A_O + 60, 7168>(mine);
      BAR_A("lgkmcnt(0)");
    } else {
      acc_to_lds<A_O + 0, 0>(mine); acc_to_lds<A_O + 4, 1024>(mine); acc_to_lds<A_O + 8, 2048>(mine); acc_to_lds<A_O + 12, 3072>(mine);
      acc_to_lds<A_O + 16, 4096>(mine); acc_to_lds<A_O + 20, 5120>(mine); acc_to_lds<A_O + 24, 6144>(mine); acc_to_lds<A_O + 28, 7168>(mine);
      if (ln < 32) *(LAS f32x2*)(lds + L_STASH + rr * 256 + ln * 8) = (f32x2){mfin, lfin};
    }
    BAR_A("lgkmcnt(0)");
    {
      const f32x2 mo = c ? mlA : *(LAS const f32x2*)(lds + L_STASH + rr * 256 + (ln & 31) * 8);
      const float m = fmaxf(mo[0], mfin);
      const float eo = __builtin_amdgcn_exp2f(mo[0] - m), em = __builtin_amdgcn_exp2f(mfin - m);
      const float inv = 1.0f / (mo[1] * eo + lfin * em);
      const float co = eo * inv, cm = em * inv;
      f32x4 w[8];
#pragma unroll
      for (int i = 0; i < 8; ++i) w[i] = *(LAS const f32x4*)(lds + theirs + i * 1024);
      u16* dstp = CF + ((size_t)(((4 * qbH + rr) * 32 + head) * 8 + 4 * c) * 64 + ln) * 8;
      if (c == 0) {
        *(u32x4*)(dstp) = acc_combine8f<A_O + 0>(w[0], w[1], co, cm);         *(u32x4*)(dstp + 512) = acc_combine8f<A_O + 8>(w[2], w[3], co, cm);
        *(u32x4*)(dstp + 1024) = acc_combine8f<A_O + 16>(w[4], w[5], co, cm); *(u32x4*)(dstp + 1536) = acc_combine8f<A_O + 24>(w[6], w[7], co, cm);
      } else {
        *(u32x4*)(dstp) = acc_combine8f<A_O + 32>(w[0], w[1], co, cm);        *(u32x4*)(dstp + 512) = acc_combine8f<A_O + 40>(w[2], w[3], co, cm);
        *(u32x4*)(dstp + 1024) = acc_combine8f<A_O + 48>(w[4], w[5], co, cm); *(u32x4*)(dstp + 1536) = acc_combine8f<A_O + 56>(w[6], w[7], co, cm);
      }
    }
  }
#undef KSLOT
#undef VSLOT
#undef STAGE_ADDR
#undef KVDMA
#undef MSTEP
#undef MSTEP4
  STAMP();
  STAMP_END(1, DBGP(CF, WS_CF));
}

constexpr int K3_STAGE = 37888, K3_NST = 4, K3_LDS = K3_STAGE * K3_NST;

__global__ __launch_bounds__(512, 2) void k3_oproj(const u16* __restrict__ CF, const u16* __restrict__ wo,
                                                   u16* __restrict__ PT) {
  extern __shared__ __attribute__((aligned(16))) char smem[];
  LAS char* lds = (LAS char*)smem;
  const unsigned lds0 = (unsigned)(uintptr_t)lds;
  const int tid = threadIdx.x, lane = tid & 63;
  const int wid = RFL(tid >> 6);
  const int b = blockIdx.x, xcd = b & 7, idx = b >> 3;
  const int ks = xcd >> 1, panel = (xcd & 1) * 8 + (idx >> 2), nt = idx & 3;
  const int tbi = wid & 3, kp = wid >> 2;
  const int h = lane >> 5, g1 = (lane >> 4) & 1, q4 = (lane & 15) >> 2, p4 = lane & 3;
  const __amdgpu_buffer_rsrc_t rC = mk_rsrc(CF, NH * SEQ * HD * 2);
  const __amdgpu_buffer_rsrc_t rW = mk_rsrc(wo, NH * HD * DM * 2);
  const unsigned vlin = lane * 16;
  unsigned voffW[3];
#pragma unroll
  for (int i = 0; i < 3; ++i) {
    const int pc = wid + 8 * i;
    const int u = 2 * pc + (lane >> 5);
    const int kb = u / 5, nb = u % 5;
    voffW[i] = (unsigned)((kb * 8 + ((lane >> 2) & 7)) * 1280 + (nb * 32 + (lane & 3) * 8) * 2);
  }
  const bool lo = wid < 4;
  const unsigned sW0 = (unsigned)(ks * 1024 * 1280 + nt * 320);
  const unsigned sC0 = (unsigned)(((panel * 4 + tbi) * 256 + ks * 64) * 1024);
  const __amdgpu_buffer_rsrc_t r2 = lo ? rW : rC;
  const unsigned v2 = lo ? voffW[2] : vlin;
  const unsigned s2 = lo ? sW0 : sC0, st2 = lo ? 81920u : 4096u, l2 = lo ? (wid + 16) * 1024 : 20480 + tbi * 4096;
  const unsigned s3 = sC0 + (lo ? 3072u : 1024u), l3 = 20480 + tbi * 4096 + (lo ? 3072 : 1024);
  const unsigned s4 = lo ? 0x7ff00000u : sC0 + 2048u, l4 = lo ? 36864 : 20480 + tbi * 4096 + 2048;
  auto piece = [&](int j, int slot, int i) __attribute__((always_inline)) {
    const unsigned sb = lds0 + slot * K3_STAGE;
    if (i == 0) dma1(rW, voffW[0], sW0 + j * 81920, sb + wid * 1024);
    else if (i == 1) dma1(rW, voffW[1], sW0 + j * 81920, sb + (wid + 8) * 1024);
    else if (i == 2) dma1(r2, v2, s2 + j * st2, sb + l2);
    else if (i == 3) dma1(rC, vlin, s3 + j * 4096, sb + l3);
    else dma1(rC, vlin, s4 + j * 4096, sb + l4);
  };
  f32x16 acc[5];
#pragma unroll
  for (int n = 0; n < 5; ++n) acc[n] = f32x16{};
  const unsigned wbase = h * 256 + q4 * 64 + g1 * 32 + p4 * 8 + kp * 4 * 2560;
  const unsigned cbase = 20480 + tbi * 4096 + lane * 16 + kp * 2048;
  auto rd = [&](bf16x8& cf, bf16x8 (&w)[5], LAS const char* sb, int si) {
    cf = lds_b128(sb + cbase + si * 1024);
#pragma unroll
    for (int n = 0; n < 5; ++n)
      w[n] = lds_tr2(sb + wbase + (2 * si) * 2560 + n * 512, sb + wbase + (2 * si + 1) * 2560 + n * 512);
  };
  bf16x8 cA, cB, wA[5], wB[5];
  STAMP_DECL();
  STAMP();
#pragma unroll
  for (int j = 0; j < 3; ++j)
#pragma unroll
    for (int i = 0; i < 5; ++i) piece(j, j, i);
  STAMP();
  auto stage = [&](const int j, const int slot, const bool more, const bool first) __attribute__((always_inline)) {
    STAMP();
    LAS const char* sb = lds + slot * K3_STAGE;
    rd(cA, wA, sb, 0);
    SBAR();
    if (!first) {
      acc[0] = MFMA32(wB[0], cB, acc[0]); acc[1] = MFMA32(wB[1], cB, acc[1]);
      if (more) piece(j + 3, (slot + 3) & 3, 0);
      SBAR();
      acc[2] = MFMA32(wB[2], cB, acc[2]); acc[3] = MFMA32(wB[3], cB, acc[3]);
      if (more) piece(j + 3, (slot + 3) & 3, 1);
      SBAR();
      acc[4] = MFMA32(wB[4], cB, acc[4]);
    } else {
      piece(3, 3, 0); piece(3, 3, 1);
    }
    SBAR();
    rd(cB, wB, sb, 1);
    SBAR();
    acc[0] = MFMA32(wA[0], cA, acc[0]);
    if (more) piece(j + 3, (slot + 3) & 3, 2);
    SBAR();
    acc[1] = MFMA32(wA[1], cA, acc[1]); acc[2] = MFMA32(wA[2], cA, acc[2]);
    if (more) piece(j + 3, (slot + 3) & 3, 3);
    SBAR();
    acc[3] = MFMA32(wA[3], cA, acc[3]); acc[4] = MFMA32(wA[4], cA, acc[4]);
    if (more) piece(j + 3, (slot + 3) & 3, 4);
    SBAR();
  };
  WAIT_VL_BAR(10); stage(0, 0, true, true);
  for (int j4 = 1; j4 < 13; j4 += 4) {
    WAIT_VL_BAR(10); stage(j4, 1, true, false);
    WAIT_VL_BAR(10); stage(j4 + 1, 2, true, false);
    WAIT_VL_BAR(10); stage(j4 + 2, 3, true, false);
    WAIT_VL_BAR(10); stage(j4 + 3, 0, true, false);
  }
  WAIT_VL_BAR(10); stage(13, 1, false, false);
  WAIT_VL_BAR(5); stage(14, 2, false, false);
  WAIT_VL_BAR(0); stage(15, 3, false, false);
#pragma unroll
  for (int n = 0; n < 5; ++n) acc[n] = MFMA32(wB[n], cB, acc[n]);
  STAMP();
  WAIT_L_BAR();
  {
    LAS char* mine = lds + (tbi * 2 + kp) * 12288 + lane * 16;
    LAS const char* theirs = lds + (tbi * 2 + (kp ^ 1)) * 12288 + lane * 16;
    auto put = [&](const f32x16& a, int slot) {
#pragma unroll
      for (int gq = 0; gq < 4; ++gq)
        *(LAS f32x4*)(mine + (slot * 4 + gq) * 1024) = (f32x4){a[4 * gq], a[4 * gq + 1], a[4 * gq + 2], a[4 * gq + 3]};
    };
    auto fin = [&](const f32x16& a, int slot, int n) {
      f32x16 t;
#pragma unroll
      for (int gq = 0; gq < 4; ++gq) {
        const f32x4 v = *(LAS const f32x4*)(theirs + (slot * 4 + gq) * 1024);
#pragma unroll
        for (int e = 0; e < 4; ++e) t[4 * gq + e] = a[4 * gq + e] + v[e];
      }
      u16* dst = PT + ((size_t)((((ks * 64 + panel * 4 + tbi) * 20 + nt * 5 + n) * 2) * 64 + lane)) * 8;
      *(u32x4*)dst = pack8(t, 0);
      *(u32x4*)(dst + 512) = pack8(t, 8);
    };
    if (kp == 0) { put(acc[3], 0); put(acc[4], 1); } else { put(acc[0], 0); put(acc[1], 1); put(acc[2], 2); }
    WAIT_L_BAR();
    if (kp == 0) { fin(acc[0], 0, 0); fin(acc[1], 1, 1); fin(acc[2], 2, 2); } else { fin(acc[3], 0, 3); fin(acc[4], 1, 4); }
  }
  STAMP();
  STAMP();
  STAMP_END(2, DBGP(PT, WS_PT));
}

__global__ __launch_bounds__(320) void k4_ln(const u16* __restrict__ x, const u16* __restrict__ PT,
                                             float* __restrict__ H, u16* __restrict__ HN, const u16* __restrict__ w1) {
  __shared__ __attribute__((aligned(16))) u16 tile[8 * 648];
  __shared__ float red[2][5][8];
  __shared__ __attribute__((aligned(16))) char dead[5 * 256];
  const int tid = threadIdx.x, lane = tid & 63, wid = RFL(tid >> 6);
  const int b = blockIdx.x;
  const int r8 = lane & 7, hh = (lane >> 3) & 1, sg = (lane >> 4) & 1, ml = lane >> 5;
  const int token = b * 8 + r8, tb32 = token >> 5, rr = token & 31;
  STAMP_DECL();
  STAMP();
  u32x2 xv[2][2];
  u32x4 pv[2][4];
#pragma unroll
  for (int i = 0; i < 2; ++i) {
    const int mb = wid * 4 + 2 * i + ml;
    const int m0 = mb * 32 + sg * 16 + hh * 4;
    xv[i][0] = *(const u32x2*)(x + (size_t)token * DM + m0);
    xv[i][1] = *(const u32x2*)(x + (size_t)token * DM + m0 + 8);
#pragma unroll
    for (int ks = 0; ks < 4; ++ks)
      pv[i][ks] = *(const u32x4*)(PT + ((size_t)((((ks * 64 + tb32) * 20 + mb) * 2 + sg) * 64 + hh * 32 + rr)) * 8);
  }
  SBAR();
  STAMP();
  float v[2][8];
  float s = 0.f;
#pragma unroll
  for (int i = 0; i < 2; ++i) {
    const int m0 = (wid * 4 + 2 * i + ml) * 32 + sg * 16 + hh * 4;
#pragma unroll
    for (int q = 0; q < 4; ++q) {
      const unsigned xw = xv[i][q >> 1][q & 1];
      v[i][2 * q] = bflo(xw) + ((bflo(pv[i][0][q]) + bflo(pv[i][1][q])) + (bflo(pv[i][2][q]) + bflo(pv[i][3][q])));
      v[i][2 * q + 1] = bfhi(xw) + ((bfhi(pv[i][0][q]) + bfhi(pv[i][1][q])) + (bfhi(pv[i][2][q]) + bfhi(pv[i][3][q])));
    }
    *(f32x4*)(H + (size_t)token * DM + m0) = (f32x4){v[i][0], v[i][1], v[i][2], v[i][3]};
    *(f32x4*)(H + (size_t)token * DM + m0 + 8) = (f32x4){v[i][4], v[i][5], v[i][6], v[i][7]};
#pragma unroll
    for (int e = 0; e < 8; ++e) s += v[i][e];
  }
  STAMP();
  {
    const int u = (b >> 3) * 5 + wid;
    const int a4 = (b & 7) >> 1;
    const unsigned dd = (unsigned)(uintptr_t)(LAS char*)dead + wid * 256;
    const __amdgpu_buffer_rsrc_t r1 = mk_rsrc(w1, SEQ * FF * 2);
#pragma unroll
    for (int i = 0; i < 2; ++i) {
      const int line = (u + 160 * i) * 64 + lane;
      const unsigned off = line < 14336 ? (unsigned)((line / 7) * (FF * 2) + a4 * 896 + (line % 7) * 128) : 0x7ff00000u;
      warm8k(r1, off, 0u, dd);
    }
  }
  s += __shfl_xor(s, 8); s += __shfl_xor(s, 16); s += __shfl_xor(s, 32);
  if (lane < 8) red[0][wid][lane] = s;
  WAIT_L_BAR();
  STAMP();
  const float mean = ((red[0][0][r8] + red[0][1][r8]) + (red[0][2][r8] + red[0][3][r8]) + red[0][4][r8]) * (1.0f / DM);
  float sq = 0.f;
#pragma unroll
  for (int i = 0; i < 2; ++i)
#pragma unroll
    for (int e = 0; e < 8; ++e) { v[i][e] -= mean; sq += v[i][e] * v[i][e]; }
  sq += __shfl_xor(sq, 8); sq += __shfl_xor(sq, 16); sq += __shfl_xor(sq, 32);
  if (lane < 8) red[1][wid][lane] = sq;
  WAIT_L_BAR();
  const float var = ((red[1][0][r8] + red[1][1][r8]) + (red[1][2][r8] + red[1][3][r8]) + red[1][4][r8]) * (1.0f / DM);
  const float r1 = 1.0f / sqrtf(var + LN_EPS);
  const float r2 = 1.0f / sqrtf(var * r1 * r1 + LN_EPS);
  const float sc = r1 * r2;
#pragma unroll
  for (int i = 0; i < 2; ++i) {
    const int m0 = (wid * 4 + 2 * i + ml) * 32 + sg * 16 + hh * 4;
    *(u32x2*)(tile + r8 * 648 + m0) = (u32x2){cvtpk(v[i][0] * sc, v[i][1] * sc), cvtpk(v[i][2] * sc, v[i][3] * sc)};
    *(u32x2*)(tile + r8 * 648 + m0 + 8) = (u32x2){cvtpk(v[i][4] * sc, v[i][5] * sc), cvtpk(v[i][6] * sc, v[i][7] * sc)};
  }
  WAIT_L_BAR();
  STAMP();
  const int ks32 = b >> 2, g = b & 3;
#pragma unroll
  for (int i = 0; i < 2; ++i) {
    const int m = tid + 320 * i;
    u16 e[8];
#pragma unroll
    for (int j = 0; j < 8; ++j) e[j] = tile[j * 648 + m];
    u32x4 w = {(unsigned)e[0] | ((unsigned)e[1] << 16), (unsigned)e[2] | ((unsigned)e[3] << 16),
               (unsigned)e[4] | ((unsigned)e[5] << 16), (unsigned)e[6] | ((unsigned)e[7] << 16)};
    *(u32x4*)(HN + ((size_t)(((m >> 4) * 64 + ks32) * 64 + g * 16 + (m & 15))) * 8) = w;
  }
  STAMP();
  STAMP();
  STAMP_END(3, DBGP(HN, WS_HN));
}

constexpr int K56_LDS = 163840;

DI float gelu_tanh(float x) {
  const float u = 0.7978845608028654f * (x + 0.044715f * x * x * x);
  const float e = __builtin_amdgcn_exp2f(u * 2.8853900817779268f);
  const float t = 1.0f - 2.0f * __builtin_amdgcn_rcpf(e + 1.0f);
  return 0.5f * x * (1.0f + t);
}

#define ACLOB56 "a0","a1","a2","a3","a4","a5","a6","a7","a8","a9","a10","a11","a12","a13","a14","a15","a16","a17","a18","a19","a20","a21","a22","a23","a24","a25","a26","a27","a28","a29","a30","a31","a32","a33","a34","a35","a36","a37","a38","a39","a40","a41","a42","a43","a44","a45","a46","a47","a48","a49","a50","a51","a52","a53","a54","a55","a56","a57","a58","a59","a60","a61","a62","a63","a64","a65","a66","a67","a68","a69","a70","a71","a72","a73","a74","a75","a76","a77","a78","a79","a80","a81","a82","a83","a84","a85","a86","a87","a88","a89","a90","a91"
template <int R> DI void m_aload(__amdgpu_buffer_rsrc_t rsrc, unsigned voff, unsigned soff) {
  asm volatile("buffer_load_dwordx4 a[%c3:%c4], %0, %1, %2 offen" ::"v"(voff), "s"(rsrc), "s"(soff), "i"(R), "i"(R + 3) : "memory");
}
template <int R, int OFF> DI void m_trread(unsigned addr) {
  asm volatile("ds_read_b64_tr_b16 a[%c1:%c2], %0 offset:%c3" ::"v"(addr), "i"(R), "i"(R + 1), "i"(OFF) : "memory");
}
template <int RA, int RB> DI void m_mfma(f32x4& d) {
  asm volatile("v_mfma_f32_16x16x32_bf16 %0, a[%c1:%c2], a[%c3:%c4], %0" : "+v"(d) : "i"(RA), "i"(RA + 3), "i"(RB), "i"(RB + 3));
}

template <int MODE>
__global__ __launch_bounds__(512, 2) void k56_mix(const u16* __restrict__ W, const u16* __restrict__ AF,
                                                  const u16* __restrict__ bias, const float* __restrict__ H,
                                                  u16* __restrict__ outp, const u16* __restrict__ wnext) {
  constexpr int LDW = MODE == 0 ? FF : SEQ;
  constexpr int NKS = MODE == 0 ? 64 : 54;
  constexpr int NK = MODE == 0 ? 8 : 7;
  constexpr int KROWS = MODE == 0 ? SEQ : FF;
  extern __shared__ __attribute__((aligned(16))) char smem[];
  LAS char* lds = (LAS char*)smem;
  const unsigned lds0 = (unsigned)(uintptr_t)lds;
  const int tid = threadIdx.x, lane = tid & 63;
  const int wid = RFL(tid >> 6);
  const int b = blockIdx.x, xcd = b & 7, idx = b >> 3;
  int wt, dt;
  if (MODE == 0) {
    wt = (xcd >> 1) * 7 + (idx >> 2); dt = (xcd & 1) * 4 + (idx & 3);
    if (idx >= 28 || wt >= 27) {
      if (idx >= 28) {
        const __amdgpu_buffer_rsrc_t r2 = mk_rsrc(wnext, SEQ * FF * 2);
#pragma unroll
        for (int i = 0; i < 7; ++i) {
          const int line = (((idx - 28) * 8 + wid) * 7 + i) * 64 + lane;
          const unsigned off = line < 13824 ? (unsigned)((line >> 3) * 4096 + (xcd >> 1) * 1024 + (line & 7) * 128) : 0x7ff00000u;
          warm8k(r2, off, 0u, lds0 + wid * 256);
        }
      }
      return;
    }
  } else {
    wt = (xcd >> 1) * 8 + (idx >> 2); dt = (xcd & 1) * 4 + (idx & 3);
  }
  const __amdgpu_buffer_rsrc_t rW = mk_rsrc(W, KROWS * LDW * 2);
  const __amdgpu_buffer_rsrc_t rA = mk_rsrc(AF, 40 * NKS * 1024);
  const unsigned vlin = lane * 16;
  const int prow = (lane >> 2) & 7;
  const unsigned voffW0 = (unsigned)(prow * LDW * 2 + (lane >> 5) * 64 +
                                     (((lane & 3) ^ (MODE == 1 ? (((prow >> 2) & 1) << 1) : 0)) << 4));
  const unsigned voffW1 = MODE == 1 ? voffW0 : (voffW0 ^ 32u);
  const unsigned myl = lds0 + wid * 20480;
  const int g = lane >> 4, q4 = (lane & 15) >> 2, p4 = lane & 3;
  unsigned wb[2][2];
#pragma unroll
  for (int t = 0; t < 2; ++t)
#pragma unroll
    for (int hf = 0; hf < 2; ++hf) {
      const int kb = MODE == 0 ? g : 2 * t + (g >> 1);
      const int row = MODE == 0 ? 4 * t + q4 : 4 * (g & 1) + q4;
      const int sw = MODE == 0 ? (kb & 1) : ((row >> 2) & 1);
      wb[t][hf] = myl + kb * 1024 + row * 64 + ((hf ^ sw) * 32) + p4 * 8;
    }
  f32x4 acc[4][5];
#pragma unroll
  for (int i = 0; i < 4; ++i)
#pragma unroll
    for (int d = 0; d < 5; ++d) acc[i][d] = (f32x4){0.f, 0.f, 0.f, 0.f};
  STAMP_DECL();
  STAMP();
  u32x2 eb0[2], eb1[2];
  f32x4 eh[3]; u16 ebs[3];
  if (MODE == 0) {
#pragma unroll
    for (int i = 0; i < 2; ++i) {
      const int u = wid + 8 * i < 10 ? wid + 8 * i : wid;
      const int f0 = wt * 64 + (u / 5) * 32 + 4 * g;
      eb0[i] = *(const u32x2*)(bias + f0); eb1[i] = *(const u32x2*)(bias + f0 + 16);
    }
  } else {
#pragma unroll
    for (int i = 0; i < 3; ++i) {
      const int u = wid + 8 * i < 20 ? wid + 8 * i : wid;
      const int s = wt * 64 + (u / 5) * 16 + (lane & 15), d0 = dt * 80 + (u % 5) * 16 + 4 * g;
      ebs[i] = bias[s];
      eh[i] = *(const f32x4*)(H + (size_t)s * DM + d0);
    }
  }
  SBAR();
#define KSTEP(IT) (wid + 8 * (IT))
#define WDMA(IT, KB) dma1(rW, ((KB) & 1) ? voffW1 : voffW0, (unsigned)((KSTEP(IT) * 32 + (KB) * 8) * LDW * 2 + wt * 128), myl + ((IT) % 5) * 4096 + (KB) * 1024)
#define ALOAD(IT, D) m_aload<((IT) % 3) * 20 + 4 * (D)>(rA, vlin, (unsigned)(((dt * 5 + (D)) * NKS + KSTEP(IT)) * 1024))
#define WDMA4(IT) do { WDMA(IT, 0); WDMA(IT, 1); WDMA(IT, 2); WDMA(IT, 3); } while (0)
#define ALOAD5(IT) do { ALOAD(IT, 0); ALOAD(IT, 1); ALOAD(IT, 2); ALOAD(IT, 3); ALOAD(IT, 4); } while (0)
#define WREAD1(IT, I) do { m_trread<60 + 16 * ((IT) & 1) + 4 * (I), ((IT) % 5) * 4096 + ((I) >> 1) * 512>(wb[0][(I) & 1]); \
                           m_trread<60 + 16 * ((IT) & 1) + 4 * (I) + 2, ((IT) % 5) * 4096 + ((I) >> 1) * 512>(wb[1][(I) & 1]); } while (0)
#define WREAD(IT) do { WREAD1(IT, 0); WREAD1(IT, 1); WREAD1(IT, 2); WREAD1(IT, 3); } while (0)
#define MM(IT, I, D) do { if constexpr (MODE == 0) m_mfma<60 + 16 * ((IT) & 1) + 4 * (I), ((IT) % 3) * 20 + 4 * (D)>(acc[I][D]); \
                          else m_mfma<((IT) % 3) * 20 + 4 * (D), 60 + 16 * ((IT) & 1) + 4 * (I)>(acc[I][D]); } while (0)
#define MMROW(IT, I) do { MM(IT, I, 0); MM(IT, I, 1); MM(IT, I, 2); MM(IT, I, 3); MM(IT, I, 4); } while (0)
#define NV(IT) (((IT) + 4 < NK ? 4 : 0) + ((IT) + 2 < NK ? 5 : 0))
#define ALLOW(IT) ((IT) == 0 ? (5 + (NK > 2 ? 4 : 0) + (NK > 3 ? 4 : 0)) : (IT) == 1 ? ((NK > 3 ? 4 : 0) + NV(0)) : NV((IT) - 1))
#define STEP(IT) do { if constexpr ((IT) < NK) { \
    asm volatile("s_waitcnt vmcnt(%c0) lgkmcnt(0)" ::"i"(ALLOW(IT)) : "memory", ACLOB56); \
    if constexpr ((IT) + 1 < NK) WREAD((IT) + 1); \
    SBAR(); \
    MMROW(IT, 0); if constexpr ((IT) + 4 < NK) { WDMA((IT) + 4, 0); WDMA((IT) + 4, 1); } SBAR(); \
    MMROW(IT, 1); if constexpr ((IT) + 4 < NK) { WDMA((IT) + 4, 2); WDMA((IT) + 4, 3); } SBAR(); \
    MMROW(IT, 2); if constexpr ((IT) + 2 < NK) { ALOAD((IT) + 2, 0); ALOAD((IT) + 2, 1); ALOAD((IT) + 2, 2); } SBAR(); \
    MMROW(IT, 3); if constexpr ((IT) + 2 < NK) { ALOAD((IT) + 2, 3); ALOAD((IT) + 2, 4); } SBAR(); \
    } } while (0)
  asm volatile("" ::: "memory", ACLOB56);
  WDMA4(0); ALOAD5(0); asm volatile("s_barrier" ::: "memory");
  WDMA4(1); ALOAD5(1); asm volatile("s_barrier" ::: "memory");
  WDMA4(2); WDMA4(3);
  asm volatile("s_waitcnt vmcnt(22)" ::: "memory");
  WREAD(0);
  STAMP();
  STEP(0); STEP(1); STEP(2); STEP(3); STEP(4); STEP(5); STEP(6); STEP(7);
  asm volatile("s_nop 15" ::: "memory");
  STAMP();
#undef KSTEP
#undef WDMA
#undef ALOAD
#undef WDMA4
#undef ALOAD5
#undef WREAD1
#undef WREAD
#undef MM
#undef MMROW
#undef NV
#undef ALLOW
#undef STEP
  WAIT_L0();
  STAMP();
  {
    LAS char* dst = lds + wid * 20480 + lane * 16;
#pragma unroll
    for (int i = 0; i < 4; ++i)
#pragma unroll
      for (int d = 0; d < 5; ++d) *(LAS f32x4*)(dst + (i * 5 + d) * 1024) = acc[i][d];
  }
  WAIT_L_BAR();
  if (MODE == 0) {
#pragma unroll
    for (int i = 0; i < 2; ++i) {
      const int u = wid + 8 * i;
      if (u < 10) {
        const int a = u / 5, d = u % 5;
        f32x4 t0 = {0.f, 0.f, 0.f, 0.f}, t1 = {0.f, 0.f, 0.f, 0.f};
#pragma unroll
        for (int w = 0; w < 8; ++w) {
          t0 += *(LAS const f32x4*)(lds + w * 20480 + ((2 * a) * 5 + d) * 1024 + lane * 16);
          t1 += *(LAS const f32x4*)(lds + w * 20480 + ((2 * a + 1) * 5 + d) * 1024 + lane * 16);
        }
        const u32x2 b0 = eb0[i], b1 = eb1[i];
        t0 += (f32x4){bflo(b0[0]), bfhi(b0[0]), bflo(b0[1]), bfhi(b0[1])};
        t1 += (f32x4){bflo(b1[0]), bfhi(b1[0]), bflo(b1[1]), bfhi(b1[1])};
        u32x4 w = {cvtpk(gelu_tanh(t0[0]), gelu_tanh(t0[1])), cvtpk(gelu_tanh(t0[2]), gelu_tanh(t0[3])),
                   cvtpk(gelu_tanh(t1[0]), gelu_tanh(t1[1])), cvtpk(gelu_tanh(t1[2]), gelu_tanh(t1[3]))};
        *(u32x4*)(outp + ((size_t)(((dt * 5 + d) * 54 + wt * 2 + a) * 64 + lane)) * 8) = w;
      }
    }
  } else {
#pragma unroll
    for (int i = 0; i < 3; ++i) {
      const int u = wid + 8 * i;
      if (u < 20) {
        f32x4 t0 = {0.f, 0.f, 0.f, 0.f};
#pragma unroll
        for (int w = 0; w < 8; ++w) t0 += *(LAS const f32x4*)(lds + w * 20480 + u * 1024 + lane * 16);
        const int s = wt * 64 + (u / 5) * 16 + (lane & 15);
        const int d0 = dt * 80 + (u % 5) * 16 + 4 * g;
        const float bb = bf2f(ebs[i]);
        const f32x4 hv = eh[i];
        u32x2 w = {cvtpk(t0[0] + bb + hv[0], t0[1] + bb + hv[1]), cvtpk(t0[2] + bb + hv[2], t0[3] + bb + hv[3])};
        *(u32x2*)(outp + (size_t)s * DM + d0) = w;
      }
    }
  }
  STAMP();
  STAMP_END(4 + MODE, DBGP(AF, MODE == 0 ? WS_HN : WS_UF));
}

}

extern "C" void kernel_launch(void* const* d_in, const int* in_sizes, int n_in, void* d_out, int out_size, void* d_ws,
                              size_t ws_size, hipStream_t stream) {
  (void)in_sizes; (void)n_in; (void)out_size; (void)ws_size;
  const u16* x = (const u16*)d_in[0];
  const u16* ckv = (const u16*)d_in[1];
  const u16* wq = (const u16*)d_in[2];
  const u16* wuk = (const u16*)d_in[3];
  const u16* wuv = (const u16*)d_in[4];
  const u16* wo = (const u16*)d_in[5];
  const u16* w1 = (const u16*)d_in[7];
  const u16* b1 = (const u16*)d_in[8];
  const u16* w2 = (const u16*)d_in[9];
  const u16* b2 = (const u16*)d_in[10];
  char* ws = (char*)d_ws;
  u16* XN = (u16*)(ws + WS_XN);
  u16* KF = (u16*)(ws + WS_KF);
  u16* VF = (u16*)(ws + WS_VF);
  u16* CF = (u16*)(ws + WS_CF);
  u16* PT = (u16*)(ws + WS_PT);
  float* H = (float*)(ws + WS_H);
  u16* HN = (u16*)(ws + WS_HN);
  u16* UF = (u16*)(ws + WS_UF);
  (void)hipFuncSetAttribute((const void*)k1_kvproj, hipFuncAttributeMaxDynamicSharedMemorySize, K1_LDS);
  (void)hipFuncSetAttribute((const void*)k2_attn, hipFuncAttributeMaxDynamicSharedMemorySize, K2_LDS);
  (void)hipFuncSetAttribute((const void*)k3_oproj, hipFuncAttributeMaxDynamicSharedMemorySize, K3_LDS);
  (void)hipFuncSetAttribute((const void*)k56_mix<0>, hipFuncAttributeMaxDynamicSharedMemorySize, K56_LDS);
  (void)hipFuncSetAttribute((const void*)k56_mix<1>, hipFuncAttributeMaxDynamicSharedMemorySize, K56_LDS);
  k1_kvproj<<<256, 512, K1_LDS, stream>>>(x, ckv, wuk, wuv, KF, VF, XN, wq);
  k2_attn<<<256, 512, K2_LDS, stream>>>(XN, wq, KF, VF, CF, (float*)(ws + WS_LAM), wo, w1, w2);
  k3_oproj<<<256, 512, K3_LDS, stream>>>(CF, wo, PT);
  k4_ln<<<256, 320, 0, stream>>>(x, PT, H, HN, w1);
  k56_mix<0><<<256, 512, K56_LDS, stream>>>(w1, HN, b1, nullptr, UF, w2);
  k56_mix<1><<<256, 512, K56_LDS, stream>>>(w2, UF, b2, H, (u16*)d_out, nullptr);
}
```

```cpp
#include <hip/hip_runtime.h>
#include <stdint.h>
#include <utility>

namespace {

typedef unsigned short u16;
typedef __bf16 bf16x8 __attribute__((ext_vector_type(8)));
typedef __bf16 bf16x2 __attribute__((ext_vector_type(2)));
typedef short s16x4 __attribute__((ext_vector_type(4)));
typedef short s16x8 __attribute__((ext_vector_type(8)));
typedef float f32x2 __attribute__((ext_vector_type(2)));
typedef float f32x4 __attribute__((ext_vector_type(4)));
typedef float f32x16 __attribute__((ext_vector_type(16)));
typedef unsigned u32x2 __attribute__((ext_vector_type(2)));
typedef unsigned u32x4 __attribute__((ext_vector_type(4)));
#define LAS __attribute__((address_space(3)))
#define DI __device__ __forceinline__
#define SBAR() __builtin_amdgcn_sched_barrier(0)
#define RFL(x) __builtin_amdgcn_readfirstlane(x)

constexpr int SEQ = 2048, DM = 640, DC = 512, NH = 32, HD = 128, FF = 1728;
constexpr float LN_EPS = 1e-5f;
constexpr float QSCALE = 0.08838834764831845f * 1.4426950408889634f;
constexpr float P_LIMIT = 1073741824.0f;
constexpr float NEG_BIG = -1e30f;

constexpr size_t WS_XN = 0;
constexpr size_t WS_KF = WS_XN + (size_t)SEQ * DM * 2;
constexpr size_t WS_VF = WS_KF + (size_t)NH * SEQ * HD * 2;
constexpr size_t WS_CF = WS_VF + (size_t)NH * SEQ * HD * 2;
constexpr size_t WS_PT = WS_CF + (size_t)NH * SEQ * HD * 2;
constexpr size_t WS_H = WS_PT + (size_t)4 * SEQ * DM * 2;
constexpr size_t WS_HN = WS_H + (size_t)SEQ * DM * 4;
constexpr size_t WS_UF = WS_HN + (size_t)SEQ * DM * 2;
constexpr size_t WS_LAM = WS_UF + (size_t)DM * FF * 2;
constexpr size_t WS_END = WS_LAM + 256 * 256 * 4;

DI __amdgpu_buffer_rsrc_t mk_rsrc(const void* p, unsigned bytes) {
  return __builtin_amdgcn_make_buffer_rsrc((void*)p, 0, bytes, 0x00020000);
}
DI void dma1(__amdgpu_buffer_rsrc_t rsrc, unsigned voff, unsigned soff, unsigned ldsaddr) {
  asm volatile("s_mov_b32 m0, %0\n\ts_nop 0\n\tbuffer_load_dwordx4 %1, %2, %3 offen lds"
               ::"s"(ldsaddr), "v"(voff), "s"(rsrc), "s"(soff) : "memory", "m0");
}
DI void warm8k(__amdgpu_buffer_rsrc_t rsrc, unsigned voff128, unsigned soff, unsigned ldsaddr) {
  asm volatile("s_mov_b32 m0, %0\n\ts_nop 0\n\tbuffer_load_dword %1, %2, %3 offen lds"
               ::"s"(ldsaddr), "v"(voff128), "s"(rsrc), "s"(soff) : "memory", "m0");
}
#define CODE_LABEL(name) asm volatile(".Lcode_" #name ":")
#define WARM_CODE(FROM, TO, u, lane, ldsaddr) do { unsigned lo_, hi_, to_; \
    asm volatile("s_getpc_b64 s[98:99]\n\t" \
                 "s_add_u32 %0, s98, .Lcode_" #FROM "@rel32@lo+4\n\ts_addc_u32 %1, s99, .Lcode_" #FROM "@rel32@hi+12\n\t" \
                 "s_getpc_b64 s[98:99]\n\ts_add_u32 %2, s98, .Lcode_" #TO "@rel32@lo+4" \
                 : "=&s"(lo_), "=&s"(hi_), "=&s"(to_) :: "s98", "s99", "scc"); \
    const unsigned sz_ = to_ - lo_;     \
    if (sz_ <= 131072u && (unsigned)(u) * 8192u < sz_) \
      warm8k(mk_rsrc((const void*)(((unsigned long long)hi_ << 32) | lo_), sz_), (unsigned)(lane) * 128u, (unsigned)(u) * 8192u, ldsaddr); \
  } while (0)
#define WAIT_VL_BAR(N) asm volatile("s_waitcnt vmcnt(" #N ") lgkmcnt(0)\n\ts_barrier" ::: "memory")
#define WAIT_L_BAR() asm volatile("s_waitcnt lgkmcnt(0)\n\ts_barrier" ::: "memory")
#define WAIT_L0() asm volatile("s_waitcnt lgkmcnt(0)" ::: "memory")

DI bf16x8 lds_b128(LAS const char* p) { return *(LAS const bf16x8*)p; }
DI bf16x8 lds_tr2(LAS const char* p0, LAS const char* p1) {
  s16x4 lo = __builtin_amdgcn_ds_read_tr16_b64_v4i16((LAS s16x4*)p0);
  s16x4 hi = __builtin_amdgcn_ds_read_tr16_b64_v4i16((LAS s16x4*)p1);
  s16x8 w = __builtin_shufflevector(lo, hi, 0, 1, 2, 3, 4, 5, 6, 7);
  return __builtin_bit_cast(bf16x8, w);
}
DI unsigned cvtpk(float lo, float hi) {
  f32x2 v = {lo, hi};
  bf16x2 b = __builtin_convertvector(v, bf16x2);
  return __builtin_bit_cast(unsigned, b);
}
DI u32x4 pack8(const f32x16& a, int o) {
  u32x4 w = {cvtpk(a[o], a[o + 1]), cvtpk(a[o + 2], a[o + 3]), cvtpk(a[o + 4], a[o + 5]), cvtpk(a[o + 6], a[o + 7])};
  return w;
}
DI float bf2f(unsigned short b) { return __uint_as_float(((unsigned)b) << 16); }
DI float bflo(unsigned w) { return __uint_as_float(w << 16); }
DI float bfhi(unsigned w) { return __uint_as_float(w & 0xffff0000u); }
#define MFMA32(a, b, c) __builtin_amdgcn_mfma_f32_32x32x16_bf16((a), (b), (c), 0, 0, 0)
#define MFMA16(a, b, c) __builtin_amdgcn_mfma_f32_16x16x32_bf16((a), (b), (c), 0, 0, 0)
#define STAMP_DECL()
#define STAMP()
#define STAMP_END(kid, dbg)
DI float swap_max(float v) {
  auto rr = __builtin_amdgcn_permlane32_swap(__float_as_uint(v), __float_as_uint(v), false, false);
  return fmaxf(__uint_as_float(rr[0]), __uint_as_float(rr[1]));
}
DI float swap_add(float v) {
  auto rr = __builtin_amdgcn_permlane32_swap(__float_as_uint(v), __float_as_uint(v), false, false);
  return __uint_as_float(rr[0]) + __uint_as_float(rr[1]);
}

constexpr int K1_LDS = 4 * 32768 + 2048;

template <bool ISV>
DI void k1_body(LAS char* lds, unsigned lds0, int wid, int lane, int head, int tb, const u16* __restrict__ ckv,
                const u16* __restrict__ wsrc, const u16* __restrict__ x, u16* __restrict__ KF, u16* __restrict__ VF,
                u16* __restrict__ XN, const u16* __restrict__ wq
                ) {
  const int wm = wid >> 2, wn = wid & 3;
  const int r = lane & 31, h = lane >> 5;
  const int g1 = (lane >> 4) & 1, q = (lane & 15) >> 2, p = lane & 3;
  const __amdgpu_buffer_rsrc_t rA = mk_rsrc(ckv, SEQ * DC * 2);
  const __amdgpu_buffer_rsrc_t rWm = mk_rsrc(wsrc, DC * NH * HD * 2);
  const unsigned voffA = (lane >> 2) * 1024 + (((lane & 3) ^ ((lane >> 4) & 3)) << 4);
  const unsigned voffW = ((lane >> 2) & 7) * 8192 + (lane >> 5) * 64 + (lane & 3) * 16;
  auto piece = [&](int kt, int slot, int i) __attribute__((always_inline)) {
    const unsigned sb = lds0 + slot * 32768;
    if (i < 2) dma1(rA, voffA, (unsigned)((tb * 256 + (wid * 2 + i) * 16) * 1024 + kt * 64), sb + (wid * 2 + i) * 1024);
    else dma1(rWm, voffW, (unsigned)((kt * 32 + (wid >> 1) * 8) * 8192 + head * 256 + (i - 2) * 128),
              sb + 16384 + ((wid >> 1) * 4 + (wid & 1) * 2 + (i - 2)) * 1024);
  };
  unsigned abase[2];
#pragma unroll
  for (int s = 0; s < 2; ++s) abase[s] = wm * 8192 + r * 64 + (((2 * s + h) ^ ((r >> 2) & 3)) << 4);
  const unsigned wbase = 16384 + h * 4096 + wn * 1024 + q * 64 + g1 * 32 + p * 8;

  f32x16 acc[4][2];
#pragma unroll
  for (int i = 0; i < 4; ++i)
#pragma unroll
    for (int n = 0; n < 2; ++n) acc[i][n] = f32x16{};
  auto rd = [&](bf16x8 (&a)[4], bf16x8 (&w)[2], LAS const char* sb, int s) {
#pragma unroll
    for (int n = 0; n < 2; ++n) w[n] = lds_tr2(sb + wbase + s * 8192 + n * 512, sb + wbase + s * 8192 + n * 512 + 256);
#pragma unroll
    for (int i = 0; i < 4; ++i) a[i] = lds_b128(sb + abase[s] + i * 2048);
  };
  auto mm2 = [&](const bf16x8 (&a)[4], const bf16x8 (&w)[2], int i) {
#pragma unroll
    for (int n = 0; n < 2; ++n) {
      if (ISV) acc[i][n] = MFMA32(a[i], w[n], acc[i][n]);
      else acc[i][n] = MFMA32(w[n], a[i], acc[i][n]);
    }
  };
  bf16x8 aA[4], wA[2], aB[4], wB[2];
  STAMP_DECL();
  STAMP();
#pragma unroll
  for (int kt = 0; kt < 3; ++kt)
#pragma unroll
    for (int i = 0; i < 4; ++i) piece(kt, kt, i);
  auto stage = [&](const int kt, const int slot, const bool more, const bool first) __attribute__((always_inline)) {
    STAMP();
    LAS const char* sb = lds + slot * 32768;
    rd(aA, wA, sb, 0);
    SBAR();
    if (!first) {
      mm2(aB, wB, 0); mm2(aB, wB, 1);
      if (more) piece(kt + 3, (slot + 3) & 3, 0);
      SBAR();
      mm2(aB, wB, 2); mm2(aB, wB, 3);
      if (more) piece(kt + 3, (slot + 3) & 3, 1);
    } else {
      piece(3, 3, 0); piece(3, 3, 1);
    }
    SBAR();
    rd(aB, wB, sb, 1);
    SBAR();
    mm2(aA, wA, 0); mm2(aA, wA, 1);
    if (more) piece(kt + 3, (slot + 3) & 3, 2);
    SBAR();
    mm2(aA, wA, 2); mm2(aA, wA, 3);
    if (more) piece(kt + 3, (slot + 3) & 3, 3);
    SBAR();
  };
  WAIT_VL_BAR(8); stage(0, 0, true, true);
  for (int k4 = 1; k4 < 13; k4 += 4) {
    WAIT_VL_BAR(8); stage(k4, 1, true, false);
    WAIT_VL_BAR(8); stage(k4 + 1, 2, true, false);
    WAIT_VL_BAR(8); stage(k4 + 2, 3, true, false);
    WAIT_VL_BAR(8); stage(k4 + 3, 0, true, false);
  }
  WAIT_VL_BAR(8); stage(13, 1, false, false);
  WAIT_VL_BAR(4); stage(14, 2, false, false);
  WAIT_VL_BAR(0); stage(15, 3, false, false);
  mm2(aB, wB, 0); mm2(aB, wB, 1); mm2(aB, wB, 2); mm2(aB, wB, 3);
  STAMP();
  {
    const __amdgpu_buffer_rsrc_t rQ = mk_rsrc(wq, DM * NH * HD * 2);
    const int u = (blockIdx.x >> 3) * 8 + wid;
    if (u < 80) warm8k(rQ, (unsigned)((lane >> 3) * 8192 + (lane & 7) * 128), (unsigned)(u * 8 * 8192 + (blockIdx.x & 7) * 1024), lds0);
  }
  WARM_CODE(k2, k3, 255 - ((int)(blockIdx.x >> 3) * 8 + wid), lane, lds0 + 256);
  const int token = blockIdx.x * 8 + wid;
  const bool has1 = lane < 16;
  const u32x4 v0 = *(const u32x4*)(x + (size_t)token * DM + lane * 8);
  u32x4 v1 = {0u, 0u, 0u, 0u};
  if (has1) v1 = *(const u32x4*)(x + (size_t)token * DM + 512 + lane * 8);
  SBAR();
#pragma unroll
  for (int i = 0; i < 4; ++i) {
    const int kb32 = tb * 8 + wm * 4 + i;
#pragma unroll
    for (int n = 0; n < 2; ++n) {
#pragma unroll
      for (int sg = 0; sg < 2; ++sg) {
        const u32x4 w = pack8(acc[i][n], 8 * sg);
        if (ISV) {
          const int db = 2 * (wn - 2) + n;
          *(u32x4*)(VF + ((size_t)((((head * 64 + kb32) * 4 + db) * 2 + sg) * 64 + lane)) * 8) = w;
        } else {
          const int db = 2 * wn + n;
          *(u32x4*)(KF + ((size_t)(((head * 64 + kb32) * 8 + 2 * db + sg) * 64 + lane)) * 8) = w;
        }
      }
    }
  }
  STAMP();
  {
    float f0[8], f1[8];
#pragma unroll
    for (int j = 0; j < 4; ++j) {
      f0[2 * j] = bflo(v0[j]); f0[2 * j + 1] = bfhi(v0[j]);
      f1[2 * j] = bflo(v1[j]); f1[2 * j + 1] = bfhi(v1[j]);
    }
    float s = 0.f;
#pragma unroll
    for (int j = 0; j < 8; ++j) s += f0[j] + f1[j];
#pragma unroll
    for (int o = 1; o < 64; o <<= 1) s += __shfl_xor(s, o);
    const float mean = s * (1.0f / DM);
    float sq = 0.f;
#pragma unroll
    for (int j = 0; j < 8; ++j) {
      const float d0 = f0[j] - mean; sq += d0 * d0;
      const float d1 = has1 ? f1[j] - mean : 0.f; sq += d1 * d1;
    }
#pragma unroll
    for (int o = 1; o < 64; o <<= 1) sq += __shfl_xor(sq, o);
    const float rstd = 1.0f / sqrtf(sq * (1.0f / DM) + LN_EPS);
    const int tb32 = token >> 5, rr = token & 31;
    u32x4 o0, o1;
#pragma unroll
    for (int j = 0; j < 4; ++j) {
      o0[j] = cvtpk((f0[2 * j] - mean) * rstd, (f0[2 * j + 1] - mean) * rstd);
      o1[j] = cvtpk((f1[2 * j] - mean) * rstd, (f1[2 * j + 1] - mean) * rstd);
    }
    *(u32x4*)(XN + ((size_t)((tb32 * 40 + (lane >> 1)) * 64 + (lane & 1) * 32 + rr)) * 8) = o0;
    if (has1) *(u32x4*)(XN + ((size_t)((tb32 * 40 + 32 + (lane >> 1)) * 64 + (lane & 1) * 32 + rr)) * 8) = o1;
  }
  STAMP();
  STAMP();
  STAMP_END(0, dbgp);
}

__global__ __launch_bounds__(512, 2) void k1_kvproj(const u16* __restrict__ x, const u16* __restrict__ ckv,
                                                    const u16* __restrict__ wuk, const u16* __restrict__ wuv,
                                                    u16* __restrict__ KF, u16* __restrict__ VF, u16* __restrict__ XN,
                                                    const u16* __restrict__ wq) {
  extern __shared__ __attribute__((aligned(16))) char smem[];
  LAS char* lds = (LAS char*)smem;
  const unsigned lds0 = (unsigned)(uintptr_t)lds;
  const int tid = threadIdx.x, lane = tid & 63;
  const int wid = RFL(tid >> 6);
  const int b = blockIdx.x, xcd = b & 7, idx = b >> 3;
  const int head = xcd * 4 + (idx & 3), tb = idx >> 2;
  CODE_LABEL(k1);
  WARM_CODE(k1, k2, idx * 8 + wid, lane, lds0 + 4 * 32768 + wid * 256);
  const u16* wsrc = (wid & 1) ? wuv : wuk;
#define K1_EXTRA
  if ((wid & 3) >= 2) k1_body<true>(lds, lds0, wid, lane, head, tb, ckv, wsrc, x, KF, VF, XN, wq K1_EXTRA);
  else k1_body<false>(lds, lds0, wid, lane, head, tb, ckv, wsrc, x, KF, VF, XN, wq K1_EXTRA);
}

constexpr int K2_LDS = 163840;
constexpr unsigned L_KB1 = 0, L_VB0 = 16384, L_VA1 = 32768, L_KA0 = 49152, L_KB0 = 65536, L_KA1 = 81920, L_VA0 = 98304,
                   L_VB1 = 114688, L_STASH = 131072;
constexpr int NSTEP = 17;
#define SPRIO 2
#define MPRIO 0
#define TRAILPRIO SPRIO

template <int OFF>
DI void dma1o(__amdgpu_buffer_rsrc_t rsrc, unsigned voff, unsigned soff, unsigned ldsaddr) {
  asm volatile("s_mov_b32 m0, %0\n\ts_nop 0\n\tbuffer_load_dwordx4 %1, %2, %3 offen offset:%4 lds"
               ::"s"(ldsaddr), "v"(voff), "s"(rsrc), "s"(soff), "i"(OFF) : "memory", "m0");
}
DI float fmul_s(float a, float b) { float r; asm("v_mul_f32 %0, %1, %2" : "=v"(r) : "v"(a), "v"(b)); return r; }
#define ACLOB "a8","a9","a10","a11","a12","a13","a14","a15","a16","a17","a18","a19","a20","a21","a22","a23","a24","a25","a26","a27","a28","a29","a30","a31","a32","a33","a34","a35","a36","a37","a38","a39","a40","a41","a42","a43","a44","a45","a46","a47","a48","a49","a50","a51","a52","a53","a54","a55","a56","a57","a58","a59","a60","a61","a62","a63","a64","a65","a66","a67","a68","a69","a70","a71","a72","a73","a74","a75","a76","a77","a78","a79","a80","a81","a82","a83","a84","a85","a86","a87","a88","a89","a90","a91","a92","a93","a94","a95","a96","a97","a98","a99","a100","a101","a102","a103","a104","a105","a106","a107","a108","a109","a110","a111","a112","a113","a114","a115","a116","a117","a118","a119","a120","a121","a122","a123","a124","a125","a126","a127"
constexpr int A_O = 8, A_Q = 72, A_R = 104, NRING = 6;
template <int SLOT, int OFF> DI void ring_read(unsigned addr) {
  asm volatile("ds_read_b128 a[%c1:%c2], %0 offset:%c3" ::"v"(addr), "i"(A_R + 4 * SLOT), "i"(A_R + 4 * SLOT + 3), "i"(OFF) : "memory");
}
template <int S, int OFF> DI void q_read(unsigned addr) {
  asm volatile("ds_read_b128 a[%c1:%c2], %0 offset:%c3" ::"v"(addr), "i"(A_Q + 4 * S), "i"(A_Q + 4 * S + 3), "i"(OFF) : "memory");
}
constexpr int V_NEGM = 64, V_S0 = 80, V_S1 = 96, V_PF = 112;
template <int W, int SLOT, int S, int DST> DI void qk_head() {
  asm volatile("s_waitcnt lgkmcnt(%c0)\n\tv_mfma_f32_32x32x16_bf16 v[%c1:%c2], a[%c3:%c4], a[%c5:%c6], v[%c7:%c8]"
               ::"i"(W), "i"(DST), "i"(DST + 15), "i"(A_R + 4 * SLOT), "i"(A_R + 4 * SLOT + 3), "i"(A_Q + 4 * S), "i"(A_Q + 4 * S + 3),
                 "i"(V_NEGM), "i"(V_NEGM + 15));
}
template <int W, int SLOT, int S, int DST> DI void qk_mfma() {
  asm volatile("s_waitcnt lgkmcnt(%c0)\n\tv_mfma_f32_32x32x16_bf16 v[%c1:%c2], a[%c3:%c4], a[%c5:%c6], v[%c1:%c2]"
               ::"i"(W), "i"(DST), "i"(DST + 15), "i"(A_R + 4 * SLOT), "i"(A_R + 4 * SLOT + 3), "i"(A_Q + 4 * S), "i"(A_Q + 4 * S + 3));
}
template <int W, int SLOT, int D, int P> DI void pv_mfma() {
  asm volatile("s_waitcnt lgkmcnt(%c0)\n\tv_mfma_f32_32x32x16_bf16 a[%c1:%c2], a[%c3:%c4], v[%c5:%c6], a[%c1:%c2]"
               ::"i"(W), "i"(A_O + 16 * D), "i"(A_O + 16 * D + 15), "i"(A_R + 4 * SLOT), "i"(A_R + 4 * SLOT + 3), "i"(V_PF + 4 * P),
                 "i"(V_PF + 4 * P + 3));
}
template <int W, int SLOT, int D, int P> DI void pv_zero() {
  asm volatile("s_waitcnt lgkmcnt(%c0)\n\tv_mfma_f32_32x32x16_bf16 a[%c1:%c2], a[%c3:%c4], v[%c5:%c6], 0"
               ::"i"(W), "i"(A_O + 16 * D), "i"(A_O + 16 * D + 15), "i"(A_R + 4 * SLOT), "i"(A_R + 4 * SLOT + 3), "i"(V_PF + 4 * P),
                 "i"(V_PF + 4 * P + 3));
}
template <int S, int P> DI void exps8(float& ps0, float& ps1, float& ps2, float& ps3) {
  float t0, t1, t2, t3, t4, t5, t6, t7;
  asm volatile("v_exp_f32 %4, v[%c12]\n\tv_exp_f32 %5, v[%c12+1]\n\tv_exp_f32 %6, v[%c12+2]\n\tv_exp_f32 %7, v[%c12+3]\n\t"
               "v_exp_f32 %8, v[%c12+4]\n\tv_exp_f32 %9, v[%c12+5]\n\tv_exp_f32 %10, v[%c12+6]\n\tv_exp_f32 %11, v[%c12+7]\n\t"
               "v_add_f32 %0, %0, %4\n\tv_add_f32 %1, %1, %5\n\tv_cvt_pk_bf16_f32 v[%c13], %4, %5\n\t"
               "v_add_f32 %2, %2, %6\n\tv_add_f32 %3, %3, %7\n\tv_cvt_pk_bf16_f32 v[%c13+1], %6, %7\n\t"
               "v_add_f32 %0, %0, %8\n\tv_add_f32 %1, %1, %9\n\tv_cvt_pk_bf16_f32 v[%c13+2], %8, %9\n\t"
               "v_add_f32 %2, %2, %10\n\tv_add_f32 %3, %3, %11\n\tv_cvt_pk_bf16_f32 v[%c13+3], %10, %11"
               : "+v"(ps0), "+v"(ps1), "+v"(ps2), "+v"(ps3), "=&v"(t0), "=&v"(t1), "=&v"(t2), "=&v"(t3), "=&v"(t4), "=&v"(t5),
                 "=&v"(t6), "=&v"(t7)
               : "i"(S), "i"(V_PF + 4 * P));
}
DI float exps32() {
  float ps0 = 0.f, ps1 = 0.f, ps2 = 0.f, ps3 = 0.f, r;
  exps8<V_S0, 0>(ps0, ps1, ps2, ps3); exps8<V_S0 + 8, 1>(ps0, ps1, ps2, ps3);
  exps8<V_S1, 2>(ps0, ps1, ps2, ps3); exps8<V_S1 + 8, 3>(ps0, ps1, ps2, ps3);
  asm volatile("v_add_f32 %1, %1, %2\n\tv_add_f32 %3, %3, %4\n\tv_add_f32 %0, %1, %3" : "=v"(r), "+v"(ps0), "+v"(ps1), "+v"(ps2), "+v"(ps3));
  return r;
}
DI float rowmax32() {
  float a, b;
  asm volatile("v_max3_f32 %0, v80, v81, v82\n\tv_max3_f32 %1, v83, v84, v85\n\tv_max3_f32 %0, %0, v86, v87\n\tv_max3_f32 %1, %1, v88, v89\n\t"
               "v_max3_f32 %0, %0, v90, v91\n\tv_max3_f32 %1, %1, v92, v93\n\tv_max3_f32 %0, %0, v94, v95\n\tv_max3_f32 %1, %1, v96, v97\n\t"
               "v_max3_f32 %0, %0, v98, v99\n\tv_max3_f32 %1, %1, v100, v101\n\tv_max3_f32 %0, %0, v102, v103\n\tv_max3_f32 %1, %1, v104, v105\n\t"
               "v_max3_f32 %0, %0, v106, v107\n\tv_max3_f32 %1, %1, v108, v109\n\tv_max3_f32 %0, %0, v110, v111\n\tv_max_f32 %0, %0, %1"
               : "=&v"(a), "=&v"(b));
  return a;
}
template <int R> DI void vreg_add(float t) { asm volatile("v_add_f32 v[%c1], v[%c1], %0" ::"v"(t), "i"(R)); }
template <int S, int... E> DI void mask_all_(float ninf, std::integer_sequence<int, E...>) { (vreg_add<S + E>(ninf), ...); }
template <int S, int... E> DI void mask_tri_(int dq, float ninf, std::integer_sequence<int, E...>) {
  (vreg_add<S + E>(((E & 3) + 8 * (E >> 2) > dq) ? ninf : 0.f), ...);
}
template <int I> DI float acc_rd() { float r; asm volatile("v_accvgpr_read_b32 %0, a[%c1]" : "=v"(r) : "i"(I)); return r; }
template <int I> DI void acc_wr(float v) { asm volatile("v_accvgpr_write_b32 a[%c1], %0" ::"v"(v), "i"(I)); }
template <int... E> DI void o_scale_(float f, std::integer_sequence<int, E...>) { (acc_wr<A_O + E>(fmul_s(acc_rd<A_O + E>(), f)), ...); }
template <int R> DI u32x4 acc_scale8(float f) {
  u32x4 o; float t0, t1, t2, t3, t4, t5, t6, t7;
  asm volatile("v_accvgpr_read_b32 %4, a[%c13]\n\tv_accvgpr_read_b32 %5, a[%c13+1]\n\tv_accvgpr_read_b32 %6, a[%c13+2]\n\t"
               "v_accvgpr_read_b32 %7, a[%c13+3]\n\tv_accvgpr_read_b32 %8, a[%c13+4]\n\tv_accvgpr_read_b32 %9, a[%c13+5]\n\t"
               "v_accvgpr_read_b32 %10, a[%c13+6]\n\tv_accvgpr_read_b32 %11, a[%c13+7]\n\t"
               "v_mul_f32 %4, %4, %12\n\tv_mul_f32 %5, %5, %12\n\tv_mul_f32 %6, %6, %12\n\tv_mul_f32 %7, %7, %12\n\t"
               "v_mul_f32 %8, %8, %12\n\tv_mul_f32 %9, %9, %12\n\tv_mul_f32 %10, %10, %12\n\tv_mul_f32 %11, %11, %12\n\t"
               "v_cvt_pk_bf16_f32 %0, %4, %5\n\tv_cvt_pk_bf16_f32 %1, %6, %7\n\tv_cvt_pk_bf16_f32 %2, %8, %9\n\tv_cvt_pk_bf16_f32 %3, %10, %11"
               : "=&v"(o[0]), "=&v"(o[1]), "=&v"(o[2]), "=&v"(o[3]), "=&v"(t0), "=&v"(t1), "=&v"(t2), "=&v"(t3), "=&v"(t4),
                 "=&v"(t5), "=&v"(t6), "=&v"(t7)
               : "v"(f), "i"(R));
  return o;
}
template <int R, int OFF> DI void acc_to_lds(unsigned addr) {
  asm volatile("ds_write_b128 %0, a[%c1:%c2] offset:%c3" ::"v"(addr), "i"(R), "i"(R + 3), "i"(OFF) : "memory");
}
template <int R> DI u32x4 acc_combine8f(const f32x4& w0, const f32x4& w1, float cw, float ca) {
  u32x4 o; float t0, t1, t2, t3, t4, t5, t6, t7;
  asm volatile("v_accvgpr_read_b32 %4, a[%c22]\n\tv_accvgpr_read_b32 %5, a[%c22+1]\n\tv_accvgpr_read_b32 %6, a[%c22+2]\n\t"
               "v_accvgpr_read_b32 %7, a[%c22+3]\n\tv_accvgpr_read_b32 %8, a[%c22+4]\n\tv_accvgpr_read_b32 %9, a[%c22+5]\n\t"
               "v_accvgpr_read_b32 %10, a[%c22+6]\n\tv_accvgpr_read_b32 %11, a[%c22+7]\n\t"
               "v_mul_f32 %4, %4, %21\n\tv_mul_f32 %5, %5, %21\n\tv_mul_f32 %6, %6, %21\n\tv_mul_f32 %7, %7, %21\n\t"
               "v_mul_f32 %8, %8, %21\n\tv_mul_f32 %9, %9, %21\n\tv_mul_f32 %10, %10, %21\n\tv_mul_f32 %11, %11, %21\n\t"
               "v_fma_f32 %4, %12, %20, %4\n\tv_fma_f32 %5, %13, %20, %5\n\tv_fma_f32 %6, %14, %20, %6\n\tv_fma_f32 %7, %15, %20, %7\n\t"
               "v_fma_f32 %8, %16, %20, %8\n\tv_fma_f32 %9, %17, %20, %9\n\tv_fma_f32 %10, %18, %20, %10\n\tv_fma_f32 %11, %19, %20, %11\n\t"
               "v_cvt_pk_bf16_f32 %0, %4, %5\n\tv_cvt_pk_bf16_f32 %1, %6, %7\n\tv_cvt_pk_bf16_f32 %2, %8, %9\n\tv_cvt_pk_bf16_f32 %3, %10, %11"
               : "=&v"(o[0]), "=&v"(o[1]), "=&v"(o[2]), "=&v"(o[3]), "=&v"(t0), "=&v"(t1), "=&v"(t2), "=&v"(t3), "=&v"(t4),
                 "=&v"(t5), "=&v"(t6), "=&v"(t7)
               : "v"(w0[0]), "v"(w0[1]), "v"(w0[2]), "v"(w0[3]), "v"(w1[0]), "v"(w1[1]), "v"(w1[2]), "v"(w1[3]), "v"(cw), "v"(ca),
                 "i"(R));
  return o;
}
DI void scores_add(float aone, float v) {
  asm volatile("s_nop 1\n\tv_mfma_f32_32x32x2_f32 v[80:95], %0, %1, v[80:95]\n\tv_mfma_f32_32x32x2_f32 v[96:111], %0, %1, v[96:111]" ::"v"(aone), "v"(v));
}
DI void negm_set(float aone, float v) { asm volatile("s_nop 1\n\tv_mfma_f32_32x32x2_f32 v[64:79], %0, %1, 0" ::"v"(aone), "v"(v)); }
#define ACC_FENCE() asm volatile("s_nop 15\n\ts_nop 3" ::: "memory")
#define BAR_A(WAITS) asm volatile("s_waitcnt " WAITS "\n\ts_barrier" ::: "memory", "v127", ACLOB)

template <int J> DI void frag_read(unsigned ka, unsigned va) {
  if constexpr (J < 16) ring_read<J % NRING, (J & 1) * 8192 + (J >> 1) * 1024>(ka);
  else { constexpr int i = J - 16; ring_read<J % NRING, (i >> 3) * 8192 + ((i & 3) * 2 + ((i >> 2) & 1)) * 1024>(va); }
}
#define PDX 4
constexpr int PD = PDX;

__global__ __launch_bounds__(512, 2) __attribute__((amdgpu_num_vgpr(64))) void k2_attn(const u16* __restrict__ XN, const u16* __restrict__ wq,
                                                  const u16* __restrict__ KF, const u16* __restrict__ VF,
                                                  u16* __restrict__ CF, float* __restrict__ LAM,
                                                  const u16* __restrict__ wo) {
  extern __shared__ __attribute__((aligned(16))) char smem[];
  LAS char* lds = (LAS char*)smem;
  const unsigned lds0 = (unsigned)(uintptr_t)lds;
  const int tid = threadIdx.x, lane = tid & 63;
  const int wid = RFL(tid >> 6);
  const int b = blockIdx.x, xcd = b & 7, idx = b >> 3;
  const int head = xcd * 4 + (idx >> 3), px = idx & 7;
  CODE_LABEL(k2);
  const int qbH = 15 - px, qbL = px;
  const int rr = wid & 3, c = wid >> 2;
  const int r = lane & 31, h = lane >> 5;
  const int g1 = (lane >> 4) & 1, q4 = (lane & 15) >> 2, p4 = lane & 3;
  const __amdgpu_buffer_rsrc_t rX = mk_rsrc(XN, SEQ * DM * 2);
  const __amdgpu_buffer_rsrc_t rW = mk_rsrc(wq, DM * NH * HD * 2);
  const __amdgpu_buffer_rsrc_t rK = mk_rsrc(KF, NH * SEQ * HD * 2);
  const __amdgpu_buffer_rsrc_t rV = mk_rsrc(VF, NH * SEQ * HD * 2);
  const unsigned vlin = lane * 16;
  const unsigned voffW = ((lane >> 2) & 7) * 8192 + (lane >> 5) * 64 + (lane & 3) * 16;
  const int p1 = 2 * (px + 1);
  auto kvoff = [&](int sg) -> unsigned {
    const int hidx = sg < p1 ? sg : 2 * sg - p1 + c;
    return (unsigned)((head * 64 + 2 * hidx) * 8192);
  };
  STAMP_DECL();
  STAMP();
  const int tbx = (wid < 4) ? (4 * qbH + wid) : (4 * qbL + wid - 4);
  auto q_load = [&](unsigned addr) {
    q_read<0, 0>(addr); q_read<1, 1024>(addr); q_read<2, 2048>(addr); q_read<3, 3072>(addr);
    q_read<4, 4096>(addr); q_read<5, 5120>(addr); q_read<6, 6144>(addr); q_read<7, 7168>(addr);
  };
  {
    auto q_piece = [&](int kt, int i) {
      const unsigned sb = lds0 + (unsigned)(kt % 3) * 49152;
      if (i < 2) dma1(rW, voffW, (unsigned)((kt * 64 + wid * 8) * 8192 + head * 256 + i * 128), sb + (wid * 2 + i) * 1024);
      else dma1(rX, vlin, (unsigned)((tbx * 40 + 4 * kt + (i - 2)) * 1024), sb + 16384 + wid * 4096 + (i - 2) * 1024);
    };
    auto kv_piece = [&](int i) {
      const unsigned part = wid * 2048 + i * 1024;
      dma1(rK, vlin, (unsigned)(head * 64 * 8192) + part, lds0 + L_KA0 + part);
    };
#pragma unroll
    for (int i = 0; i < 6; ++i) q_piece(0, i);
#pragma unroll
    for (int i = 0; i < 6; ++i) q_piece(1, i);
    f32x16 qa[2][2];
#pragma unroll
    for (int i = 0; i < 2; ++i)
#pragma unroll
      for (int n = 0; n < 2; ++n) qa[i][n] = f32x16{};
    const unsigned wbase = h * 2048 + c * 1024 + q4 * 64 + g1 * 32 + p4 * 8;
    const unsigned xbase = 16384 + rr * 4096 + lane * 16;
    bf16x8 wA[2], xA[2], wB[2], xB[2];
    auto rdq = [&](bf16x8 (&w)[2], bf16x8 (&x)[2], LAS const char* sb, int s) {
      w[0] = lds_tr2(sb + wbase + s * 4096, sb + wbase + s * 4096 + 256);
      x[0] = lds_b128(sb + xbase + s * 1024);
      w[1] = lds_tr2(sb + wbase + s * 4096 + 512, sb + wbase + s * 4096 + 768);
      x[1] = lds_b128(sb + xbase + 16384 + s * 1024);
    };
    auto mmq = [&](const bf16x8 (&w)[2], const bf16x8 (&x)[2]) {
      qa[0][0] = MFMA32(w[0], x[0], qa[0][0]);
      qa[1][0] = MFMA32(w[0], x[1], qa[1][0]);
      qa[0][1] = MFMA32(w[1], x[0], qa[0][1]);
      qa[1][1] = MFMA32(w[1], x[1], qa[1][1]);
    };
    for (int kt = 0; kt < 10; ++kt) {
      if (kt < 9) { WAIT_VL_BAR(6); } else { WAIT_VL_BAR(2); }
      LAS const char* sb = lds + (unsigned)(kt % 3) * 49152;
      auto piece = [&](int i) {
        if (kt < 8) q_piece(kt + 2, i);
        else if (kt == 8 && i < 2) kv_piece(i);
      };
      rdq(wA, xA, sb, 0);
      SBAR();
      if (kt > 0) mmq(wB, xB);
      piece(0); SBAR();
      rdq(wB, xB, sb, 1);
      SBAR();
      mmq(wA, xA);
      piece(1); piece(2); SBAR();
      rdq(wA, xA, sb, 2);
      SBAR();
      mmq(wB, xB);
      piece(3); piece(4); SBAR();
      rdq(wB, xB, sb, 3);
      SBAR();
      mmq(wA, xA);
      piece(5); SBAR();
    }
    mmq(wB, xB);
    STAMP();
    WAIT_L_BAR();
#pragma unroll
    for (int i = 0; i < 2; ++i) {
      LAS char* dst = lds + (i == 0 ? L_STASH : 0u) + rr * 8192 + lane * 16;
#pragma unroll
      for (int n = 0; n < 2; ++n) {
        const f32x16 t = qa[i][n] * QSCALE;
#pragma unroll
        for (int sg = 0; sg < 2; ++sg) *(LAS u32x4*)(dst + (4 * c + 2 * n + sg) * 1024) = pack8(t, 8 * sg);
      }
    }
    WAIT_L_BAR();
    q_load(lds0 + (c ? 0u : L_STASH) + rr * 8192 + lane * 16);
    BAR_A("vmcnt(0) lgkmcnt(0)");
  }
  STAMP();

  negm_set(0.f, 0.f);
  float mref = 0.f, lsum = 0.f, mfin = 0.f, lfin = 0.f;
  const unsigned kown0 = c ? L_KB0 : L_KA0, kown1 = c ? L_KB1 : L_KA1;
  const unsigned vown0 = c ? L_VB0 : L_VA0, vown1 = c ? L_VB1 : L_VA1;
  const unsigned lrd = lds0 + lane * 16;
  float* lamp = LAM + (size_t)b * 256 + rr * 64;
  f32x2 mlA = {0.f, 0.f};

  auto refix = [&](const bool first) __attribute__((always_inline)) {
    const float rm = swap_max(rowmax32());
    const float dl = first ? fmaxf(rm, NEG_BIG) : fmaxf(rm, 0.f);
    const float f = first ? 0.f : __builtin_amdgcn_exp2f(-dl);
    mref = first ? dl : mref + dl;
    lsum = first ? 0.f : lsum * f;
    int ln = lane;
    asm volatile("" : "+v"(ln));
    const float aone = ln < 32 ? 1.f : 0.f;
    scores_add(aone, -dl);
    negm_set(aone, -mref);
    ACC_FENCE();
    if (!first) { o_scale_(f, std::make_integer_sequence<int, 64>{}); ACC_FENCE(); }
  };
#define TRAIL() do { pv_mfma<0, 28 % NRING, 0, 3>(); pv_mfma<0, 29 % NRING, 1, 3>(); pv_mfma<0, 30 % NRING, 2, 3>(); pv_mfma<0, 31 % NRING, 3, 3>(); SBAR(); } while (0)
  auto splain = [&]() __attribute__((always_inline)) {
    __builtin_amdgcn_s_setprio(TRAILPRIO);
    TRAIL();
    if (TRAILPRIO != SPRIO) __builtin_amdgcn_s_setprio(SPRIO);
    float pst = exps32();
    if (__builtin_expect(__any(pst > P_LIMIT), 0)) { refix(false); pst = exps32(); }
    asm volatile("v_add_f32 %0, %0, %1" : "+v"(lsum) : "v"(pst));
    __builtin_amdgcn_s_setprio(MPRIO);
  };
  auto sgen = [&](const int tau) __attribute__((always_inline)) {
    __builtin_amdgcn_s_setprio(SPRIO);
    if (tau > 0) TRAIL();
    const bool lastL = c == 1 && tau == p1 - 1;
    const bool diag = tau == NSTEP - 1 || (c == 1 && (tau == p1 - 2 || tau == p1 - 1));
    const bool first = tau == 0 || (c == 1 && tau == p1);
    int ln = lane;
    asm volatile("" : "+v"(ln));
    if (diag) {
      const int hp = tau == NSTEP - 1 ? c : tau - (p1 - 2);
      float ninf = -INFINITY;
      asm volatile("" : "+v"(ninf));
      const int dq = (ln & 31) - 4 * (ln >> 5);
      if (2 * hp > rr) mask_all_<V_S0>(ninf, std::make_integer_sequence<int, 16>{});
      else if (2 * hp == rr) mask_tri_<V_S0>(dq, ninf, std::make_integer_sequence<int, 16>{});
      if (2 * hp + 1 > rr) mask_all_<V_S1>(ninf, std::make_integer_sequence<int, 16>{});
      else if (2 * hp + 1 == rr) mask_tri_<V_S1>(dq, ninf, std::make_integer_sequence<int, 16>{});
    }
    if (first && tau > 0) {
      ACC_FENCE();
      const float inv = 1.0f / lfin;
      u16* dstp = CF + ((size_t)(((4 * qbL + rr) * 32 + head) * 8) * 64 + ln) * 8;
      *(u32x4*)(dstp) = acc_scale8<A_O + 0>(inv);          *(u32x4*)(dstp + 512) = acc_scale8<A_O + 8>(inv);
      *(u32x4*)(dstp + 1024) = acc_scale8<A_O + 16>(inv);  *(u32x4*)(dstp + 1536) = acc_scale8<A_O + 24>(inv);
      *(u32x4*)(dstp + 2048) = acc_scale8<A_O + 32>(inv);  *(u32x4*)(dstp + 2560) = acc_scale8<A_O + 40>(inv);
      *(u32x4*)(dstp + 3072) = acc_scale8<A_O + 48>(inv);  *(u32x4*)(dstp + 3584) = acc_scale8<A_O + 56>(inv);
    }
    if (first) refix(true);
    float pst = exps32();
    if (!first && __any(pst > P_LIMIT)) { refix(false); pst = exps32(); }
    asm volatile("v_add_f32 %0, %0, %1" : "+v"(lsum) : "v"(pst));
    if (lastL || tau == NSTEP - 1) {
      mfin = mref; lfin = swap_add(lsum);
      if (lastL) {
        negm_set(0.f, 0.f);
        q_load(lds0 + L_STASH + rr * 8192 + ln * 16);
      } else {
        f32x2* lp = (f32x2*)lamp + (ln & 31);
        if (c == 0) { if (ln < 32) *lp = (f32x2){mfin, lfin}; }
        else mlA = *lp;
      }
    }
    __builtin_amdgcn_s_setprio(0);
  };
#define KVDMA(i) do { if ((i) == 0) dma1o<0>(rK, vlin, gk, lk); else if ((i) == 1) dma1o<1024>(rK, vlin, gk, lk); \
    else if ((i) == 2) dma1o<2048>(rK, vlin, gk, lk); else if ((i) == 3) dma1o<3072>(rK, vlin, gk, lk); \
    else if ((i) == 4) dma1o<0>(rV, vlin, gv, lv); else if ((i) == 5) dma1o<1024>(rV, vlin, gv, lv); \
    else if ((i) == 6) dma1o<2048>(rV, vlin, gv, lv); else dma1o<3072>(rV, vlin, gv, lv); } while (0)
#define MSTEP(J, FIRST, LAST, DMA, ZO) do { \
    constexpr int out_ = ((J) + PD - 1 < (LAST) ? (J) + PD - 1 : (LAST)) - (J);     \
    if constexpr ((J) < 2) qk_head<out_, (J) % NRING, 0, ((J) & 1) ? V_S1 : V_S0>(); \
    else if constexpr ((J) < 16) qk_mfma<out_, (J) % NRING, ((J) >> 1), ((J) & 1) ? V_S1 : V_S0>(); \
    else if constexpr ((J) < 20 && (ZO)) { if (zero_o) pv_zero<out_, (J) % NRING, (J) & 3, 0>(); else pv_mfma<out_, (J) % NRING, (J) & 3, 0>(); } \
    else pv_mfma<out_, (J) % NRING, (J) & 3, (((J) - 16) >> 2)>(); \
    if constexpr ((J) + PD <= (LAST)) frag_read<(J) + PD>(ka, va); \
    if constexpr ((DMA) && ((J) & 1) && (J) < 16) KVDMA((J) >> 1); \
    SBAR(); } while (0)
#define MSTEP4(J, FIRST, LAST, DMA, ZO) MSTEP(J, FIRST, LAST, DMA, ZO); MSTEP((J) + 1, FIRST, LAST, DMA, ZO); MSTEP((J) + 2, FIRST, LAST, DMA, ZO); MSTEP((J) + 3, FIRST, LAST, DMA, ZO)
#define KSLOT(t) (((t) < p1) ? (((t) & 1) ? L_KA1 : L_KA0) : (((t) & 1) ? kown1 : kown0))
#define VSLOT(t) (((t) < p1) ? (((t) & 1) ? L_VA1 : L_VA0) : (((t) & 1) ? vown1 : vown0))
#define STAGE_ADDR(t) \
      const unsigned gk = ((t) + 1 < NSTEP && (c == 0 || (t) + 1 >= p1)) ? kvoff((t) + 1) + rr * 4096 : 0x7ff00000u; \
      const unsigned gv = (c == 0 || (t) >= p1) ? kvoff(t) + rr * 4096 : 0x7ff00000u; \
      const unsigned lk = lds0 + ((((t) + 1) & 1) ? kown1 : kown0) + rr * 4096; \
      const unsigned lv = lds0 + (((t) & 1) ? vown1 : vown0) + rr * 4096;

  if (c == 1) asm volatile("s_barrier" ::: "memory");
  {
    const unsigned ka = lrd + L_KA0, va = 0;
    STAGE_ADDR(0)
    constexpr bool zero_o = false;
    frag_read<0>(ka, va); frag_read<1>(ka, va); frag_read<2>(ka, va); frag_read<3>(ka, va);
    SBAR();
    MSTEP4(0, 0, 15, true, false); MSTEP4(4, 0, 15, true, false); MSTEP4(8, 0, 15, true, false); MSTEP4(12, 0, 15, true, false);
    BAR_A("lgkmcnt(0)");
    ACC_FENCE();
    sgen(0);
    BAR_A("vmcnt(0) lgkmcnt(0)");
  }
#define MPHASE(tau, DMA, ZO) do { \
      const bool zero_o = (tau) == 1 || (c == 1 && (tau) == p1 + 1);     \
      (void)zero_o; \
      const unsigned ka = lrd + KSLOT(tau); \
      const unsigned va = lrd + VSLOT((tau) - 1); \
      STAGE_ADDR(tau) \
      (void)gk; (void)gv; (void)lk; (void)lv; \
      frag_read<0>(ka, va); frag_read<1>(ka, va); frag_read<2>(ka, va); frag_read<3>(ka, va); \
      SBAR(); \
      MSTEP4(0, 0, 31, DMA, ZO); MSTEP4(4, 0, 31, DMA, ZO); MSTEP4(8, 0, 31, DMA, ZO); MSTEP4(12, 0, 31, DMA, ZO); \
      MSTEP4(16, 0, 31, DMA, ZO); MSTEP4(20, 0, 31, DMA, ZO); MSTEP4(24, 0, 31, DMA, ZO); \
      BAR_A("lgkmcnt(0)"); } while (0)
  int tau = 1;
  while (tau < NSTEP) {
    const bool special = tau == 1 || tau == NSTEP - 1 || (c == 1 && tau >= p1 - 2 && tau <= p1 + 1);
    if (special) {
      STAMP();
      MPHASE(tau, true, true);
      STAMP();
      sgen(tau);
      BAR_A("vmcnt(0) lgkmcnt(0)");
      ++tau;
    } else if (c == 1 && tau < p1 - 2) {
      do {
        STAMP();
        MPHASE(tau, false, false);
        STAMP();
        splain();
        BAR_A("lgkmcnt(0)");
        ++tau;
      } while (tau < p1 - 2);
    } else {
      do {
        STAMP();
        MPHASE(tau, true, false);
        STAMP();
        splain();
        BAR_A("vmcnt(0) lgkmcnt(0)");
        ++tau;
      } while (tau < NSTEP - 1);
    }
  }
#undef MPHASE
#undef TRAIL
  {
    const unsigned v128 = lane * 128;
    const unsigned dead = lds0 + vown1 + rr * 256;
    const int u = idx * 8 + wid;
    if (u < 160) warm8k(mk_rsrc(wo, NH * HD * DM * 2), v128, (unsigned)((xcd >> 1) * 1024 * 1280 + u * 8192), dead);
  }
  WARM_CODE(k3, k5, 255 - (idx * 8 + wid), lane, lds0 + vown1 + 1024 + rr * 256);
  {
    const unsigned ka = 0, va = lrd + VSLOT(NSTEP - 1);
    const unsigned gk = 0, gv = 0, lk = 0, lv = 0;
    (void)gk; (void)gv; (void)lk; (void)lv; (void)ka;
    const bool zero_o = c == 1 && p1 == NSTEP - 1;
    frag_read<16>(ka, va); frag_read<17>(ka, va); frag_read<18>(ka, va); frag_read<19>(ka, va);
    SBAR();
    MSTEP4(16, 16, 31, false, true); MSTEP4(20, 16, 31, false, true); MSTEP4(24, 16, 31, false, true); MSTEP4(28, 16, 31, false, true);
    BAR_A("lgkmcnt(0)");
    int ln = lane;
    asm volatile("" : "+v"(ln));
    const unsigned mine = lds0 + (c ? L_KB1 : L_KA0) + ((rr & 2) ? (c ? L_KB0 - L_KB1 : L_KA1 - L_KA0) : 0u) + (rr & 1) * 8192 + ln * 16;
    const unsigned theirs = (c ? L_KA0 : L_KB1) + ((rr & 2) ? (c ? L_KA1 - L_KA0 : L_KB0 - L_KB1) : 0u) + (rr & 1) * 8192 + ln * 16;
    ACC_FENCE();
    if (c == 0) {
      acc_to_lds<A_O + 32, 0>(mine); acc_to_lds<A_O + 36, 1024>(mine); acc_to_lds<A_O + 40, 2048>(mine); acc_to_lds<A_O + 44, 3072>(mine);
      acc_to_lds<A_O + 48, 4096>(mine); acc_to_lds<A_O + 52, 5120>(mine); acc_to_lds<A_O + 56, 6144>(mine); acc_to_lds<A_O + 60, 7168>(mine);
      BAR_A("lgkmcnt(0)");
    } else {
      acc_to_lds<A_O + 0, 0>(mine); acc_to_lds<A_O + 4, 1024>(mine); acc_to_lds<A_O + 8, 2048>(mine); acc_to_lds<A_O + 12, 3072>(mine);
      acc_to_lds<A_O + 16, 4096>(mine); acc_to_lds<A_O + 20, 5120>(mine); acc_to_lds<A_O + 24, 6144>(mine); acc_to_lds<A_O + 28, 7168>(mine);
      if (ln < 32) *(LAS f32x2*)(lds + L_STASH + rr * 256 + ln * 8) = (f32x2){mfin, lfin};
    }
    BAR_A("lgkmcnt(0)");
    {
      const f32x2 mo = c ? mlA : *(LAS const f32x2*)(lds + L_STASH + rr * 256 + (ln & 31) * 8);
      const float m = fmaxf(mo[0], mfin);
      const float eo = __builtin_amdgcn_exp2f(mo[0] - m), em = __builtin_amdgcn_exp2f(mfin - m);
      const float inv = 1.0f / (mo[1] * eo + lfin * em);
      const float co = eo * inv, cm = em * inv;
      f32x4 w[8];
#pragma unroll
      for (int i = 0; i < 8; ++i) w[i] = *(LAS const f32x4*)(lds + theirs + i * 1024);
      u16* dstp = CF + ((size_t)(((4 * qbH + rr) * 32 + head) * 8 + 4 * c) * 64 + ln) * 8;
      if (c == 0) {
        *(u32x4*)(dstp) = acc_combine8f<A_O + 0>(w[0], w[1], co, cm);         *(u32x4*)(dstp + 512) = acc_combine8f<A_O + 8>(w[2], w[3], co, cm);
        *(u32x4*)(dstp + 1024) = acc_combine8f<A_O + 16>(w[4], w[5], co, cm); *(u32x4*)(dstp + 1536) = acc_combine8f<A_O + 24>(w[6], w[7], co, cm);
      } else {
        *(u32x4*)(dstp) = acc_combine8f<A_O + 32>(w[0], w[1], co, cm);        *(u32x4*)(dstp + 512) = acc_combine8f<A_O + 40>(w[2], w[3], co, cm);
        *(u32x4*)(dstp + 1024) = acc_combine8f<A_O + 48>(w[4], w[5], co, cm); *(u32x4*)(dstp + 1536) = acc_combine8f<A_O + 56>(w[6], w[7], co, cm);
      }
    }
  }
#undef KSLOT
#undef VSLOT
#undef STAGE_ADDR
#undef KVDMA
#undef MSTEP
#undef MSTEP4
  STAMP();
  STAMP_END(1, DBGP(CF, WS_CF));
}

constexpr int K3_STAGE = 37888, K3_NST = 4, K3_LDS = K3_STAGE * K3_NST;

__global__ __launch_bounds__(512, 2) void k3_oproj(const u16* __restrict__ CF, const u16* __restrict__ wo,
                                                   u16* __restrict__ PT) {
  extern __shared__ __attribute__((aligned(16))) char smem[];
  LAS char* lds = (LAS char*)smem;
  const unsigned lds0 = (unsigned)(uintptr_t)lds;
  const int tid = threadIdx.x, lane = tid & 63;
  const int wid = RFL(tid >> 6);
  const int b = blockIdx.x, xcd = b & 7, idx = b >> 3;
  CODE_LABEL(k3);
  const int ks = xcd >> 1, panel = (xcd & 1) * 8 + (idx >> 2), nt = idx & 3;
  const int tbi = wid & 3, kp = wid >> 2;
  const int h = lane >> 5, g1 = (lane >> 4) & 1, q4 = (lane & 15) >> 2, p4 = lane & 3;
  const __amdgpu_buffer_rsrc_t rC = mk_rsrc(CF, NH * SEQ * HD * 2);
  const __amdgpu_buffer_rsrc_t rW = mk_rsrc(wo, NH * HD * DM * 2);
  const unsigned vlin = lane * 16;
  unsigned voffW[3];
#pragma unroll
  for (int i = 0; i < 3; ++i) {
    const int pc = wid + 8 * i;
    const int u = 2 * pc + (lane >> 5);
    const int kb = u / 5, nb = u % 5;
    voffW[i] = (unsigned)((kb * 8 + ((lane >> 2) & 7)) * 1280 + (nb * 32 + (lane & 3) * 8) * 2);
  }
  const bool lo = wid < 4;
  const unsigned sW0 = (unsigned)(ks * 1024 * 1280 + nt * 320);
  const unsigned sC0 = (unsigned)(((panel * 4 + tbi) * 256 + ks * 64) * 1024);
  const __amdgpu_buffer_rsrc_t r2 = lo ? rW : rC;
  const unsigned v2 = lo ? voffW[2] : vlin;
  const unsigned s2 = lo ? sW0 : sC0, st2 = lo ? 81920u : 4096u, l2 = lo ? (wid + 16) * 1024 : 20480 + tbi * 4096;
  const unsigned s3 = sC0 + (lo ? 3072u : 1024u), l3 = 20480 + tbi * 4096 + (lo ? 3072 : 1024);
  const unsigned s4 = lo ? 0x7ff00000u : sC0 + 2048u, l4 = lo ? 36864 : 20480 + tbi * 4096 + 2048;
  auto piece = [&](int j, int slot, int i) __attribute__((always_inline)) {
    const unsigned sb = lds0 + slot * K3_STAGE;
    if (i == 0) dma1(rW, voffW[0], sW0 + j * 81920, sb + wid * 1024);
    else if (i == 1) dma1(rW, voffW[1], sW0 + j * 81920, sb + (wid + 8) * 1024);
    else if (i == 2) dma1(r2, v2, s2 + j * st2, sb + l2);
    else if (i == 3) dma1(rC, vlin, s3 + j * 4096, sb + l3);
    else dma1(rC, vlin, s4 + j * 4096, sb + l4);
  };
  f32x16 acc[5];
#pragma unroll
  for (int n = 0; n < 5; ++n) acc[n] = f32x16{};
  const unsigned wbase = h * 256 + q4 * 64 + g1 * 32 + p4 * 8 + kp * 4 * 2560;
  const unsigned cbase = 20480 + tbi * 4096 + lane * 16 + kp * 2048;
  auto rd = [&](bf16x8& cf, bf16x8 (&w)[5], LAS const char* sb, int si) {
    cf = lds_b128(sb + cbase + si * 1024);
#pragma unroll
    for (int n = 0; n < 5; ++n)
      w[n] = lds_tr2(sb + wbase + (2 * si) * 2560 + n * 512, sb + wbase + (2 * si + 1) * 2560 + n * 512);
  };
  bf16x8 cA, cB, wA[5], wB[5];
  STAMP_DECL();
  STAMP();
#pragma unroll
  for (int j = 0; j < 3; ++j)
#pragma unroll
    for (int i = 0; i < 5; ++i) piece(j, j, i);
  STAMP();
  auto stage = [&](const int j, const int slot, const bool more, const bool first) __attribute__((always_inline)) {
    STAMP();
    LAS const char* sb = lds + slot * K3_STAGE;
    rd(cA, wA, sb, 0);
    SBAR();
    if (!first) {
      acc[0] = MFMA32(wB[0], cB, acc[0]); acc[1] = MFMA32(wB[1], cB, acc[1]);
      if (more) piece(j + 3, (slot + 3) & 3, 0);
      SBAR();
      acc[2] = MFMA32(wB[2], cB, acc[2]); acc[3] = MFMA32(wB[3], cB, acc[3]);
      if (more) piece(j + 3, (slot + 3) & 3, 1);
      SBAR();
      acc[4] = MFMA32(wB[4], cB, acc[4]);
    } else {
      piece(3, 3, 0); piece(3, 3, 1);
    }
    SBAR();
    rd(cB, wB, sb, 1);
    SBAR();
    acc[0] = MFMA32(wA[0], cA, acc[0]);
    if (more) piece(j + 3, (slot + 3) & 3, 2);
    SBAR();
    acc[1] = MFMA32(wA[1], cA, acc[1]); acc[2] = MFMA32(wA[2], cA, acc[2]);
    if (more) piece(j + 3, (slot + 3) & 3, 3);
    SBAR();
    acc[3] = MFMA32(wA[3], cA, acc[3]); acc[4] = MFMA32(wA[4], cA, acc[4]);
    if (more) piece(j + 3, (slot + 3) & 3, 4);
    SBAR();
  };
  WAIT_VL_BAR(10); stage(0, 0, true, true);
  for (int j4 = 1; j4 < 13; j4 += 4) {
    WAIT_VL_BAR(10); stage(j4, 1, true, false);
    WAIT_VL_BAR(10); stage(j4 + 1, 2, true, false);
    WAIT_VL_BAR(10); stage(j4 + 2, 3, true, false);
    WAIT_VL_BAR(10); stage(j4 + 3, 0, true, false);
  }
  WAIT_VL_BAR(10); stage(13, 1, false, false);
  WAIT_VL_BAR(5); stage(14, 2, false, false);
  WAIT_VL_BAR(0); stage(15, 3, false, false);
#pragma unroll
  for (int n = 0; n < 5; ++n) acc[n] = MFMA32(wB[n], cB, acc[n]);
  STAMP();
  WAIT_L_BAR();
  {
    LAS char* mine = lds + (tbi * 2 + kp) * 12288 + lane * 16;
    LAS const char* theirs = lds + (tbi * 2 + (kp ^ 1)) * 12288 + lane * 16;
    auto put = [&](const f32x16& a, int slot) {
#pragma unroll
      for (int gq = 0; gq < 4; ++gq)
        *(LAS f32x4*)(mine + (slot * 4 + gq) * 1024) = (f32x4){a[4 * gq], a[4 * gq + 1], a[4 * gq + 2], a[4 * gq + 3]};
    };
    auto fin = [&](const f32x16& a, int slot, int n) {
      f32x16 t;
#pragma unroll
      for (int gq = 0; gq < 4; ++gq) {
        const f32x4 v = *(LAS const f32x4*)(theirs + (slot * 4 + gq) * 1024);
#pragma unroll
        for (int e = 0; e < 4; ++e) t[4 * gq + e] = a[4 * gq + e] + v[e];
      }
      u16* dst = PT + ((size_t)((((ks * 64 + panel * 4 + tbi) * 20 + nt * 5 + n) * 2) * 64 + lane)) * 8;
      *(u32x4*)dst = pack8(t, 0);
      *(u32x4*)(dst + 512) = pack8(t, 8);
    };
    if (kp == 0) { put(acc[3], 0); put(acc[4], 1); } else { put(acc[0], 0); put(acc[1], 1); put(acc[2], 2); }
    WAIT_L_BAR();
    if (kp == 0) { fin(acc[0], 0, 0); fin(acc[1], 1, 1); fin(acc[2], 2, 2); } else { fin(acc[3], 0, 3); fin(acc[4], 1, 4); }
  }
  STAMP();
  STAMP();
  STAMP_END(2, DBGP(PT, WS_PT));
}

__global__ __launch_bounds__(320) void k4_ln(const u16* __restrict__ x, const u16* __restrict__ PT,
                                             float* __restrict__ H, u16* __restrict__ HN, const u16* __restrict__ w1) {
  __shared__ __attribute__((aligned(16))) u16 tile[8 * 648];
  __shared__ float red[2][5][8];
  __shared__ __attribute__((aligned(16))) char dead[5 * 256];
  const int tid = threadIdx.x, lane = tid & 63, wid = RFL(tid >> 6);
  const int b = blockIdx.x;
  CODE_LABEL(k4);
  const int r8 = lane & 7, hh = (lane >> 3) & 1, sg = (lane >> 4) & 1, ml = lane >> 5;
  const int token = b * 8 + r8, tb32 = token >> 5, rr = token & 31;
  STAMP_DECL();
  STAMP();
  u32x2 xv[2][2];
  u32x4 pv[2][4];
#pragma unroll
  for (int i = 0; i < 2; ++i) {
    const int mb = wid * 4 + 2 * i + ml;
    const int m0 = mb * 32 + sg * 16 + hh * 4;
    xv[i][0] = *(const u32x2*)(x + (size_t)token * DM + m0);
    xv[i][1] = *(const u32x2*)(x + (size_t)token * DM + m0 + 8);
#pragma unroll
    for (int ks = 0; ks < 4; ++ks)
      pv[i][ks] = *(const u32x4*)(PT + ((size_t)((((ks * 64 + tb32) * 20 + mb) * 2 + sg) * 64 + hh * 32 + rr)) * 8);
  }
  SBAR();
  STAMP();
  float v[2][8];
  float s = 0.f;
#pragma unroll
  for (int i = 0; i < 2; ++i) {
    const int m0 = (wid * 4 + 2 * i + ml) * 32 + sg * 16 + hh * 4;
#pragma unroll
    for (int q = 0; q < 4; ++q) {
      const unsigned xw = xv[i][q >> 1][q & 1];
      v[i][2 * q] = bflo(xw) + ((bflo(pv[i][0][q]) + bflo(pv[i][1][q])) + (bflo(pv[i][2][q]) + bflo(pv[i][3][q])));
      v[i][2 * q + 1] = bfhi(xw) + ((bfhi(pv[i][0][q]) + bfhi(pv[i][1][q])) + (bfhi(pv[i][2][q]) + bfhi(pv[i][3][q])));
    }
    *(f32x4*)(H + (size_t)token * DM + m0) = (f32x4){v[i][0], v[i][1], v[i][2], v[i][3]};
    *(f32x4*)(H + (size_t)token * DM + m0 + 8) = (f32x4){v[i][4], v[i][5], v[i][6], v[i][7]};
#pragma unroll
    for (int e = 0; e < 8; ++e) s += v[i][e];
  }
  STAMP();
  {
    const int u = (b >> 3) * 5 + wid;
    const int a4 = (b & 7) >> 1;
    const unsigned dd = (unsigned)(uintptr_t)(LAS char*)dead + wid * 256;
    const __amdgpu_buffer_rsrc_t r1 = mk_rsrc(w1, SEQ * FF * 2);
#pragma unroll
    for (int i = 0; i < 2; ++i) {
      const int line = (u + 160 * i) * 64 + lane;
      const unsigned off = line < 14336 ? (unsigned)((line / 7) * (FF * 2) + a4 * 896 + (line % 7) * 128) : 0x7ff00000u;
      warm8k(r1, off, 0u, dd);
    }
  }
  WARM_CODE(k5, end, 159 - ((b >> 3) * 5 + wid), lane, (unsigned)(uintptr_t)(LAS char*)dead + wid * 256);
  s += __shfl_xor(s, 8); s += __shfl_xor(s, 16); s += __shfl_xor(s, 32);
  if (lane < 8) red[0][wid][lane] = s;
  WAIT_L_BAR();
  STAMP();
  const float mean = ((red[0][0][r8] + red[0][1][r8]) + (red[0][2][r8] + red[0][3][r8]) + red[0][4][r8]) * (1.0f / DM);
  float sq = 0.f;
#pragma unroll
  for (int i = 0; i < 2; ++i)
#pragma unroll
    for (int e = 0; e < 8; ++e) { v[i][e] -= mean; sq += v[i][e] * v[i][e]; }
  sq += __shfl_xor(sq, 8); sq += __shfl_xor(sq, 16); sq += __shfl_xor(sq, 32);
  if (lane < 8) red[1][wid][lane] = sq;
  WAIT_L_BAR();
  const float var = ((red[1][0][r8] + red[1][1][r8]) + (red[1][2][r8] + red[1][3][r8]) + red[1][4][r8]) * (1.0f / DM);
  const float r1 = 1.0f / sqrtf(var + LN_EPS);
  const float r2 = 1.0f / sqrtf(var * r1 * r1 + LN_EPS);
  const float sc = r1 * r2;
#pragma unroll
  for (int i = 0; i < 2; ++i) {
    const int m0 = (wid * 4 + 2 * i + ml) * 32 + sg * 16 + hh * 4;
    *(u32x2*)(tile + r8 * 648 + m0) = (u32x2){cvtpk(v[i][0] * sc, v[i][1] * sc), cvtpk(v[i][2] * sc, v[i][3] * sc)};
    *(u32x2*)(tile + r8 * 648 + m0 + 8) = (u32x2){cvtpk(v[i][4] * sc, v[i][5] * sc), cvtpk(v[i][6] * sc, v[i][7] * sc)};
  }
  WAIT_L_BAR();
  STAMP();
  const int ks32 = b >> 2, g = b & 3;
#pragma unroll
  for (int i = 0; i < 2; ++i) {
    const int m = tid + 320 * i;
    u16 e[8];
#pragma unroll
    for (int j = 0; j < 8; ++j) e[j] = tile[j * 648 + m];
    u32x4 w = {(unsigned)e[0] | ((unsigned)e[1] << 16), (unsigned)e[2] | ((unsigned)e[3] << 16),
               (unsigned)e[4] | ((unsigned)e[5] << 16), (unsigned)e[6] | ((unsigned)e[7] << 16)};
    *(u32x4*)(HN + ((size_t)(((m >> 4) * 64 + ks32) * 64 + g * 16 + (m & 15))) * 8) = w;
  }
  STAMP();
  STAMP();
  STAMP_END(3, DBGP(HN, WS_HN));
}

constexpr int K56_LDS = 163840;

DI float gelu_tanh(float x) {
  const float u = 0.7978845608028654f * (x + 0.044715f * x * x * x);
  const float e = __builtin_amdgcn_exp2f(u * 2.8853900817779268f);
  const float t = 1.0f - 2.0f * __builtin_amdgcn_rcpf(e + 1.0f);
  return 0.5f * x * (1.0f + t);
}

#define ACLOB56 "a0","a1","a2","a3","a4","a5","a6","a7","a8","a9","a10","a11","a12","a13","a14","a15","a16","a17","a18","a19","a20","a21","a22","a23","a24","a25","a26","a27","a28","a29","a30","a31","a32","a33","a34","a35","a36","a37","a38","a39","a40","a41","a42","a43","a44","a45","a46","a47","a48","a49","a50","a51","a52","a53","a54","a55","a56","a57","a58","a59","a60","a61","a62","a63","a64","a65","a66","a67","a68","a69","a70","a71","a72","a73","a74","a75","a76","a77","a78","a79","a80","a81","a82","a83","a84","a85","a86","a87","a88","a89","a90","a91"
template <int R> DI void m_aload(__amdgpu_buffer_rsrc_t rsrc, unsigned voff, unsigned soff) {
  asm volatile("buffer_load_dwordx4 a[%c3:%c4], %0, %1, %2 offen" ::"v"(voff), "s"(rsrc), "s"(soff), "i"(R), "i"(R + 3) : "memory");
}
template <int R, int OFF> DI void m_trread(unsigned addr) {
  asm volatile("ds_read_b64_tr_b16 a[%c1:%c2], %0 offset:%c3" ::"v"(addr), "i"(R), "i"(R + 1), "i"(OFF) : "memory");
}
template <int RA, int RB> DI void m_mfma(f32x4& d) {
  asm volatile("v_mfma_f32_16x16x32_bf16 %0, a[%c1:%c2], a[%c3:%c4], %0" : "+v"(d) : "i"(RA), "i"(RA + 3), "i"(RB), "i"(RB + 3));
}

template <int MODE>
__global__ __launch_bounds__(512, 2) void k56_mix(const u16* __restrict__ W, const u16* __restrict__ AF,
                                                  const u16* __restrict__ bias, const float* __restrict__ H,
                                                  u16* __restrict__ outp, const u16* __restrict__ wnext) {
  constexpr int LDW = MODE == 0 ? FF : SEQ;
  constexpr int NKS = MODE == 0 ? 64 : 54;
  constexpr int NK = MODE == 0 ? 8 : 7;
  constexpr int KROWS = MODE == 0 ? SEQ : FF;
  extern __shared__ __attribute__((aligned(16))) char smem[];
  LAS char* lds = (LAS char*)smem;
  const unsigned lds0 = (unsigned)(uintptr_t)lds;
  const int tid = threadIdx.x, lane = tid & 63;
  const int wid = RFL(tid >> 6);
  const int b = blockIdx.x, xcd = b & 7, idx = b >> 3;
  if constexpr (MODE == 0) CODE_LABEL(k5); else CODE_LABEL(k6);
  int wt, dt;
  if (MODE == 0) {
    wt = (xcd >> 1) * 7 + (idx >> 2); dt = (xcd & 1) * 4 + (idx & 3);
    if (idx >= 28 || wt >= 27) {
      if (idx >= 28) {
        const __amdgpu_buffer_rsrc_t r2 = mk_rsrc(wnext, SEQ * FF * 2);
#pragma unroll
        for (int i = 0; i < 7; ++i) {
          const int line = (((idx - 28) * 8 + wid) * 7 + i) * 64 + lane;
          const unsigned off = line < 13824 ? (unsigned)((line >> 3) * 4096 + (xcd >> 1) * 1024 + (line & 7) * 128) : 0x7ff00000u;
          warm8k(r2, off, 0u, lds0 + wid * 256);
        }
      }
      return;
    }
  } else {
    wt = (xcd >> 1) * 8 + (idx >> 2); dt = (xcd & 1) * 4 + (idx & 3);
  }
  const __amdgpu_buffer_rsrc_t rW = mk_rsrc(W, KROWS * LDW * 2);
  const __amdgpu_buffer_rsrc_t rA = mk_rsrc(AF, 40 * NKS * 1024);
  const unsigned vlin = lane * 16;
  const int prow = (lane >> 2) & 7;
  const unsigned voffW0 = (unsigned)(prow * LDW * 2 + (lane >> 5) * 64 +
                                     (((lane & 3) ^ (MODE == 1 ? (((prow >> 2) & 1) << 1) : 0)) << 4));
  const unsigned voffW1 = MODE == 1 ? voffW0 : (voffW0 ^ 32u);
  const unsigned myl = lds0 + wid * 20480;
  const int g = lane >> 4, q4 = (lane & 15) >> 2, p4 = lane & 3;
  unsigned wb[2][2];
#pragma unroll
  for (int t = 0; t < 2; ++t)
#pragma unroll
    for (int hf = 0; hf < 2; ++hf) {
      const int kb = MODE == 0 ? g : 2 * t + (g >> 1);
      const int row = MODE == 0 ? 4 * t + q4 : 4 * (g & 1) + q4;
      const int sw = MODE == 0 ? (kb & 1) : ((row >> 2) & 1);
      wb[t][hf] = myl + kb * 1024 + row * 64 + ((hf ^ sw) * 32) + p4 * 8;
    }
  f32x4 acc[4][5];
#pragma unroll
  for (int i = 0; i < 4; ++i)
#pragma unroll
    for (int d = 0; d < 5; ++d) acc[i][d] = (f32x4){0.f, 0.f, 0.f, 0.f};
  STAMP_DECL();
  STAMP();
  u32x2 eb0[2], eb1[2];
  f32x4 eh[3]; u16 ebs[3];
  if (MODE == 0) {
#pragma unroll
    for (int i = 0; i < 2; ++i) {
      const int u = wid + 8 * i < 10 ? wid + 8 * i : wid;
      const int f0 = wt * 64 + (u / 5) * 32 + 4 * g;
      eb0[i] = *(const u32x2*)(bias + f0); eb1[i] = *(const u32x2*)(bias + f0 + 16);
    }
  } else {
#pragma unroll
    for (int i = 0; i < 3; ++i) {
      const int u = wid + 8 * i < 20 ? wid + 8 * i : wid;
      const int s = wt * 64 + (u / 5) * 16 + (lane & 15), d0 = dt * 80 + (u % 5) * 16 + 4 * g;
      ebs[i] = bias[s];
      eh[i] = *(const f32x4*)(H + (size_t)s * DM + d0);
    }
  }
  SBAR();
#define KSTEP(IT) (wid + 8 * (IT))
#define WDMA(IT, KB) dma1(rW, ((KB) & 1) ? voffW1 : voffW0, (unsigned)((KSTEP(IT) * 32 + (KB) * 8) * LDW * 2 + wt * 128), myl + ((IT) % 5) * 4096 + (KB) * 1024)
#define ALOAD(IT, D) m_aload<((IT) % 3) * 20 + 4 * (D)>(rA, vlin, (unsigned)(((dt * 5 + (D)) * NKS + KSTEP(IT)) * 1024))
#define WDMA4(IT) do { WDMA(IT, 0); WDMA(IT, 1); WDMA(IT, 2); WDMA(IT, 3); } while (0)
#define ALOAD5(IT) do { ALOAD(IT, 0); ALOAD(IT, 1); ALOAD(IT, 2); ALOAD(IT, 3); ALOAD(IT, 4); } while (0)
#define WREAD1(IT, I) do { m_trread<60 + 16 * ((IT) & 1) + 4 * (I), ((IT) % 5) * 4096 + ((I) >> 1) * 512>(wb[0][(I) & 1]); \
                           m_trread<60 + 16 * ((IT) & 1) + 4 * (I) + 2, ((IT) % 5) * 4096 + ((I) >> 1) * 512>(wb[1][(I) & 1]); } while (0)
#define WREAD(IT) do { WREAD1(IT, 0); WREAD1(IT, 1); WREAD1(IT, 2); WREAD1(IT, 3); } while (0)
#define MM(IT, I, D) do { if constexpr (MODE == 0) m_mfma<60 + 16 * ((IT) & 1) + 4 * (I), ((IT) % 3) * 20 + 4 * (D)>(acc[I][D]); \
                          else m_mfma<((IT) % 3) * 20 + 4 * (D), 60 + 16 * ((IT) & 1) + 4 * (I)>(acc[I][D]); } while (0)
#define MMROW(IT, I) do { MM(IT, I, 0); MM(IT, I, 1); MM(IT, I, 2); MM(IT, I, 3); MM(IT, I, 4); } while (0)
#define NV(IT) (((IT) + 4 < NK ? 4 : 0) + ((IT) + 2 < NK ? 5 : 0))
#define ALLOW(IT) ((IT) == 0 ? (5 + (NK > 2 ? 4 : 0) + (NK > 3 ? 4 : 0)) : (IT) == 1 ? ((NK > 3 ? 4 : 0) + NV(0)) : NV((IT) - 1))
#define STEP(IT) do { if constexpr ((IT) < NK) { \
    asm volatile("s_waitcnt vmcnt(%c0) lgkmcnt(0)" ::"i"(ALLOW(IT)) : "memory", ACLOB56); \
    if constexpr ((IT) + 1 < NK) WREAD((IT) + 1); \
    SBAR(); \
    MMROW(IT, 0); if constexpr ((IT) + 4 < NK) { WDMA((IT) + 4, 0); WDMA((IT) + 4, 1); } SBAR(); \
    MMROW(IT, 1); if constexpr ((IT) + 4 < NK) { WDMA((IT) + 4, 2); WDMA((IT) + 4, 3); } SBAR(); \
    MMROW(IT, 2); if constexpr ((IT) + 2 < NK) { ALOAD((IT) + 2, 0); ALOAD((IT) + 2, 1); ALOAD((IT) + 2, 2); } SBAR(); \
    MMROW(IT, 3); if constexpr ((IT) + 2 < NK) { ALOAD((IT) + 2, 3); ALOAD((IT) + 2, 4); } SBAR(); \
    } } while (0)
  asm volatile("" ::: "memory", ACLOB56);
  WDMA4(0); ALOAD5(0); asm volatile("s_barrier" ::: "memory");
  WDMA4(1); ALOAD5(1); asm volatile("s_barrier" ::: "memory");
  WDMA4(2); WDMA4(3);
  asm volatile("s_waitcnt vmcnt(22)" ::: "memory");
  WREAD(0);
  STAMP();
  STEP(0); STEP(1); STEP(2); STEP(3); STEP(4); STEP(5); STEP(6); STEP(7);
  asm volatile("s_nop 15" ::: "memory");
  STAMP();
#undef KSTEP
#undef WDMA
#undef ALOAD
#undef WDMA4
#undef ALOAD5
#undef WREAD1
#undef WREAD
#undef MM
#undef MMROW
#undef NV
#undef ALLOW
#undef STEP
  WAIT_L0();
  STAMP();
  {
    LAS char* dst = lds + wid * 20480 + lane * 16;
#pragma unroll
    for (int i = 0; i < 4; ++i)
#pragma unroll
      for (int d = 0; d < 5; ++d) *(LAS f32x4*)(dst + (i * 5 + d) * 1024) = acc[i][d];
  }
  WAIT_L_BAR();
  if (MODE == 0) {
#pragma unroll
    for (int i = 0; i < 2; ++i) {
      const int u = wid + 8 * i;
      if (u < 10) {
        const int a = u / 5, d = u % 5;
        f32x4 t0 = {0.f, 0.f, 0.f, 0.f}, t1 = {0.f, 0.f, 0.f, 0.f};
#pragma unroll
        for (int w = 0; w < 8; ++w) {
          t0 += *(LAS const f32x4*)(lds + w * 20480 + ((2 * a) * 5 + d) * 1024 + lane * 16);
          t1 += *(LAS const f32x4*)(lds + w * 20480 + ((2 * a + 1) * 5 + d) * 1024 + lane * 16);
        }
        const u32x2 b0 = eb0[i], b1 = eb1[i];
        t0 += (f32x4){bflo(b0[0]), bfhi(b0[0]), bflo(b0[1]), bfhi(b0[1])};
        t1 += (f32x4){bflo(b1[0]), bfhi(b1[0]), bflo(b1[1]), bfhi(b1[1])};
        u32x4 w = {cvtpk(gelu_tanh(t0[0]), gelu_tanh(t0[1])), cvtpk(gelu_tanh(t0[2]), gelu_tanh(t0[3])),
                   cvtpk(gelu_tanh(t1[0]), gelu_tanh(t1[1])), cvtpk(gelu_tanh(t1[2]), gelu_tanh(t1[3]))};
        *(u32x4*)(outp + ((size_t)(((dt * 5 + d) * 54 + wt * 2 + a) * 64 + lane)) * 8) = w;
      }
    }
  } else {
#pragma unroll
    for (int i = 0; i < 3; ++i) {
      const int u = wid + 8 * i;
      if (u < 20) {
        f32x4 t0 = {0.f, 0.f, 0.f, 0.f};
#pragma unroll
        for (int w = 0; w < 8; ++w) t0 += *(LAS const f32x4*)(lds + w * 20480 + u * 1024 + lane * 16);
        const int s = wt * 64 + (u / 5) * 16 + (lane & 15);
        const int d0 = dt * 80 + (u % 5) * 16 + 4 * g;
        const float bb = bf2f(ebs[i]);
        const f32x4 hv = eh[i];
        u32x2 w = {cvtpk(t0[0] + bb + hv[0], t0[1] + bb + hv[1]), cvtpk(t0[2] + bb + hv[2], t0[3] + bb + hv[3])};
        *(u32x2*)(outp + (size_t)s * DM + d0) = w;
      }
    }
  }
  if constexpr (MODE == 1) CODE_LABEL(end);
  STAMP();
  STAMP_END(4 + MODE, DBGP(AF, MODE == 0 ? WS_HN : WS_UF));
}

}

extern "C" void kernel_launch(void* const* d_in, const int* in_sizes, int n_in, void* d_out, int out_size, void* d_ws,
                              size_t ws_size, hipStream_t stream) {
  (void)in_sizes; (void)n_in; (void)out_size; (void)ws_size;
  const u16* x = (const u16*)d_in[0];
  const u16* ckv = (const u16*)d_in[1];
  const u16* wq = (const u16*)d_in[2];
  const u16* wuk = (const u16*)d_in[3];
  const u16* wuv = (const u16*)d_in[4];
  const u16* wo = (const u16*)d_in[5];
  const u16* w1 = (const u16*)d_in[7];
  const u16* b1 = (const u16*)d_in[8];
  const u16* w2 = (const u16*)d_in[9];
  const u16* b2 = (const u16*)d_in[10];
  char* ws = (char*)d_ws;
  u16* XN = (u16*)(ws + WS_XN);
  u16* KF = (u16*)(ws + WS_KF);
  u16* VF = (u16*)(ws + WS_VF);
  u16* CF = (u16*)(ws + WS_CF);
  u16* PT = (u16*)(ws + WS_PT);
  float* H = (float*)(ws + WS_H);
  u16* HN = (u16*)(ws + WS_HN);
  u16* UF = (u16*)(ws + WS_UF);
  (void)hipFuncSetAttribute((const void*)k1_kvproj, hipFuncAttributeMaxDynamicSharedMemorySize, K1_LDS);
  (void)hipFuncSetAttribute((const void*)k2_attn, hipFuncAttributeMaxDynamicSharedMemorySize, K2_LDS);
  (void)hipFuncSetAttribute((const void*)k3_oproj, hipFuncAttributeMaxDynamicSharedMemorySize, K3_LDS);
  (void)hipFuncSetAttribute((const void*)k56_mix<0>, hipFuncAttributeMaxDynamicSharedMemorySize, K56_LDS);
  (void)hipFuncSetAttribute((const void*)k56_mix<1>, hipFuncAttributeMaxDynamicSharedMemorySize, K56_LDS);
  k1_kvproj<<<256, 512, K1_LDS, stream>>>(x, ckv, wuk, wuv, KF, VF, XN, wq);
  k2_attn<<<256, 512, K2_LDS, stream>>>(XN, wq, KF, VF, CF, (float*)(ws + WS_LAM), wo);
  k3_oproj<<<256, 512, K3_LDS, stream>>>(CF, wo, PT);
  k4_ln<<<256, 320, 0, stream>>>(x, PT, H, HN, w1);
  k56_mix<0><<<256, 512, K56_LDS, stream>>>(w1, HN, b1, nullptr, UF, w2);
  k56_mix<1><<<256, 512, K56_LDS, stream>>>(w2, UF, b2, H, (u16*)d_out, nullptr);
}
```

```cpp
#include <hip/hip_runtime.h>
#include <stdint.h>
#include <utility>

namespace {

typedef unsigned short u16;
typedef __bf16 bf16x8 __attribute__((ext_vector_type(8)));
typedef __bf16 bf16x2 __attribute__((ext_vector_type(2)));
typedef short s16x4 __attribute__((ext_vector_type(4)));
typedef short s16x8 __attribute__((ext_vector_type(8)));
typedef float f32x2 __attribute__((ext_vector_type(2)));
typedef float f32x4 __attribute__((ext_vector_type(4)));
typedef float f32x16 __attribute__((ext_vector_type(16)));
typedef unsigned u32x2 __attribute__((ext_vector_type(2)));
typedef unsigned u32x4 __attribute__((ext_vector_type(4)));
#define LAS __attribute__((address_space(3)))
#define DI __device__ __forceinline__
#define SBAR() __builtin_amdgcn_sched_barrier(0)
#define RFL(x) __builtin_amdgcn_readfirstlane(x)

constexpr int SEQ = 2048, DM = 640, DC = 512, NH = 32, HD = 128, FF = 1728;
constexpr float LN_EPS = 1e-5f;
constexpr float QSCALE = 0.08838834764831845f * 1.4426950408889634f;
constexpr float P_LIMIT = 1073741824.0f;
constexpr float NEG_BIG = -1e30f;

constexpr size_t WS_XN = 0;
constexpr size_t WS_KF = WS_XN + (size_t)SEQ * DM * 2;
constexpr size_t WS_VF = WS_KF + (size_t)NH * SEQ * HD * 2;
constexpr size_t WS_CF = WS_VF + (size_t)NH * SEQ * HD * 2;
constexpr size_t WS_PT = WS_CF + (size_t)NH * SEQ * HD * 2;
constexpr size_t WS_H = WS_PT + (size_t)4 * SEQ * DM * 2;
constexpr size_t WS_HN = WS_H + (size_t)SEQ * DM * 4;
constexpr size_t WS_UF = WS_HN + (size_t)SEQ * DM * 2;
constexpr size_t WS_LAM = WS_UF + (size_t)DM * FF * 2;
constexpr size_t WS_END = WS_LAM + 256 * 256 * 4;
static_assert(WS_END <= ((size_t)256 << 20), "workspace");

DI __amdgpu_buffer_rsrc_t mk_rsrc(const void* p, unsigned bytes) {
  return __builtin_amdgcn_make_buffer_rsrc((void*)p, 0, bytes, 0x00020000);
}
DI void dma1(__amdgpu_buffer_rsrc_t rsrc, unsigned voff, unsigned soff, unsigned ldsaddr) {
  asm volatile("s_mov_b32 m0, %0\n\ts_nop 0\n\tbuffer_load_dwordx4 %1, %2, %3 offen lds"
               ::"s"(ldsaddr), "v"(voff), "s"(rsrc), "s"(soff) : "memory", "m0");
}
DI void warm8k(__amdgpu_buffer_rsrc_t rsrc, unsigned voff128, unsigned soff, unsigned ldsaddr) {
  asm volatile("s_mov_b32 m0, %0\n\ts_nop 0\n\tbuffer_load_dword %1, %2, %3 offen lds"
               ::"s"(ldsaddr), "v"(voff128), "s"(rsrc), "s"(soff) : "memory", "m0");
}
#define WAIT_VL_BAR(N) asm volatile("s_waitcnt vmcnt(" #N ") lgkmcnt(0)\n\ts_barrier" ::: "memory")
#define WAIT_L_BAR() asm volatile("s_waitcnt lgkmcnt(0)\n\ts_barrier" ::: "memory")
#define WAIT_L0() asm volatile("s_waitcnt lgkmcnt(0)" ::: "memory")

DI bf16x8 lds_b128(LAS const char* p) { return *(LAS const bf16x8*)p; }
DI bf16x8 lds_tr2(LAS const char* p0, LAS const char* p1) {
  s16x4 lo = __builtin_amdgcn_ds_read_tr16_b64_v4i16((LAS s16x4*)p0);
  s16x4 hi = __builtin_amdgcn_ds_read_tr16_b64_v4i16((LAS s16x4*)p1);
  s16x8 w = __builtin_shufflevector(lo, hi, 0, 1, 2, 3, 4, 5, 6, 7);
  return __builtin_bit_cast(bf16x8, w);
}
DI unsigned cvtpk(float lo, float hi) {
  f32x2 v = {lo, hi};
  bf16x2 b = __builtin_convertvector(v, bf16x2);
  return __builtin_bit_cast(unsigned, b);
}
DI u32x4 pack8(const f32x16& a, int o) {
  u32x4 w = {cvtpk(a[o], a[o + 1]), cvtpk(a[o + 2], a[o + 3]), cvtpk(a[o + 4], a[o + 5]), cvtpk(a[o + 6], a[o + 7])};
  return w;
}
DI float bf2f(unsigned short b) { return __uint_as_float(((unsigned)b) << 16); }
DI float bflo(unsigned w) { return __uint_as_float(w << 16); }
DI float bfhi(unsigned w) { return __uint_as_float(w & 0xffff0000u); }
#define MFMA32(a, b, c) __builtin_amdgcn_mfma_f32_32x32x16_bf16((a), (b), (c), 0, 0, 0)
#define MFMA16(a, b, c) __builtin_amdgcn_mfma_f32_16x16x32_bf16((a), (b), (c), 0, 0, 0)
DI float swap_max(float v) {
  auto rr = __builtin_amdgcn_permlane32_swap(__float_as_uint(v), __float_as_uint(v), false, false);
  return fmaxf(__uint_as_float(rr[0]), __uint_as_float(rr[1]));
}
DI float swap_add(float v) {
  auto rr = __builtin_amdgcn_permlane32_swap(__float_as_uint(v), __float_as_uint(v), false, false);
  return __uint_as_float(rr[0]) + __uint_as_float(rr[1]);
}

constexpr int K1_LDS = 4 * 32768;

template <bool ISV>
DI void k1_body(LAS char* lds, unsigned lds0, int wid, int lane, int head, int tb, const u16* __restrict__ ckv,
                const u16* __restrict__ wsrc, const u16* __restrict__ x, u16* __restrict__ KF, u16* __restrict__ VF,
                u16* __restrict__ XN, const u16* __restrict__ wq) {
  const int wm = wid >> 2, wn = wid & 3;
  const int r = lane & 31, h = lane >> 5;
  const int g1 = (lane >> 4) & 1, q = (lane & 15) >> 2, p = lane & 3;
  const __amdgpu_buffer_rsrc_t rA = mk_rsrc(ckv, SEQ * DC * 2);
  const __amdgpu_buffer_rsrc_t rWm = mk_rsrc(wsrc, DC * NH * HD * 2);
  const unsigned voffA = (lane >> 2) * 1024 + (((lane & 3) ^ ((lane >> 4) & 3)) << 4);
  const unsigned voffW = ((lane >> 2) & 7) * 8192 + (lane >> 5) * 64 + (lane & 3) * 16;
  auto piece = [&](int kt, int slot, int i) __attribute__((always_inline)) {
    const unsigned sb = lds0 + slot * 32768;
    if (i < 2) dma1(rA, voffA, (unsigned)((tb * 256 + (wid * 2 + i) * 16) * 1024 + kt * 64), sb + (wid * 2 + i) * 1024);
    else dma1(rWm, voffW, (unsigned)((kt * 32 + (wid >> 1) * 8) * 8192 + head * 256 + (i - 2) * 128),
              sb + 16384 + ((wid >> 1) * 4 + (wid & 1) * 2 + (i - 2)) * 1024);
  };
  unsigned abase[2];
#pragma unroll
  for (int s = 0; s < 2; ++s) abase[s] = wm * 8192 + r * 64 + (((2 * s + h) ^ ((r >> 2) & 3)) << 4);
  const unsigned wbase = 16384 + h * 4096 + wn * 1024 + q * 64 + g1 * 32 + p * 8;

  f32x16 acc[4][2];
#pragma unroll
  for (int i = 0; i < 4; ++i)
#pragma unroll
    for (int n = 0; n < 2; ++n) acc[i][n] = f32x16{};
  auto rd = [&](bf16x8 (&a)[4], bf16x8 (&w)[2], LAS const char* sb, int s) {
#pragma unroll
    for (int n = 0; n < 2; ++n) w[n] = lds_tr2(sb + wbase + s * 8192 + n * 512, sb + wbase + s * 8192 + n * 512 + 256);
#pragma unroll
    for (int i = 0; i < 4; ++i) a[i] = lds_b128(sb + abase[s] + i * 2048);
  };
  auto mm2 = [&](const bf16x8 (&a)[4], const bf16x8 (&w)[2], int i) {
#pragma unroll
    for (int n = 0; n < 2; ++n) {
      if (ISV) acc[i][n] = MFMA32(a[i], w[n], acc[i][n]);
      else acc[i][n] = MFMA32(w[n], a[i], acc[i][n]);
    }
  };
  bf16x8 aA[4], wA[2], aB[4], wB[2];
#pragma unroll
  for (int kt = 0; kt < 3; ++kt)
#pragma unroll
    for (int i = 0; i < 4; ++i) piece(kt, kt, i);
  auto stage = [&](const int kt, const int slot, const bool more, const bool first) __attribute__((always_inline)) {
    LAS const char* sb = lds + slot * 32768;
    rd(aA, wA, sb, 0);
    SBAR();
    if (!first) {
      mm2(aB, wB, 0); mm2(aB, wB, 1);
      if (more) piece(kt + 3, (slot + 3) & 3, 0);
      SBAR();
      mm2(aB, wB, 2); mm2(aB, wB, 3);
      if (more) piece(kt + 3, (slot + 3) & 3, 1);
    } else {
      piece(3, 3, 0); piece(3, 3, 1);
    }
    SBAR();
    rd(aB, wB, sb, 1);
    SBAR();
    mm2(aA, wA, 0); mm2(aA, wA, 1);
    if (more) piece(kt + 3, (slot + 3) & 3, 2);
    SBAR();
    mm2(aA, wA, 2); mm2(aA, wA, 3);
    if (more) piece(kt + 3, (slot + 3) & 3, 3);
    SBAR();
  };
  WAIT_VL_BAR(8); stage(0, 0, true, true);
  for (int k4 = 1; k4 < 13; k4 += 4) {
    WAIT_VL_BAR(8); stage(k4, 1, true, false);
    WAIT_VL_BAR(8); stage(k4 + 1, 2, true, false);
    WAIT_VL_BAR(8); stage(k4 + 2, 3, true, false);
    WAIT_VL_BAR(8); stage(k4 + 3, 0, true, false);
  }
  WAIT_VL_BAR(8); stage(13, 1, false, false);
  WAIT_VL_BAR(4); stage(14, 2, false, false);
  WAIT_VL_BAR(0); stage(15, 3, false, false);
  mm2(aB, wB, 0); mm2(aB, wB, 1); mm2(aB, wB, 2); mm2(aB, wB, 3);
  {
    const __amdgpu_buffer_rsrc_t rQ = mk_rsrc(wq, DM * NH * HD * 2);
    const int u = (blockIdx.x >> 3) * 8 + wid;
    if (u < 80) warm8k(rQ, (unsigned)((lane >> 3) * 8192 + (lane & 7) * 128), (unsigned)(u * 8 * 8192 + (blockIdx.x & 7) * 1024), lds0);
  }
  const int token = blockIdx.x * 8 + wid;
  const bool has1 = lane < 16;
  const u32x4 v0 = *(const u32x4*)(x + (size_t)token * DM + lane * 8);
  u32x4 v1 = {0u, 0u, 0u, 0u};
  if (has1) v1 = *(const u32x4*)(x + (size_t)token * DM + 512 + lane * 8);
  SBAR();
#pragma unroll
  for (int i = 0; i < 4; ++i) {
    const int kb32 = tb * 8 + wm * 4 + i;
#pragma unroll
    for (int n = 0; n < 2; ++n) {
#pragma unroll
      for (int sg = 0; sg < 2; ++sg) {
        const u32x4 w = pack8(acc[i][n], 8 * sg);
        if (ISV) {
          const int db = 2 * (wn - 2) + n;
          *(u32x4*)(VF + ((size_t)((((head * 64 + kb32) * 4 + db) * 2 + sg) * 64 + lane)) * 8) = w;
        } else {
          const int db = 2 * wn + n;
          *(u32x4*)(KF + ((size_t)(((head * 64 + kb32) * 8 + 2 * db + sg) * 64 + lane)) * 8) = w;
        }
      }
    }
  }
  {
    float f0[8], f1[8];
#pragma unroll
    for (int j = 0; j < 4; ++j) {
      f0[2 * j] = bflo(v0[j]); f0[2 * j + 1] = bfhi(v0[j]);
      f1[2 * j] = bflo(v1[j]); f1[2 * j + 1] = bfhi(v1[j]);
    }
    float s = 0.f;
#pragma unroll
    for (int j = 0; j < 8; ++j) s += f0[j] + f1[j];
#pragma unroll
    for (int o = 1; o < 64; o <<= 1) s += __shfl_xor(s, o);
    const float mean = s * (1.0f / DM);
    float sq = 0.f;
#pragma unroll
    for (int j = 0; j < 8; ++j) {
      const float d0 = f0[j] - mean; sq += d0 * d0;
      const float d1 = has1 ? f1[j] - mean : 0.f; sq += d1 * d1;
    }
#pragma unroll
    for (int o = 1; o < 64; o <<= 1) sq += __shfl_xor(sq, o);
    const float rstd = 1.0f / sqrtf(sq * (1.0f / DM) + LN_EPS);
    const int tb32 = token >> 5, rr = token & 31;
    u32x4 o0, o1;
#pragma unroll
    for (int j = 0; j < 4; ++j) {
      o0[j] = cvtpk((f0[2 * j] - mean) * rstd, (f0[2 * j + 1] - mean) * rstd);
      o1[j] = cvtpk((f1[2 * j] - mean) * rstd, (f1[2 * j + 1] - mean) * rstd);
    }
    *(u32x4*)(XN + ((size_t)((tb32 * 40 + (lane >> 1)) * 64 + (lane & 1) * 32 + rr)) * 8) = o0;
    if (has1) *(u32x4*)(XN + ((size_t)((tb32 * 40 + 32 + (lane >> 1)) * 64 + (lane & 1) * 32 + rr)) * 8) = o1;
  }
}

__global__ __launch_bounds__(512, 2) void k1_kvproj(const u16* __restrict__ x, const u16* __restrict__ ckv,
                                                    const u16* __restrict__ wuk, const u16* __restrict__ wuv,
                                                    u16* __restrict__ KF, u16* __restrict__ VF, u16* __restrict__ XN,
                                                    const u16* __restrict__ wq) {
  extern __shared__ __attribute__((aligned(16))) char smem[];
  LAS char* lds = (LAS char*)smem;
  const unsigned lds0 = (unsigned)(uintptr_t)lds;
  const int tid = threadIdx.x, lane = tid & 63;
  const int wid = RFL(tid >> 6);
  const int b = blockIdx.x, xcd = b & 7, idx = b >> 3;
  const int head = xcd * 4 + (idx & 3), tb = idx >> 2;
  const u16* wsrc = (wid & 1) ? wuv : wuk;
  if ((wid & 3) >= 2) k1_body<true>(lds, lds0, wid, lane, head, tb, ckv, wsrc, x, KF, VF, XN, wq);
  else k1_body<false>(lds, lds0, wid, lane, head, tb, ckv, wsrc, x, KF, VF, XN, wq);
}

constexpr int K2_LDS = 163840;
constexpr unsigned L_KB1 = 0, L_VB0 = 16384, L_VA1 = 32768, L_KA0 = 49152, L_KB0 = 65536, L_KA1 = 81920, L_VA0 = 98304,
                   L_VB1 = 114688, L_STASH = 131072;
constexpr int NSTEP = 17;
#define SPRIO 2
#define MPRIO 0
#define TRAILPRIO SPRIO

template <int OFF>
DI void dma1o(__amdgpu_buffer_rsrc_t rsrc, unsigned voff, unsigned soff, unsigned ldsaddr) {
  asm volatile("s_mov_b32 m0, %0\n\ts_nop 0\n\tbuffer_load_dwordx4 %1, %2, %3 offen offset:%4 lds"
               ::"s"(ldsaddr), "v"(voff), "s"(rsrc), "s"(soff), "i"(OFF) : "memory", "m0");
}
DI float fmul_s(float a, float b) { float r; asm("v_mul_f32 %0, %1, %2" : "=v"(r) : "v"(a), "v"(b)); return r; }
#define ACLOB "a8","a9","a10","a11","a12","a13","a14","a15","a16","a17","a18","a19","a20","a21","a22","a23","a24","a25","a26","a27","a28","a29","a30","a31","a32","a33","a34","a35","a36","a37","a38","a39","a40","a41","a42","a43","a44","a45","a46","a47","a48","a49","a50","a51","a52","a53","a54","a55","a56","a57","a58","a59","a60","a61","a62","a63","a64","a65","a66","a67","a68","a69","a70","a71","a72","a73","a74","a75","a76","a77","a78","a79","a80","a81","a82","a83","a84","a85","a86","a87","a88","a89","a90","a91","a92","a93","a94","a95","a96","a97","a98","a99","a100","a101","a102","a103","a104","a105","a106","a107","a108","a109","a110","a111","a112","a113","a114","a115","a116","a117","a118","a119","a120","a121","a122","a123","a124","a125","a126","a127"
constexpr int A_O = 8, A_Q = 72, A_R = 104, NRING = 6;
template <int SLOT, int OFF> DI void ring_read(unsigned addr) {
  asm volatile("ds_read_b128 a[%c1:%c2], %0 offset:%c3" ::"v"(addr), "i"(A_R + 4 * SLOT), "i"(A_R + 4 * SLOT + 3), "i"(OFF) : "memory");
}
template <int S, int OFF> DI void q_read(unsigned addr) {
  asm volatile("ds_read_b128 a[%c1:%c2], %0 offset:%c3" ::"v"(addr), "i"(A_Q + 4 * S), "i"(A_Q + 4 * S + 3), "i"(OFF) : "memory");
}
constexpr int V_NEGM = 64, V_S0 = 80, V_S1 = 96, V_PF = 112;
template <int W, int SLOT, int S, int DST> DI void qk_head() {
  asm volatile("s_waitcnt lgkmcnt(%c0)\n\tv_mfma_f32_32x32x16_bf16 v[%c1:%c2], a[%c3:%c4], a[%c5:%c6], v[%c7:%c8]"
               ::"i"(W), "i"(DST), "i"(DST + 15), "i"(A_R + 4 * SLOT), "i"(A_R + 4 * SLOT + 3), "i"(A_Q + 4 * S), "i"(A_Q + 4 * S + 3),
                 "i"(V_NEGM), "i"(V_NEGM + 15));
}
template <int W, int SLOT, int S, int DST> DI void qk_mfma() {
  asm volatile("s_waitcnt lgkmcnt(%c0)\n\tv_mfma_f32_32x32x16_bf16 v[%c1:%c2], a[%c3:%c4], a[%c5:%c6], v[%c1:%c2]"
               ::"i"(W), "i"(DST), "i"(DST + 15), "i"(A_R + 4 * SLOT), "i"(A_R + 4 * SLOT + 3), "i"(A_Q + 4 * S), "i"(A_Q + 4 * S + 3));
}
template <int W, int SLOT, int D, int P> DI void pv_mfma() {
  asm volatile("s_waitcnt lgkmcnt(%c0)\n\tv_mfma_f32_32x32x16_bf16 a[%c1:%c2], a[%c3:%c4], v[%c5:%c6], a[%c1:%c2]"
               ::"i"(W), "i"(A_O + 16 * D), "i"(A_O + 16 * D + 15), "i"(A_R + 4 * SLOT), "i"(A_R + 4 * SLOT + 3), "i"(V_PF + 4 * P),
                 "i"(V_PF + 4 * P + 3));
}
template <int W, int SLOT, int D, int P> DI void pv_zero() {
  asm volatile("s_waitcnt lgkmcnt(%c0)\n\tv_mfma_f32_32x32x16_bf16 a[%c1:%c2], a[%c3:%c4], v[%c5:%c6], 0"
               ::"i"(W), "i"(A_O + 16 * D), "i"(A_O + 16 * D + 15), "i"(A_R + 4 * SLOT), "i"(A_R + 4 * SLOT + 3), "i"(V_PF + 4 * P),
                 "i"(V_PF + 4 * P + 3));
}
template <int S, int P> DI void exps8(float& ps0, float& ps1, float& ps2, float& ps3) {
  float t0, t1, t2, t3, t4, t5, t6, t7;
  asm volatile("v_exp_f32 %4, v[%c12]\n\tv_exp_f32 %5, v[%c12+1]\n\tv_exp_f32 %6, v[%c12+2]\n\tv_exp_f32 %7, v[%c12+3]\n\t"
               "v_exp_f32 %8, v[%c12+4]\n\tv_exp_f32 %9, v[%c12+5]\n\tv_exp_f32 %10, v[%c12+6]\n\tv_exp_f32 %11, v[%c12+7]\n\t"
               "v_add_f32 %0, %0, %4\n\tv_add_f32 %1, %1, %5\n\tv_cvt_pk_bf16_f32 v[%c13], %4, %5\n\t"
               "v_add_f32 %2, %2, %6\n\tv_add_f32 %3, %3, %7\n\tv_cvt_pk_bf16_f32 v[%c13+1], %6, %7\n\t"
               "v_add_f32 %0, %0, %8\n\tv_add_f32 %1, %1, %9\n\tv_cvt_pk_bf16_f32 v[%c13+2], %8, %9\n\t"
               "v_add_f32 %2, %2, %10\n\tv_add_f32 %3, %3, %11\n\tv_cvt_pk_bf16_f32 v[%c13+3], %10, %11"
               : "+v"(ps0), "+v"(ps1), "+v"(ps2), "+v"(ps3), "=&v"(t0), "=&v"(t1), "=&v"(t2), "=&v"(t3), "=&v"(t4), "=&v"(t5),
                 "=&v"(t6), "=&v"(t7)
               : "i"(S), "i"(V_PF + 4 * P));
}
DI float exps32() {
  float ps0 = 0.f, ps1 = 0.f, ps2 = 0.f, ps3 = 0.f, r;
  exps8<V_S0, 0>(ps0, ps1, ps2, ps3); exps8<V_S0 + 8, 1>(ps0, ps1, ps2, ps3);
  exps8<V_S1, 2>(ps0, ps1, ps2, ps3); exps8<V_S1 + 8, 3>(ps0, ps1, ps2, ps3);
  asm volatile("v_add_f32 %1, %1, %2\n\tv_add_f32 %3, %3, %4\n\tv_add_f32 %0, %1, %3" : "=v"(r), "+v"(ps0), "+v"(ps1), "+v"(ps2), "+v"(ps3));
  return r;
}
DI float rowmax32() {
  float a, b;
  asm volatile("v_max3_f32 %0, v80, v81, v82\n\tv_max3_f32 %1, v83, v84, v85\n\tv_max3_f32 %0, %0, v86, v87\n\tv_max3_f32 %1, %1, v88, v89\n\t"
               "v_max3_f32 %0, %0, v90, v91\n\tv_max3_f32 %1, %1, v92, v93\n\tv_max3_f32 %0, %0, v94, v95\n\tv_max3_f32 %1, %1, v96, v97\n\t"
               "v_max3_f32 %0, %0, v98, v99\n\tv_max3_f32 %1, %1, v100, v101\n\tv_max3_f32 %0, %0, v102, v103\n\tv_max3_f32 %1, %1, v104, v105\n\t"
               "v_max3_f32 %0, %0, v106, v107\n\tv_max3_f32 %1, %1, v108, v109\n\tv_max3_f32 %0, %0, v110, v111\n\tv_max_f32 %0, %0, %1"
               : "=&v"(a), "=&v"(b));
  return a;
}
template <int R> DI void vreg_add(float t) { asm volatile("v_add_f32 v[%c1], v[%c1], %0" ::"v"(t), "i"(R)); }
template <int S, int... E> DI void mask_all_(float ninf, std::integer_sequence<int, E...>) { (vreg_add<S + E>(ninf), ...); }
template <int S, int... E> DI void mask_tri_(int dq, float ninf, std::integer_sequence<int, E...>) {
  (vreg_add<S + E>(((E & 3) + 8 * (E >> 2) > dq) ? ninf : 0.f), ...);
}
template <int I> DI float acc_rd() { float r; asm volatile("v_accvgpr_read_b32 %0, a[%c1]" : "=v"(r) : "i"(I)); return r; }
template <int I> DI void acc_wr(float v) { asm volatile("v_accvgpr_write_b32 a[%c1], %0" ::"v"(v), "i"(I)); }
template <int... E> DI void o_scale_(float f, std::integer_sequence<int, E...>) { (acc_wr<A_O + E>(fmul_s(acc_rd<A_O + E>(), f)), ...); }
template <int R> DI u32x4 acc_scale8(float f) {
  u32x4 o; float t0, t1, t2, t3, t4, t5, t6, t7;
  asm volatile("v_accvgpr_read_b32 %4, a[%c13]\n\tv_accvgpr_read_b32 %5, a[%c13+1]\n\tv_accvgpr_read_b32 %6, a[%c13+2]\n\t"
               "v_accvgpr_read_b32 %7, a[%c13+3]\n\tv_accvgpr_read_b32 %8, a[%c13+4]\n\tv_accvgpr_read_b32 %9, a[%c13+5]\n\t"
               "v_accvgpr_read_b32 %10, a[%c13+6]\n\tv_accvgpr_read_b32 %11, a[%c13+7]\n\t"
               "v_mul_f32 %4, %4, %12\n\tv_mul_f32 %5, %5, %12\n\tv_mul_f32 %6, %6, %12\n\tv_mul_f32 %7, %7, %12\n\t"
               "v_mul_f32 %8, %8, %12\n\tv_mul_f32 %9, %9, %12\n\tv_mul_f32 %10, %10, %12\n\tv_mul_f32 %11, %11, %12\n\t"
               "v_cvt_pk_bf16_f32 %0, %4, %5\n\tv_cvt_pk_bf16_f32 %1, %6, %7\n\tv_cvt_pk_bf16_f32 %2, %8, %9\n\tv_cvt_pk_bf16_f32 %3, %10, %11"
               : "=&v"(o[0]), "=&v"(o[1]), "=&v"(o[2]), "=&v"(o[3]), "=&v"(t0), "=&v"(t1), "=&v"(t2), "=&v"(t3), "=&v"(t4),
                 "=&v"(t5), "=&v"(t6), "=&v"(t7)
               : "v"(f), "i"(R));
  return o;
}
template <int R, int OFF> DI void acc_to_lds(unsigned addr) {
  asm volatile("ds_write_b128 %0, a[%c1:%c2] offset:%c3" ::"v"(addr), "i"(R), "i"(R + 3), "i"(OFF) : "memory");
}
template <int R> DI u32x4 acc_combine8f(const f32x4& w0, const f32x4& w1, float cw, float ca) {
  u32x4 o; float t0, t1, t2, t3, t4, t5, t6, t7;
  asm volatile("v_accvgpr_read_b32 %4, a[%c22]\n\tv_accvgpr_read_b32 %5, a[%c22+1]\n\tv_accvgpr_read_b32 %6, a[%c22+2]\n\t"
               "v_accvgpr_read_b32 %7, a[%c22+3]\n\tv_accvgpr_read_b32 %8, a[%c22+4]\n\tv_accvgpr_read_b32 %9, a[%c22+5]\n\t"
               "v_accvgpr_read_b32 %10, a[%c22+6]\n\tv_accvgpr_read_b32 %11, a[%c22+7]\n\t"
               "v_mul_f32 %4, %4, %21\n\tv_mul_f32 %5, %5, %21\n\tv_mul_f32 %6, %6, %21\n\tv_mul_f32 %7, %7, %21\n\t"
               "v_mul_f32 %8, %8, %21\n\tv_mul_f32 %9, %9, %21\n\tv_mul_f32 %10, %10, %21\n\tv_mul_f32 %11, %11, %21\n\t"
               "v_fma_f32 %4, %12, %20, %4\n\tv_fma_f32 %5, %13, %20, %5\n\tv_fma_f32 %6, %14, %20, %6\n\tv_fma_f32 %7, %15, %20, %7\n\t"
               "v_fma_f32 %8, %16, %20, %8\n\tv_fma_f32 %9, %17, %20, %9\n\tv_fma_f32 %10, %18, %20, %10\n\tv_fma_f32 %11, %19, %20, %11\n\t"
               "v_cvt_pk_bf16_f32 %0, %4, %5\n\tv_cvt_pk_bf16_f32 %1, %6, %7\n\tv_cvt_pk_bf16_f32 %2, %8, %9\n\tv_cvt_pk_bf16_f32 %3, %10, %11"
               : "=&v"(o[0]), "=&v"(o[1]), "=&v"(o[2]), "=&v"(o[3]), "=&v"(t0), "=&v"(t1), "=&v"(t2), "=&v"(t3), "=&v"(t4),
                 "=&v"(t5), "=&v"(t6), "=&v"(t7)
               : "v"(w0[0]), "v"(w0[1]), "v"(w0[2]), "v"(w0[3]), "v"(w1[0]), "v"(w1[1]), "v"(w1[2]), "v"(w1[3]), "v"(cw), "v"(ca),
                 "i"(R));
  return o;
}
DI void scores_add(float aone, float v) {
  asm volatile("s_nop 1\n\tv_mfma_f32_32x32x2_f32 v[80:95], %0, %1, v[80:95]\n\tv_mfma_f32_32x32x2_f32 v[96:111], %0, %1, v[96:111]" ::"v"(aone), "v"(v));
}
DI void negm_set(float aone, float v) { asm volatile("s_nop 1\n\tv_mfma_f32_32x32x2_f32 v[64:79], %0, %1, 0" ::"v"(aone), "v"(v)); }
#define ACC_FENCE() asm volatile("s_nop 15\n\ts_nop 3" ::: "memory")
#define BAR_A(WAITS) asm volatile("s_waitcnt " WAITS "\n\ts_barrier" ::: "memory", "v127", ACLOB)

template <int J> DI void frag_read(unsigned ka, unsigned va) {
  if constexpr (J < 16) ring_read<J % NRING, (J & 1) * 8192 + (J >> 1) * 1024>(ka);
  else { constexpr int i = J - 16; ring_read<J % NRING, (i >> 3) * 8192 + ((i & 3) * 2 + ((i >> 2) & 1)) * 1024>(va); }
}
#define PDX 4
constexpr int PD = PDX;

__global__ __launch_bounds__(512, 2) __attribute__((amdgpu_num_vgpr(64))) void k2_attn(const u16* __restrict__ XN, const u16* __restrict__ wq,
                                                  const u16* __restrict__ KF, const u16* __restrict__ VF,
                                                  u16* __restrict__ CF, float* __restrict__ LAM,
                                                  const u16* __restrict__ wo) {
  extern __shared__ __attribute__((aligned(16))) char smem[];
  LAS char* lds = (LAS char*)smem;
  const unsigned lds0 = (unsigned)(uintptr_t)lds;
  const int tid = threadIdx.x, lane = tid & 63;
  const int wid = RFL(tid >> 6);
  const int b = blockIdx.x, xcd = b & 7, idx = b >> 3;
  const int head = xcd * 4 + (idx >> 3), px = idx & 7;
  const int qbH = 15 - px, qbL = px;
  const int rr = wid & 3, c = wid >> 2;
  const int h = lane >> 5;
  const int g1 = (lane >> 4) & 1, q4 = (lane & 15) >> 2, p4 = lane & 3;
  const __amdgpu_buffer_rsrc_t rX = mk_rsrc(XN, SEQ * DM * 2);
  const __amdgpu_buffer_rsrc_t rW = mk_rsrc(wq, DM * NH * HD * 2);
  const __amdgpu_buffer_rsrc_t rK = mk_rsrc(KF, NH * SEQ * HD * 2);
  const __amdgpu_buffer_rsrc_t rV = mk_rsrc(VF, NH * SEQ * HD * 2);
  const unsigned vlin = lane * 16;
  const unsigned voffW = ((lane >> 2) & 7) * 8192 + (lane >> 5) * 64 + (lane & 3) * 16;
  const int p1 = 2 * (px + 1);
  auto kvoff = [&](int sg) -> unsigned {
    const int hidx = sg < p1 ? sg : 2 * sg - p1 + c;
    return (unsigned)((head * 64 + 2 * hidx) * 8192);
  };
  const int tbx = (wid < 4) ? (4 * qbH + wid) : (4 * qbL + wid - 4);
  auto q_load = [&](unsigned addr) {
    q_read<0, 0>(addr); q_read<1, 1024>(addr); q_read<2, 2048>(addr); q_read<3, 3072>(addr);
    q_read<4, 4096>(addr); q_read<5, 5120>(addr); q_read<6, 6144>(addr); q_read<7, 7168>(addr);
  };
  {
    auto q_piece = [&](int kt, int i) {
      const unsigned sb = lds0 + (unsigned)(kt % 3) * 49152;
      if (i < 2) dma1(rW, voffW, (unsigned)((kt * 64 + wid * 8) * 8192 + head * 256 + i * 128), sb + (wid * 2 + i) * 1024);
      else dma1(rX, vlin, (unsigned)((tbx * 40 + 4 * kt + (i - 2)) * 1024), sb + 16384 + wid * 4096 + (i - 2) * 1024);
    };
    auto kv_piece = [&](int i) {
      const unsigned part = wid * 2048 + i * 1024;
      dma1(rK, vlin, (unsigned)(head * 64 * 8192) + part, lds0 + L_KA0 + part);
    };
#pragma unroll
    for (int i = 0; i < 6; ++i) q_piece(0, i);
#pragma unroll
    for (int i = 0; i < 6; ++i) q_piece(1, i);
    f32x16 qa[2][2];
#pragma unroll
    for (int i = 0; i < 2; ++i)
#pragma unroll
      for (int n = 0; n < 2; ++n) qa[i][n] = f32x16{};
    const unsigned wbase = h * 2048 + c * 1024 + q4 * 64 + g1 * 32 + p4 * 8;
    const unsigned xbase = 16384 + rr * 4096 + lane * 16;
    bf16x8 wA[2], xA[2], wB[2], xB[2];
    auto rdq = [&](bf16x8 (&w)[2], bf16x8 (&x)[2], LAS const char* sb, int s) {
      w[0] = lds_tr2(sb + wbase + s * 4096, sb + wbase + s * 4096 + 256);
      x[0] = lds_b128(sb + xbase + s * 1024);
      w[1] = lds_tr2(sb + wbase + s * 4096 + 512, sb + wbase + s * 4096 + 768);
      x[1] = lds_b128(sb + xbase + 16384 + s * 1024);
    };
    auto mmq = [&](const bf16x8 (&w)[2], const bf16x8 (&x)[2]) {
      qa[0][0] = MFMA32(w[0], x[0], qa[0][0]);
      qa[1][0] = MFMA32(w[0], x[1], qa[1][0]);
      qa[0][1] = MFMA32(w[1], x[0], qa[0][1]);
      qa[1][1] = MFMA32(w[1], x[1], qa[1][1]);
    };
    for (int kt = 0; kt < 10; ++kt) {
      if (kt < 9) { WAIT_VL_BAR(6); } else { WAIT_VL_BAR(2); }
      LAS const char* sb = lds + (unsigned)(kt % 3) * 49152;
      auto piece = [&](int i) {
        if (kt < 8) q_piece(kt + 2, i);
        else if (kt == 8 && i < 2) kv_piece(i);
      };
      rdq(wA, xA, sb, 0);
      SBAR();
      if (kt > 0) mmq(wB, xB);
      piece(0); SBAR();
      rdq(wB, xB, sb, 1);
      SBAR();
      mmq(wA, xA);
      piece(1); piece(2); SBAR();
      rdq(wA, xA, sb, 2);
      SBAR();
      mmq(wB, xB);
      piece(3); piece(4); SBAR();
      rdq(wB, xB, sb, 3);
      SBAR();
      mmq(wA, xA);
      piece(5); SBAR();
    }
    mmq(wB, xB);
    WAIT_L_BAR();
#pragma unroll
    for (int i = 0; i < 2; ++i) {
      LAS char* dst = lds + (i == 0 ? L_STASH : 0u) + rr * 8192 + lane * 16;
#pragma unroll
      for (int n = 0; n < 2; ++n) {
        const f32x16 t = qa[i][n] * QSCALE;
#pragma unroll
        for (int sg = 0; sg < 2; ++sg) *(LAS u32x4*)(dst + (4 * c + 2 * n + sg) * 1024) = pack8(t, 8 * sg);
      }
    }
    WAIT_L_BAR();
    q_load(lds0 + (c ? 0u : L_STASH) + rr * 8192 + lane * 16);
    BAR_A("vmcnt(0) lgkmcnt(0)");
  }
  negm_set(0.f, 0.f);
  float mref = 0.f, lsum = 0.f, mfin = 0.f, lfin = 0.f;
  const unsigned kown0 = c ? L_KB0 : L_KA0, kown1 = c ? L_KB1 : L_KA1;
  const unsigned vown0 = c ? L_VB0 : L_VA0, vown1 = c ? L_VB1 : L_VA1;
  const unsigned lrd = lds0 + lane * 16;
  float* lamp = LAM + (size_t)b * 256 + rr * 64;
  f32x2 mlA = {0.f, 0.f};

  auto refix = [&](const bool first) __attribute__((always_inline)) {
    const float rm = swap_max(rowmax32());
    const float dl = first ? fmaxf(rm, NEG_BIG) : fmaxf(rm, 0.f);
    const float f = first ? 0.f : __builtin_amdgcn_exp2f(-dl);
    mref = first ? dl : mref + dl;
    lsum = first ? 0.f : lsum * f;
    int ln = lane;
    asm volatile("" : "+v"(ln));
    const float aone = ln < 32 ? 1.f : 0.f;
    scores_add(aone, -dl);
    negm_set(aone, -mref);
    ACC_FENCE();
    if (!first) { o_scale_(f, std::make_integer_sequence<int, 64>{}); ACC_FENCE(); }
  };
#define TRAIL() do { pv_mfma<0, 28 % NRING, 0, 3>(); pv_mfma<0, 29 % NRING, 1, 3>(); pv_mfma<0, 30 % NRING, 2, 3>(); pv_mfma<0, 31 % NRING, 3, 3>(); SBAR(); } while (0)
  auto splain = [&]() __attribute__((always_inline)) {
    __builtin_amdgcn_s_setprio(TRAILPRIO);
    TRAIL();
    if (TRAILPRIO != SPRIO) __builtin_amdgcn_s_setprio(SPRIO);
    float pst = exps32();
    if (__builtin_expect(__any(pst > P_LIMIT), 0)) { refix(false); pst = exps32(); }
    asm volatile("v_add_f32 %0, %0, %1" : "+v"(lsum) : "v"(pst));
    __builtin_amdgcn_s_setprio(MPRIO);
  };
  auto sgen = [&](const int tau) __attribute__((always_inline)) {
    __builtin_amdgcn_s_setprio(SPRIO);
    if (tau > 0) TRAIL();
    const bool lastL = c == 1 && tau == p1 - 1;
    const bool diag = tau == NSTEP - 1 || (c == 1 && (tau == p1 - 2 || tau == p1 - 1));
    const bool first = tau == 0 || (c == 1 && tau == p1);
    int ln = lane;
    asm volatile("" : "+v"(ln));
    if (diag) {
      const int hp = tau == NSTEP - 1 ? c : tau - (p1 - 2);
      float ninf = -INFINITY;
      asm volatile("" : "+v"(ninf));
      const int dq = (ln & 31) - 4 * (ln >> 5);
      if (2 * hp > rr) mask_all_<V_S0>(ninf, std::make_integer_sequence<int, 16>{});
      else if (2 * hp == rr) mask_tri_<V_S0>(dq, ninf, std::make_integer_sequence<int, 16>{});
      if (2 * hp + 1 > rr) mask_all_<V_S1>(ninf, std::make_integer_sequence<int, 16>{});
      else if (2 * hp + 1 == rr) mask_tri_<V_S1>(dq, ninf, std::make_integer_sequence<int, 16>{});
    }
    if (first && tau > 0) {
      ACC_FENCE();
      const float inv = 1.0f / lfin;
      u16* dstp = CF + ((size_t)(((4 * qbL + rr) * 32 + head) * 8) * 64 + ln) * 8;
      *(u32x4*)(dstp) = acc_scale8<A_O + 0>(inv);          *(u32x4*)(dstp + 512) = acc_scale8<A_O + 8>(inv);
      *(u32x4*)(dstp + 1024) = acc_scale8<A_O + 16>(inv);  *(u32x4*)(dstp + 1536) = acc_scale8<A_O + 24>(inv);
      *(u32x4*)(dstp + 2048) = acc_scale8<A_O + 32>(inv);  *(u32x4*)(dstp + 2560) = acc_scale8<A_O + 40>(inv);
      *(u32x4*)(dstp + 3072) = acc_scale8<A_O + 48>(inv);  *(u32x4*)(dstp + 3584) = acc_scale8<A_O + 56>(inv);
    }
    if (first) refix(true);
    float pst = exps32();
    if (!first && __any(pst > P_LIMIT)) { refix(false); pst = exps32(); }
    asm volatile("v_add_f32 %0, %0, %1" : "+v"(lsum) : "v"(pst));
    if (lastL || tau == NSTEP - 1) {
      mfin = mref; lfin = swap_add(lsum);
      if (lastL) {
        negm_set(0.f, 0.f);
        q_load(lds0 + L_STASH + rr * 8192 + ln * 16);
      } else {
        f32x2* lp = (f32x2*)lamp + (ln & 31);
        if (c == 0) { if (ln < 32) *lp = (f32x2){mfin, lfin}; }
        else mlA = *lp;
      }
    }
    __builtin_amdgcn_s_setprio(0);
  };
#define KVDMA(i) do { if ((i) == 0) dma1o<0>(rK, vlin, gk, lk); else if ((i) == 1) dma1o<1024>(rK, vlin, gk, lk); \
    else if ((i) == 2) dma1o<2048>(rK, vlin, gk, lk); else if ((i) == 3) dma1o<3072>(rK, vlin, gk, lk); \
    else if ((i) == 4) dma1o<0>(rV, vlin, gv, lv); else if ((i) == 5) dma1o<1024>(rV, vlin, gv, lv); \
    else if ((i) == 6) dma1o<2048>(rV, vlin, gv, lv); else dma1o<3072>(rV, vlin, gv, lv); } while (0)
#define MSTEP(J, FIRST, LAST, DMA, ZO) do { \
    constexpr int out_ = ((J) + PD - 1 < (LAST) ? (J) + PD - 1 : (LAST)) - (J);     \
    if constexpr ((J) < 2) qk_head<out_, (J) % NRING, 0, ((J) & 1) ? V_S1 : V_S0>(); \
    else if constexpr ((J) < 16) qk_mfma<out_, (J) % NRING, ((J) >> 1), ((J) & 1) ? V_S1 : V_S0>(); \
    else if constexpr ((J) < 20 && (ZO)) { if (zero_o) pv_zero<out_, (J) % NRING, (J) & 3, 0>(); else pv_mfma<out_, (J) % NRING, (J) & 3, 0>(); } \
    else pv_mfma<out_, (J) % NRING, (J) & 3, (((J) - 16) >> 2)>(); \
    if constexpr ((J) + PD <= (LAST)) frag_read<(J) + PD>(ka, va); \
    if constexpr ((DMA) && ((J) & 1) && (J) < 16) KVDMA((J) >> 1); \
    SBAR(); } while (0)
#define MSTEP4(J, FIRST, LAST, DMA, ZO) MSTEP(J, FIRST, LAST, DMA, ZO); MSTEP((J) + 1, FIRST, LAST, DMA, ZO); MSTEP((J) + 2, FIRST, LAST, DMA, ZO); MSTEP((J) + 3, FIRST, LAST, DMA, ZO)
#define KSLOT(t) (((t) < p1) ? (((t) & 1) ? L_KA1 : L_KA0) : (((t) & 1) ? kown1 : kown0))
#define VSLOT(t) (((t) < p1) ? (((t) & 1) ? L_VA1 : L_VA0) : (((t) & 1) ? vown1 : vown0))
#define STAGE_ADDR(t) \
      const unsigned gk = ((t) + 1 < NSTEP && (c == 0 || (t) + 1 >= p1)) ? kvoff((t) + 1) + rr * 4096 : 0x7ff00000u; \
      const unsigned gv = (c == 0 || (t) >= p1) ? kvoff(t) + rr * 4096 : 0x7ff00000u; \
      const unsigned lk = lds0 + ((((t) + 1) & 1) ? kown1 : kown0) + rr * 4096; \
      const unsigned lv = lds0 + (((t) & 1) ? vown1 : vown0) + rr * 4096;

  if (c == 1) asm volatile("s_barrier" ::: "memory");
  {
    const unsigned ka = lrd + L_KA0, va = 0;
    STAGE_ADDR(0)
    constexpr bool zero_o = false;
    frag_read<0>(ka, va); frag_read<1>(ka, va); frag_read<2>(ka, va); frag_read<3>(ka, va);
    SBAR();
    MSTEP4(0, 0, 15, true, false); MSTEP4(4, 0, 15, true, false); MSTEP4(8, 0, 15, true, false); MSTEP4(12, 0, 15, true, false);
    BAR_A("lgkmcnt(0)");
    ACC_FENCE();
    sgen(0);
    BAR_A("vmcnt(0) lgkmcnt(0)");
  }
#define MPHASE(tau, DMA, ZO) do { \
      const bool zero_o = (tau) == 1 || (c == 1 && (tau) == p1 + 1);     \
      (void)zero_o; \
      const unsigned ka = lrd + KSLOT(tau); \
      const unsigned va = lrd + VSLOT((tau) - 1); \
      STAGE_ADDR(tau) \
      (void)gk; (void)gv; (void)lk; (void)lv; \
      frag_read<0>(ka, va); frag_read<1>(ka, va); frag_read<2>(ka, va); frag_read<3>(ka, va); \
      SBAR(); \
      MSTEP4(0, 0, 31, DMA, ZO); MSTEP4(4, 0, 31, DMA, ZO); MSTEP4(8, 0, 31, DMA, ZO); MSTEP4(12, 0, 31, DMA, ZO); \
      MSTEP4(16, 0, 31, DMA, ZO); MSTEP4(20, 0, 31, DMA, ZO); MSTEP4(24, 0, 31, DMA, ZO); \
      BAR_A("lgkmcnt(0)"); } while (0)
  int tau = 1;
  while (tau < NSTEP) {
    const bool special = tau == 1 || tau == NSTEP - 1 || (c == 1 && tau >= p1 - 2 && tau <= p1 + 1);
    if (special) {
      MPHASE(tau, true, true);
      sgen(tau);
      BAR_A("vmcnt(0) lgkmcnt(0)");
      ++tau;
    } else if (c == 1 && tau < p1 - 2) {
      do {
        MPHASE(tau, false, false);
        splain();
        BAR_A("lgkmcnt(0)");
        ++tau;
      } while (tau < p1 - 2);
    } else {
      do {
        MPHASE(tau, true, false);
        splain();
        BAR_A("vmcnt(0) lgkmcnt(0)");
        ++tau;
      } while (tau < NSTEP - 1);
    }
  }
#undef MPHASE
#undef TRAIL
  {
    const unsigned v128 = lane * 128;
    const unsigned dead = lds0 + vown1 + rr * 256;
    const int u = idx * 8 + wid;
    if (u < 160) warm8k(mk_rsrc(wo, NH * HD * DM * 2), v128, (unsigned)((xcd >> 1) * 1024 * 1280 + u * 8192), dead);
  }
  {
    const unsigned ka = 0, va = lrd + VSLOT(NSTEP - 1);
    const unsigned gk = 0, gv = 0, lk = 0, lv = 0;
    (void)gk; (void)gv; (void)lk; (void)lv; (void)ka;
    const bool zero_o = c == 1 && p1 == NSTEP - 1;
    frag_read<16>(ka, va); frag_read<17>(ka, va); frag_read<18>(ka, va); frag_read<19>(ka, va);
    SBAR();
    MSTEP4(16, 16, 31, false, true); MSTEP4(20, 16, 31, false, true); MSTEP4(24, 16, 31, false, true); MSTEP4(28, 16, 31, false, true);
    BAR_A("lgkmcnt(0)");
    int ln = lane;
    asm volatile("" : "+v"(ln));
    const unsigned mine = lds0 + (c ? L_KB1 : L_KA0) + ((rr & 2) ? (c ? L_KB0 - L_KB1 : L_KA1 - L_KA0) : 0u) + (rr & 1) * 8192 + ln * 16;
    const unsigned theirs = (c ? L_KA0 : L_KB1) + ((rr & 2) ? (c ? L_KA1 - L_KA0 : L_KB0 - L_KB1) : 0u) + (rr & 1) * 8192 + ln * 16;
    ACC_FENCE();
    if (c == 0) {
      acc_to_lds<A_O + 32, 0>(mine); acc_to_lds<A_O + 36, 1024>(mine); acc_to_lds<A_O + 40, 2048>(mine); acc_to_lds<A_O + 44, 3072>(mine);
      acc_to_lds<A_O + 48, 4096>(mine); acc_to_lds<A_O + 52, 5120>(mine); acc_to_lds<A_O + 56, 6144>(mine); acc_to_lds<A_O + 60, 7168>(mine);
      BAR_A("lgkmcnt(0)");
    } else {
      acc_to_lds<A_O + 0, 0>(mine); acc_to_lds<A_O + 4, 1024>(mine); acc_to_lds<A_O + 8, 2048>(mine); acc_to_lds<A_O + 12, 3072>(mine);
      acc_to_lds<A_O + 16, 4096>(mine); acc_to_lds<A_O + 20, 5120>(mine); acc_to_lds<A_O + 24, 6144>(mine); acc_to_lds<A_O + 28, 7168>(mine);
      if (ln < 32) *(LAS f32x2*)(lds + L_STASH + rr * 256 + ln * 8) = (f32x2){mfin, lfin};
    }
    BAR_A("lgkmcnt(0)");
    {
      const f32x2 mo = c ? mlA : *(LAS const f32x2*)(lds + L_STASH + rr * 256 + (ln & 31) * 8);
      const float m = fmaxf(mo[0], mfin);
      const float eo = __builtin_amdgcn_exp2f(mo[0] - m), em = __builtin_amdgcn_exp2f(mfin - m);
      const float inv = 1.0f / (mo[1] * eo + lfin * em);
      const float co = eo * inv, cm = em * inv;
      f32x4 w[8];
#pragma unroll
      for (int i = 0; i < 8; ++i) w[i] = *(LAS const f32x4*)(lds + theirs + i * 1024);
      u16* dstp = CF + ((size_t)(((4 * qbH + rr) * 32 + head) * 8 + 4 * c) * 64 + ln) * 8;
      if (c == 0) {
        *(u32x4*)(dstp) = acc_combine8f<A_O + 0>(w[0], w[1], co, cm);         *(u32x4*)(dstp + 512) = acc_combine8f<A_O + 8>(w[2], w[3], co, cm);
        *(u32x4*)(dstp + 1024) = acc_combine8f<A_O + 16>(w[4], w[5], co, cm); *(u32x4*)(dstp + 1536) = acc_combine8f<A_O + 24>(w[6], w[7], co, cm);
      } else {
        *(u32x4*)(dstp) = acc_combine8f<A_O + 32>(w[0], w[1], co, cm);        *(u32x4*)(dstp + 512) = acc_combine8f<A_O + 40>(w[2], w[3], co, cm);
        *(u32x4*)(dstp + 1024) = acc_combine8f<A_O + 48>(w[4], w[5], co, cm); *(u32x4*)(dstp + 1536) = acc_combine8f<A_O + 56>(w[6], w[7], co, cm);
      }
    }
  }
#undef KSLOT
#undef VSLOT
#undef STAGE_ADDR
#undef KVDMA
#undef MSTEP
#undef MSTEP4
}

constexpr int K3_STAGE = 37888, K3_NST = 4, K3_LDS = K3_STAGE * K3_NST;

__global__ __launch_bounds__(512, 2) void k3_oproj(const u16* __restrict__ CF, const u16* __restrict__ wo,
                                                   u16* __restrict__ PT) {
  extern __shared__ __attribute__((aligned(16))) char smem[];
  LAS char* lds = (LAS char*)smem;
  const unsigned lds0 = (unsigned)(uintptr_t)lds;
  const int tid = threadIdx.x, lane = tid & 63;
  const int wid = RFL(tid >> 6);
  const int b = blockIdx.x, xcd = b & 7, idx = b >> 3;
  const int ks = xcd >> 1, panel = (xcd & 1) * 8 + (idx >> 2), nt = idx & 3;
  const int tbi = wid & 3, kp = wid >> 2;
  const int h = lane >> 5, g1 = (lane >> 4) & 1, q4 = (lane & 15) >> 2, p4 = lane & 3;
  const __amdgpu_buffer_rsrc_t rC = mk_rsrc(CF, NH * SEQ * HD * 2);
  const __amdgpu_buffer_rsrc_t rW = mk_rsrc(wo, NH * HD * DM * 2);
  const unsigned vlin = lane * 16;
  unsigned voffW[3];
#pragma unroll
  for (int i = 0; i < 3; ++i) {
    const int pc = wid + 8 * i;
    const int u = 2 * pc + (lane >> 5);
    const int kb = u / 5, nb = u % 5;
    voffW[i] = (unsigned)((kb * 8 + ((lane >> 2) & 7)) * 1280 + (nb * 32 + (lane & 3) * 8) * 2);
  }
  const bool lo = wid < 4;
  const unsigned sW0 = (unsigned)(ks * 1024 * 1280 + nt * 320);
  const unsigned sC0 = (unsigned)(((panel * 4 + tbi) * 256 + ks * 64) * 1024);
  const __amdgpu_buffer_rsrc_t r2 = lo ? rW : rC;
  const unsigned v2 = lo ? voffW[2] : vlin;
  const unsigned s2 = lo ? sW0 : sC0, st2 = lo ? 81920u : 4096u, l2 = lo ? (wid + 16) * 1024 : 20480 + tbi * 4096;
  const unsigned s3 = sC0 + (lo ? 3072u : 1024u), l3 = 20480 + tbi * 4096 + (lo ? 3072 : 1024);
  const unsigned s4 = lo ? 0x7ff00000u : sC0 + 2048u, l4 = lo ? 36864 : 20480 + tbi * 4096 + 2048;
  auto piece = [&](int j, int slot, int i) __attribute__((always_inline)) {
    const unsigned sb = lds0 + slot * K3_STAGE;
    if (i == 0) dma1(rW, voffW[0], sW0 + j * 81920, sb + wid * 1024);
    else if (i == 1) dma1(rW, voffW[1], sW0 + j * 81920, sb + (wid + 8) * 1024);
    else if (i == 2) dma1(r2, v2, s2 + j * st2, sb + l2);
    else if (i == 3) dma1(rC, vlin, s3 + j * 4096, sb + l3);
    else dma1(rC, vlin, s4 + j * 4096, sb + l4);
  };
  f32x16 acc[5];
#pragma unroll
  for (int n = 0; n < 5; ++n) acc[n] = f32x16{};
  const unsigned wbase = h * 256 + q4 * 64 + g1 * 32 + p4 * 8 + kp * 4 * 2560;
  const unsigned cbase = 20480 + tbi * 4096 + lane * 16 + kp * 2048;
  auto rd = [&](bf16x8& cf, bf16x8 (&w)[5], LAS const char* sb, int si) {
    cf = lds_b128(sb + cbase + si * 1024);
#pragma unroll
    for (int n = 0; n < 5; ++n)
      w[n] = lds_tr2(sb + wbase + (2 * si) * 2560 + n * 512, sb + wbase + (2 * si + 1) * 2560 + n * 512);
  };
  bf16x8 cA, cB, wA[5], wB[5];
#pragma unroll
  for (int j = 0; j < 3; ++j)
#pragma unroll
    for (int i = 0; i < 5; ++i) piece(j, j, i);
  auto stage = [&](const int j, const int slot, const bool more, const bool first) __attribute__((always_inline)) {
    LAS const char* sb = lds + slot * K3_STAGE;
    rd(cA, wA, sb, 0);
    SBAR();
    if (!first) {
      acc[0] = MFMA32(wB[0], cB, acc[0]); acc[1] = MFMA32(wB[1], cB, acc[1]);
      if (more) piece(j + 3, (slot + 3) & 3, 0);
      SBAR();
      acc[2] = MFMA32(wB[2], cB, acc[2]); acc[3] = MFMA32(wB[3], cB, acc[3]);
      if (more) piece(j + 3, (slot + 3) & 3, 1);
      SBAR();
      acc[4] = MFMA32(wB[4], cB, acc[4]);
    } else {
      piece(3, 3, 0); piece(3, 3, 1);
    }
    SBAR();
    rd(cB, wB, sb, 1);
    SBAR();
    acc[0] = MFMA32(wA[0], cA, acc[0]);
    if (more) piece(j + 3, (slot + 3) & 3, 2);
    SBAR();
    acc[1] = MFMA32(wA[1], cA, acc[1]); acc[2] = MFMA32(wA[2], cA, acc[2]);
    if (more) piece(j + 3, (slot + 3) & 3, 3);
    SBAR();
    acc[3] = MFMA32(wA[3], cA, acc[3]); acc[4] = MFMA32(wA[4], cA, acc[4]);
    if (more) piece(j + 3, (slot + 3) & 3, 4);
    SBAR();
  };
  WAIT_VL_BAR(10); stage(0, 0, true, true);
  for (int j4 = 1; j4 < 13; j4 += 4) {
    WAIT_VL_BAR(10); stage(j4, 1, true, false);
    WAIT_VL_BAR(10); stage(j4 + 1, 2, true, false);
    WAIT_VL_BAR(10); stage(j4 + 2, 3, true, false);
    WAIT_VL_BAR(10); stage(j4 + 3, 0, true, false);
  }
  WAIT_VL_BAR(10); stage(13, 1, false, false);
  WAIT_VL_BAR(5); stage(14, 2, false, false);
  WAIT_VL_BAR(0); stage(15, 3, false, false);
#pragma unroll
  for (int n = 0; n < 5; ++n) acc[n] = MFMA32(wB[n], cB, acc[n]);
  WAIT_L_BAR();
  {
    LAS char* mine = lds + (tbi * 2 + kp) * 12288 + lane * 16;
    LAS const char* theirs = lds + (tbi * 2 + (kp ^ 1)) * 12288 + lane * 16;
    auto put = [&](const f32x16& a, int slot) {
#pragma unroll
      for (int gq = 0; gq < 4; ++gq)
        *(LAS f32x4*)(mine + (slot * 4 + gq) * 1024) = (f32x4){a[4 * gq], a[4 * gq + 1], a[4 * gq + 2], a[4 * gq + 3]};
    };
    auto fin = [&](const f32x16& a, int slot, int n) {
      f32x16 t;
#pragma unroll
      for (int gq = 0; gq < 4; ++gq) {
        const f32x4 v = *(LAS const f32x4*)(theirs + (slot * 4 + gq) * 1024);
#pragma unroll
        for (int e = 0; e < 4; ++e) t[4 * gq + e] = a[4 * gq + e] + v[e];
      }
      u16* dst = PT + ((size_t)((((ks * 64 + panel * 4 + tbi) * 20 + nt * 5 + n) * 2) * 64 + lane)) * 8;
      *(u32x4*)dst = pack8(t, 0);
      *(u32x4*)(dst + 512) = pack8(t, 8);
    };
    if (kp == 0) { put(acc[3], 0); put(acc[4], 1); } else { put(acc[0], 0); put(acc[1], 1); put(acc[2], 2); }
    WAIT_L_BAR();
    if (kp == 0) { fin(acc[0], 0, 0); fin(acc[1], 1, 1); fin(acc[2], 2, 2); } else { fin(acc[3], 0, 3); fin(acc[4], 1, 4); }
  }
}

__global__ __launch_bounds__(320) void k4_ln(const u16* __restrict__ x, const u16* __restrict__ PT,
                                             float* __restrict__ H, u16* __restrict__ HN, const u16* __restrict__ w1) {
  __shared__ __attribute__((aligned(16))) u16 tile[8 * 648];
  __shared__ float red[2][5][8];
  __shared__ __attribute__((aligned(16))) char dead[5 * 256];
  const int tid = threadIdx.x, lane = tid & 63, wid = RFL(tid >> 6);
  const int b = blockIdx.x;
  const int r8 = lane & 7, hh = (lane >> 3) & 1, sg = (lane >> 4) & 1, ml = lane >> 5;
  const int token = b * 8 + r8, tb32 = token >> 5, rr = token & 31;
  u32x2 xv[2][2];
  u32x4 pv[2][4];
#pragma unroll
  for (int i = 0; i < 2; ++i) {
    const int mb = wid * 4 + 2 * i + ml;
    const int m0 = mb * 32 + sg * 16 + hh * 4;
    xv[i][0] = *(const u32x2*)(x + (size_t)token * DM + m0);
    xv[i][1] = *(const u32x2*)(x + (size_t)token * DM + m0 + 8);
#pragma unroll
    for (int ks = 0; ks < 4; ++ks)
      pv[i][ks] = *(const u32x4*)(PT + ((size_t)((((ks * 64 + tb32) * 20 + mb) * 2 + sg) * 64 + hh * 32 + rr)) * 8);
  }
  SBAR();
  float v[2][8];
  float s = 0.f;
#pragma unroll
  for (int i = 0; i < 2; ++i) {
    const int m0 = (wid * 4 + 2 * i + ml) * 32 + sg * 16 + hh * 4;
#pragma unroll
    for (int q = 0; q < 4; ++q) {
      const unsigned xw = xv[i][q >> 1][q & 1];
      v[i][2 * q] = bflo(xw) + ((bflo(pv[i][0][q]) + bflo(pv[i][1][q])) + (bflo(pv[i][2][q]) + bflo(pv[i][3][q])));
      v[i][2 * q + 1] = bfhi(xw) + ((bfhi(pv[i][0][q]) + bfhi(pv[i][1][q])) + (bfhi(pv[i][2][q]) + bfhi(pv[i][3][q])));
    }
    *(f32x4*)(H + (size_t)token * DM + m0) = (f32x4){v[i][0], v[i][1], v[i][2], v[i][3]};
    *(f32x4*)(H + (size_t)token * DM + m0 + 8) = (f32x4){v[i][4], v[i][5], v[i][6], v[i][7]};
#pragma unroll
    for (int e = 0; e < 8; ++e) s += v[i][e];
  }
  {
    const int u = (b >> 3) * 5 + wid;
    const int a4 = (b & 7) >> 1;
    const unsigned dd = (unsigned)(uintptr_t)(LAS char*)dead + wid * 256;
    const __amdgpu_buffer_rsrc_t r1 = mk_rsrc(w1, SEQ * FF * 2);
#pragma unroll
    for (int i = 0; i < 2; ++i) {
      const int line = (u + 160 * i) * 64 + lane;
      const unsigned off = line < 14336 ? (unsigned)((line / 7) * (FF * 2) + a4 * 896 + (line % 7) * 128) : 0x7ff00000u;
      warm8k(r1, off, 0u, dd);
    }
  }
  s += __shfl_xor(s, 8); s += __shfl_xor(s, 16); s += __shfl_xor(s, 32);
  if (lane < 8) red[0][wid][lane] = s;
  WAIT_L_BAR();
  const float mean = ((red[0][0][r8] + red[0][1][r8]) + (red[0][2][r8] + red[0][3][r8]) + red[0][4][r8]) * (1.0f / DM);
  float sq = 0.f;
#pragma unroll
  for (int i = 0; i < 2; ++i)
#pragma unroll
    for (int e = 0; e < 8; ++e) { v[i][e] -= mean; sq += v[i][e] * v[i][e]; }
  sq += __shfl_xor(sq, 8); sq += __shfl_xor(sq, 16); sq += __shfl_xor(sq, 32);
  if (lane < 8) red[1][wid][lane] = sq;
  WAIT_L_BAR();
  const float var = ((red[1][0][r8] + red[1][1][r8]) + (red[1][2][r8] + red[1][3][r8]) + red[1][4][r8]) * (1.0f / DM);
  const float r1 = 1.0f / sqrtf(var + LN_EPS);
  const float r2 = 1.0f / sqrtf(var * r1 * r1 + LN_EPS);
  const float sc = r1 * r2;
#pragma unroll
  for (int i = 0; i < 2; ++i) {
    const int m0 = (wid * 4 + 2 * i + ml) * 32 + sg * 16 + hh * 4;
    *(u32x2*)(tile + r8 * 648 + m0) = (u32x2){cvtpk(v[i][0] * sc, v[i][1] * sc), cvtpk(v[i][2] * sc, v[i][3] * sc)};
    *(u32x2*)(tile + r8 * 648 + m0 + 8) = (u32x2){cvtpk(v[i][4] * sc, v[i][5] * sc), cvtpk(v[i][6] * sc, v[i][7] * sc)};
  }
  WAIT_L_BAR();
  const int ks32 = b >> 2, g = b & 3;
#pragma unroll
  for (int i = 0; i < 2; ++i) {
    const int m = tid + 320 * i;
    u16 e[8];
#pragma unroll
    for (int j = 0; j < 8; ++j) e[j] = tile[j * 648 + m];
    u32x4 w = {(unsigned)e[0] | ((unsigned)e[1] << 16), (unsigned)e[2] | ((unsigned)e[3] << 16),
               (unsigned)e[4] | ((unsigned)e[5] << 16), (unsigned)e[6] | ((unsigned)e[7] << 16)};
    *(u32x4*)(HN + ((size_t)(((m >> 4) * 64 + ks32) * 64 + g * 16 + (m & 15))) * 8) = w;
  }
}

constexpr int K56_LDS = 163840;

DI float gelu_tanh(float x) {
  const float u = 0.7978845608028654f * (x + 0.044715f * x * x * x);
  const float e = __builtin_amdgcn_exp2f(u * 2.8853900817779268f);
  const float t = 1.0f - 2.0f * __builtin_amdgcn_rcpf(e + 1.0f);
  return 0.5f * x * (1.0f + t);
}

#define ACLOB56 "a0","a1","a2","a3","a4","a5","a6","a7","a8","a9","a10","a11","a12","a13","a14","a15","a16","a17","a18","a19","a20","a21","a22","a23","a24","a25","a26","a27","a28","a29","a30","a31","a32","a33","a34","a35","a36","a37","a38","a39","a40","a41","a42","a43","a44","a45","a46","a47","a48","a49","a50","a51","a52","a53","a54","a55","a56","a57","a58","a59","a60","a61","a62","a63","a64","a65","a66","a67","a68","a69","a70","a71","a72","a73","a74","a75","a76","a77","a78","a79","a80","a81","a82","a83","a84","a85","a86","a87","a88","a89","a90","a91"
template <int R> DI void m_aload(__amdgpu_buffer_rsrc_t rsrc, unsigned voff, unsigned soff) {
  asm volatile("buffer_load_dwordx4 a[%c3:%c4], %0, %1, %2 offen" ::"v"(voff), "s"(rsrc), "s"(soff), "i"(R), "i"(R + 3) : "memory");
}
template <int R, int OFF> DI void m_trread(unsigned addr) {
  asm volatile("ds_read_b64_tr_b16 a[%c1:%c2], %0 offset:%c3" ::"v"(addr), "i"(R), "i"(R + 1), "i"(OFF) : "memory");
}
template <int RA, int RB> DI void m_mfma(f32x4& d) {
  asm volatile("v_mfma_f32_16x16x32_bf16 %0, a[%c1:%c2], a[%c3:%c4], %0" : "+v"(d) : "i"(RA), "i"(RA + 3), "i"(RB), "i"(RB + 3));
}

template <int MODE>
__global__ __launch_bounds__(512, 2) void k56_mix(const u16* __restrict__ W, const u16* __restrict__ AF,
                                                  const u16* __restrict__ bias, const float* __restrict__ H,
                                                  u16* __restrict__ outp, const u16* __restrict__ wnext) {
  constexpr int LDW = MODE == 0 ? FF : SEQ;
  constexpr int NKS = MODE == 0 ? 64 : 54;
  constexpr int NK = MODE == 0 ? 8 : 7;
  constexpr int KROWS = MODE == 0 ? SEQ : FF;
  extern __shared__ __attribute__((aligned(16))) char smem[];
  LAS char* lds = (LAS char*)smem;
  const unsigned lds0 = (unsigned)(uintptr_t)lds;
  const int tid = threadIdx.x, lane = tid & 63;
  const int wid = RFL(tid >> 6);
  const int b = blockIdx.x, xcd = b & 7, idx = b >> 3;
  int wt, dt;
  if (MODE == 0) {
    wt = (xcd >> 1) * 7 + (idx >> 2); dt = (xcd & 1) * 4 + (idx & 3);
    if (idx >= 28 || wt >= 27) {
      if (idx >= 28) {
        const __amdgpu_buffer_rsrc_t r2 = mk_rsrc(wnext, SEQ * FF * 2);
#pragma unroll
        for (int i = 0; i < 7; ++i) {
          const int line = (((idx - 28) * 8 + wid) * 7 + i) * 64 + lane;
          const unsigned off = line < 13824 ? (unsigned)((line >> 3) * 4096 + (xcd >> 1) * 1024 + (line & 7) * 128) : 0x7ff00000u;
          warm8k(r2, off, 0u, lds0 + wid * 256);
        }
      }
      return;
    }
  } else {
    wt = (xcd >> 1) * 8 + (idx >> 2); dt = (xcd & 1) * 4 + (idx & 3);
  }
  const __amdgpu_buffer_rsrc_t rW = mk_rsrc(W, KROWS * LDW * 2);
  const __amdgpu_buffer_rsrc_t rA = mk_rsrc(AF, 40 * NKS * 1024);
  const unsigned vlin = lane * 16;
  const int prow = (lane >> 2) & 7;
  const unsigned voffW0 = (unsigned)(prow * LDW * 2 + (lane >> 5) * 64 +
                                     (((lane & 3) ^ (MODE == 1 ? (((prow >> 2) & 1) << 1) : 0)) << 4));
  const unsigned voffW1 = MODE == 1 ? voffW0 : (voffW0 ^ 32u);
  const unsigned myl = lds0 + wid * 20480;
  const int g = lane >> 4, q4 = (lane & 15) >> 2, p4 = lane & 3;
  unsigned wb[2][2];
#pragma unroll
  for (int t = 0; t < 2; ++t)
#pragma unroll
    for (int hf = 0; hf < 2; ++hf) {
      const int kb = MODE == 0 ? g : 2 * t + (g >> 1);
      const int row = MODE == 0 ? 4 * t + q4 : 4 * (g & 1) + q4;
      const int sw = MODE == 0 ? (kb & 1) : ((row >> 2) & 1);
      wb[t][hf] = myl + kb * 1024 + row * 64 + ((hf ^ sw) * 32) + p4 * 8;
    }
  f32x4 acc[4][5];
#pragma unroll
  for (int i = 0; i < 4; ++i)
#pragma unroll
    for (int d = 0; d < 5; ++d) acc[i][d] = (f32x4){0.f, 0.f, 0.f, 0.f};
  u32x2 eb0[2], eb1[2];
  f32x4 eh[3]; u16 ebs[3];
  if (MODE == 0) {
#pragma unroll
    for (int i = 0; i < 2; ++i) {
      const int u = wid + 8 * i < 10 ? wid + 8 * i : wid;
      const int f0 = wt * 64 + (u / 5) * 32 + 4 * g;
      eb0[i] = *(const u32x2*)(bias + f0); eb1[i] = *(const u32x2*)(bias + f0 + 16);
    }
  } else {
#pragma unroll
    for (int i = 0; i < 3; ++i) {
      const int u = wid + 8 * i < 20 ? wid + 8 * i : wid;
      const int s = wt * 64 + (u / 5) * 16 + (lane & 15), d0 = dt * 80 + (u % 5) * 16 + 4 * g;
      ebs[i] = bias[s];
      eh[i] = *(const f32x4*)(H + (size_t)s * DM + d0);
    }
  }
  SBAR();
#define KSTEP(IT) (wid + 8 * (IT))
#define WDMA(IT, KB) dma1(rW, ((KB) & 1) ? voffW1 : voffW0, (unsigned)((KSTEP(IT) * 32 + (KB) * 8) * LDW * 2 + wt * 128), myl + ((IT) % 5) * 4096 + (KB) * 1024)
#define ALOAD(IT, D) m_aload<((IT) % 3) * 20 + 4 * (D)>(rA, vlin, (unsigned)(((dt * 5 + (D)) * NKS + KSTEP(IT)) * 1024))
#define WDMA4(IT) do { WDMA(IT, 0); WDMA(IT, 1); WDMA(IT, 2); WDMA(IT, 3); } while (0)
#define ALOAD5(IT) do { ALOAD(IT, 0); ALOAD(IT, 1); ALOAD(IT, 2); ALOAD(IT, 3); ALOAD(IT, 4); } while (0)
#define WREAD1(IT, I) do { m_trread<60 + 16 * ((IT) & 1) + 4 * (I), ((IT) % 5) * 4096 + ((I) >> 1) * 512>(wb[0][(I) & 1]); \
                           m_trread<60 + 16 * ((IT) & 1) + 4 * (I) + 2, ((IT) % 5) * 4096 + ((I) >> 1) * 512>(wb[1][(I) & 1]); } while (0)
#define WREAD(IT) do { WREAD1(IT, 0); WREAD1(IT, 1); WREAD1(IT, 2); WREAD1(IT, 3); } while (0)
#define MM(IT, I, D) do { if constexpr (MODE == 0) m_mfma<60 + 16 * ((IT) & 1) + 4 * (I), ((IT) % 3) * 20 + 4 * (D)>(acc[I][D]); \
                          else m_mfma<((IT) % 3) * 20 + 4 * (D), 60 + 16 * ((IT) & 1) + 4 * (I)>(acc[I][D]); } while (0)
#define MMROW(IT, I) do { MM(IT, I, 0); MM(IT, I, 1); MM(IT, I, 2); MM(IT, I, 3); MM(IT, I, 4); } while (0)
#define NV(IT) (((IT) + 4 < NK ? 4 : 0) + ((IT) + 2 < NK ? 5 : 0))
#define ALLOW(IT) ((IT) == 0 ? (5 + (NK > 2 ? 4 : 0) + (NK > 3 ? 4 : 0)) : (IT) == 1 ? ((NK > 3 ? 4 : 0) + NV(0)) : NV((IT) - 1))
#define STEP(IT) do { if constexpr ((IT) < NK) { \
    asm volatile("s_waitcnt vmcnt(%c0) lgkmcnt(0)" ::"i"(ALLOW(IT)) : "memory", ACLOB56); \
    if constexpr ((IT) + 1 < NK) WREAD((IT) + 1); \
    SBAR(); \
    MMROW(IT, 0); if constexpr ((IT) + 4 < NK) { WDMA((IT) + 4, 0); WDMA((IT) + 4, 1); } SBAR(); \
    MMROW(IT, 1); if constexpr ((IT) + 4 < NK) { WDMA((IT) + 4, 2); WDMA((IT) + 4, 3); } SBAR(); \
    MMROW(IT, 2); if constexpr ((IT) + 2 < NK) { ALOAD((IT) + 2, 0); ALOAD((IT) + 2, 1); ALOAD((IT) + 2, 2); } SBAR(); \
    MMROW(IT, 3); if constexpr ((IT) + 2 < NK) { ALOAD((IT) + 2, 3); ALOAD((IT) + 2, 4); } SBAR(); \
    } } while (0)
  asm volatile("" ::: "memory", ACLOB56);
  WDMA4(0); ALOAD5(0); asm volatile("s_barrier" ::: "memory");
  WDMA4(1); ALOAD5(1); asm volatile("s_barrier" ::: "memory");
  WDMA4(2); WDMA4(3);
  asm volatile("s_waitcnt vmcnt(22)" ::: "memory");
  WREAD(0);
  STEP(0); STEP(1); STEP(2); STEP(3); STEP(4); STEP(5); STEP(6); STEP(7);
  asm volatile("s_nop 15" ::: "memory");
#undef KSTEP
#undef WDMA
#undef ALOAD
#undef WDMA4
#undef ALOAD5
#undef WREAD1
#undef WREAD
#undef MM
#undef MMROW
#undef NV
#undef ALLOW
#undef STEP
  WAIT_L0();
  {
    LAS char* dst = lds + wid * 20480 + lane * 16;
#pragma unroll
    for (int i = 0; i < 4; ++i)
#pragma unroll
      for (int d = 0; d < 5; ++d) *(LAS f32x4*)(dst + (i * 5 + d) * 1024) = acc[i][d];
  }
  WAIT_L_BAR();
  if (MODE == 0) {
#pragma unroll
    for (int i = 0; i < 2; ++i) {
      const int u = wid + 8 * i;
      if (u < 10) {
        const int a = u / 5, d = u % 5;
        f32x4 t0 = {0.f, 0.f, 0.f, 0.f}, t1 = {0.f, 0.f, 0.f, 0.f};
#pragma unroll
        for (int w = 0; w < 8; ++w) {
          t0 += *(LAS const f32x4*)(lds + w * 20480 + ((2 * a) * 5 + d) * 1024 + lane * 16);
          t1 += *(LAS const f32x4*)(lds + w * 20480 + ((2 * a + 1) * 5 + d) * 1024 + lane * 16);
        }
        const u32x2 b0 = eb0[i], b1 = eb1[i];
        t0 += (f32x4){bflo(b0[0]), bfhi(b0[0]), bflo(b0[1]), bfhi(b0[1])};
        t1 += (f32x4){bflo(b1[0]), bfhi(b1[0]), bflo(b1[1]), bfhi(b1[1])};
        u32x4 w = {cvtpk(gelu_tanh(t0[0]), gelu_tanh(t0[1])), cvtpk(gelu_tanh(t0[2]), gelu_tanh(t0[3])),
                   cvtpk(gelu_tanh(t1[0]), gelu_tanh(t1[1])), cvtpk(gelu_tanh(t1[2]), gelu_tanh(t1[3]))};
        *(u32x4*)(outp + ((size_t)(((dt * 5 + d) * 54 + wt * 2 + a) * 64 + lane)) * 8) = w;
      }
    }
  } else {
#pragma unroll
    for (int i = 0; i < 3; ++i) {
      const int u = wid + 8 * i;
      if (u < 20) {
        f32x4 t0 = {0.f, 0.f, 0.f, 0.f};
#pragma unroll
        for (int w = 0; w < 8; ++w) t0 += *(LAS const f32x4*)(lds + w * 20480 + u * 1024 + lane * 16);
        const int s = wt * 64 + (u / 5) * 16 + (lane & 15);
        const int d0 = dt * 80 + (u % 5) * 16 + 4 * g;
        const float bb = bf2f(ebs[i]);
        const f32x4 hv = eh[i];
        u32x2 w = {cvtpk(t0[0] + bb + hv[0], t0[1] + bb + hv[1]), cvtpk(t0[2] + bb + hv[2], t0[3] + bb + hv[3])};
        *(u32x2*)(outp + (size_t)s * DM + d0) = w;
      }
    }
  }
}

}

extern "C" void kernel_launch(void* const* d_in, const int* in_sizes, int n_in, void* d_out, int out_size, void* d_ws,
                              size_t ws_size, hipStream_t stream) {
  (void)in_sizes; (void)n_in; (void)out_size; (void)ws_size;
  const u16* x = (const u16*)d_in[0];
  const u16* ckv = (const u16*)d_in[1];
  const u16* wq = (const u16*)d_in[2];
  const u16* wuk = (const u16*)d_in[3];
  const u16* wuv = (const u16*)d_in[4];
  const u16* wo = (const u16*)d_in[5];
  const u16* w1 = (const u16*)d_in[7];
  const u16* b1 = (const u16*)d_in[8];
  const u16* w2 = (const u16*)d_in[9];
  const u16* b2 = (const u16*)d_in[10];
  char* ws = (char*)d_ws;
  u16* XN = (u16*)(ws + WS_XN);
  u16* KF = (u16*)(ws + WS_KF);
  u16* VF = (u16*)(ws + WS_VF);
  u16* CF = (u16*)(ws + WS_CF);
  u16* PT = (u16*)(ws + WS_PT);
  float* H = (float*)(ws + WS_H);
  u16* HN = (u16*)(ws + WS_HN);
  u16* UF = (u16*)(ws + WS_UF);
  (void)hipFuncSetAttribute((const void*)k1_kvproj, hipFuncAttributeMaxDynamicSharedMemorySize, K1_LDS);
  (void)hipFuncSetAttribute((const void*)k2_attn, hipFuncAttributeMaxDynamicSharedMemorySize, K2_LDS);
  (void)hipFuncSetAttribute((const void*)k3_oproj, hipFuncAttributeMaxDynamicSharedMemorySize, K3_LDS);
  (void)hipFuncSetAttribute((const void*)k56_mix<0>, hipFuncAttributeMaxDynamicSharedMemorySize, K56_LDS);
  (void)hipFuncSetAttribute((const void*)k56_mix<1>, hipFuncAttributeMaxDynamicSharedMemorySize, K56_LDS);
  k1_kvproj<<<256, 512, K1_LDS, stream>>>(x, ckv, wuk, wuv, KF, VF, XN, wq);
  k2_attn<<<256, 512, K2_LDS, stream>>>(XN, wq, KF, VF, CF, (float*)(ws + WS_LAM), wo);
  k3_oproj<<<256, 512, K3_LDS, stream>>>(CF, wo, PT);
  k4_ln<<<256, 320, 0, stream>>>(x, PT, H, HN, w1);
  k56_mix<0><<<256, 512, K56_LDS, stream>>>(w1, HN, b1, nullptr, UF, w2);
  k56_mix<1><<<256, 512, K56_LDS, stream>>>(w2, UF, b2, H, (u16*)d_out, nullptr);
}
```

```cpp
#include <hip/hip_runtime.h>
#include <stdint.h>
#include <utility>

namespace {

typedef unsigned short u16;
typedef __bf16 bf16x8 __attribute__((ext_vector_type(8)));
typedef __bf16 bf16x2 __attribute__((ext_vector_type(2)));
typedef short s16x4 __attribute__((ext_vector_type(4)));
typedef short s16x8 __attribute__((ext_vector_type(8)));
typedef float f32x2 __attribute__((ext_vector_type(2)));
typedef float f32x4 __attribute__((ext_vector_type(4)));
typedef float f32x16 __attribute__((ext_vector_type(16)));
typedef unsigned u32x2 __attribute__((ext_vector_type(2)));
typedef unsigned u32x4 __attribute__((ext_vector_type(4)));
#define LAS __attribute__((address_space(3)))
#define DI __device__ __forceinline__
#define SBAR() __builtin_amdgcn_sched_barrier(0)
#define RFL(x) __builtin_amdgcn_readfirstlane(x)

constexpr int SEQ = 2048, DM = 640, DC = 512, NH = 32, HD = 128, FF = 1728;
constexpr float LN_EPS = 1e-5f;
constexpr float QSCALE = 0.08838834764831845f * 1.4426950408889634f;
constexpr float P_LIMIT = 1073741824.0f;
constexpr float NEG_BIG = -1e30f;

constexpr size_t WS_XN = 0;
constexpr size_t WS_KF = WS_XN + (size_t)SEQ * DM * 2;
constexpr size_t WS_VF = WS_KF + (size_t)NH * SEQ * HD * 2;
constexpr size_t WS_CF = WS_VF + (size_t)NH * SEQ * HD * 2;
constexpr size_t WS_PT = WS_CF + (size_t)NH * SEQ * HD * 2;
constexpr size_t WS_H = WS_PT + (size_t)4 * SEQ * DM * 2;
constexpr size_t WS_HN = WS_H + (size_t)SEQ * DM * 4;
constexpr size_t WS_UF = WS_HN + (size_t)SEQ * DM * 2;
constexpr size_t WS_LAM = WS_UF + (size_t)DM * FF * 2;
constexpr size_t WS_END = WS_LAM + 256 * 256 * 4;
static_assert(WS_END <= ((size_t)256 << 20), "workspace");

DI __amdgpu_buffer_rsrc_t mk_rsrc(const void* p, unsigned bytes) {
  return __builtin_amdgcn_make_buffer_rsrc((void*)p, 0, bytes, 0x00020000);
}
DI void dma1(__amdgpu_buffer_rsrc_t rsrc, unsigned voff, unsigned soff, unsigned ldsaddr) {
  asm volatile("s_mov_b32 m0, %0\n\ts_nop 0\n\tbuffer_load_dwordx4 %1, %2, %3 offen lds"
               ::"s"(ldsaddr), "v"(voff), "s"(rsrc), "s"(soff) : "memory", "m0");
}
DI void warm8k(__amdgpu_buffer_rsrc_t rsrc, unsigned voff128, unsigned soff, unsigned ldsaddr) {
  asm volatile("s_mov_b32 m0, %0\n\ts_nop 0\n\tbuffer_load_dword %1, %2, %3 offen lds"
               ::"s"(ldsaddr), "v"(voff128), "s"(rsrc), "s"(soff) : "memory", "m0");
}
#define WAIT_VL_BAR(N) asm volatile("s_waitcnt vmcnt(" #N ") lgkmcnt(0)\n\ts_barrier" ::: "memory")
#define WAIT_L_BAR() asm volatile("s_waitcnt lgkmcnt(0)\n\ts_barrier" ::: "memory")
#define WAIT_L0() asm volatile("s_waitcnt lgkmcnt(0)" ::: "memory")

DI bf16x8 lds_b128(LAS const char* p) { return *(LAS const bf16x8*)p; }
DI bf16x8 lds_tr2(LAS const char* p0, LAS const char* p1) {
  s16x4 lo = __builtin_amdgcn_ds_read_tr16_b64_v4i16((LAS s16x4*)p0);
  s16x4 hi = __builtin_amdgcn_ds_read_tr16_b64_v4i16((LAS s16x4*)p1);
  s16x8 w = __builtin_shufflevector(lo, hi, 0, 1, 2, 3, 4, 5, 6, 7);
  return __builtin_bit_cast(bf16x8, w);
}
DI unsigned cvtpk(float lo, float hi) {
  f32x2 v = {lo, hi};
  bf16x2 b = __builtin_convertvector(v, bf16x2);
  return __builtin_bit_cast(unsigned, b);
}
DI u32x4 pack8(const f32x16& a, int o) {
  u32x4 w = {cvtpk(a[o], a[o + 1]), cvtpk(a[o + 2], a[o + 3]), cvtpk(a[o + 4], a[o + 5]), cvtpk(a[o + 6], a[o + 7])};
  return w;
}
DI float bf2f(unsigned short b) { return __uint_as_float(((unsigned)b) << 16); }
DI float bflo(unsigned w) { return __uint_as_float(w << 16); }
DI float bfhi(unsigned w) { return __uint_as_float(w & 0xffff0000u); }
#define MFMA32(a, b, c) __builtin_amdgcn_mfma_f32_32x32x16_bf16((a), (b), (c), 0, 0, 0)
#define MFMA16(a, b, c) __builtin_amdgcn_mfma_f32_16x16x32_bf16((a), (b), (c), 0, 0, 0)
DI float swap_max(float v) {
  auto rr = __builtin_amdgcn_permlane32_swap(__float_as_uint(v), __float_as_uint(v), false, false);
  return fmaxf(__uint_as_float(rr[0]), __uint_as_float(rr[1]));
}
DI float swap_add(float v) {
  auto rr = __builtin_amdgcn_permlane32_swap(__float_as_uint(v), __float_as_uint(v), false, false);
  return __uint_as_float(rr[0]) + __uint_as_float(rr[1]);
}

constexpr int K1_LDS = 5 * 32768;

template <bool ISV>
DI void k1_body(LAS char* lds, unsigned lds0, int wid, int lane, int head, int tb, const u16* __restrict__ ckv,
                const u16* __restrict__ wsrc, const u16* __restrict__ x, u16* __restrict__ KF, u16* __restrict__ VF,
                u16* __restrict__ XN, const u16* __restrict__ wq) {
  const int wm = wid >> 2, wn = wid & 3;
  const int r = lane & 31, h = lane >> 5;
  const int g1 = (lane >> 4) & 1, q = (lane & 15) >> 2, p = lane & 3;
  const __amdgpu_buffer_rsrc_t rA = mk_rsrc(ckv, SEQ * DC * 2);
  const __amdgpu_buffer_rsrc_t rWm = mk_rsrc(wsrc, DC * NH * HD * 2);
  const unsigned voffA = (lane >> 2) * 1024 + (((lane & 3) ^ ((lane >> 4) & 3)) << 4);
  const unsigned voffW = ((lane >> 2) & 7) * 8192 + (lane >> 5) * 64 + (lane & 3) * 16;
  auto piece = [&](int kt, int slot, int i) __attribute__((always_inline)) {
    const unsigned sb = lds0 + slot * 32768;
    if (i < 2) dma1(rA, voffA, (unsigned)((tb * 256 + (wid * 2 + i) * 16) * 1024 + kt * 64), sb + (wid * 2 + i) * 1024);
    else dma1(rWm, voffW, (unsigned)((kt * 32 + (wid >> 1) * 8) * 8192 + head * 256 + (i - 2) * 128),
              sb + 16384 + ((wid >> 1) * 4 + (wid & 1) * 2 + (i - 2)) * 1024);
  };
  unsigned abase[2];
#pragma unroll
  for (int s = 0; s < 2; ++s) abase[s] = wm * 8192 + r * 64 + (((2 * s + h) ^ ((r >> 2) & 3)) << 4);
  const unsigned wbase = 16384 + h * 4096 + wn * 1024 + q * 64 + g1 * 32 + p * 8;

  f32x16 acc[4][2];
#pragma unroll
  for (int i = 0; i < 4; ++i)
#pragma unroll
    for (int n = 0; n < 2; ++n) acc[i][n] = f32x16{};
  auto rd = [&](bf16x8 (&a)[4], bf16x8 (&w)[2], LAS const char* sb, int s) {
#pragma unroll
    for (int n = 0; n < 2; ++n) w[n] = lds_tr2(sb + wbase + s * 8192 + n * 512, sb + wbase + s * 8192 + n * 512 + 256);
#pragma unroll
    for (int i = 0; i < 4; ++i) a[i] = lds_b128(sb + abase[s] + i * 2048);
  };
  auto mm2 = [&](const bf16x8 (&a)[4], const bf16x8 (&w)[2], int i) {
#pragma unroll
    for (int n = 0; n < 2; ++n) {
      if (ISV) acc[i][n] = MFMA32(a[i], w[n], acc[i][n]);
      else acc[i][n] = MFMA32(w[n], a[i], acc[i][n]);
    }
  };
  bf16x8 aA[4], wA[2], aB[4], wB[2];
#pragma unroll
  for (int kt = 0; kt < 4; ++kt)
#pragma unroll
    for (int i = 0; i < 4; ++i) piece(kt, kt, i);
  auto stage = [&](const int kt, const int slot, const bool more, const bool first) __attribute__((always_inline)) {
    LAS const char* sb = lds + slot * 32768;
    rd(aA, wA, sb, 0);
    SBAR();
    if (!first) {
      mm2(aB, wB, 0); mm2(aB, wB, 1);
      if (more) piece(kt + 4, (slot + 4) % 5, 0);
      SBAR();
      mm2(aB, wB, 2); mm2(aB, wB, 3);
      if (more) piece(kt + 4, (slot + 4) % 5, 1);
    } else {
      piece(4, 4, 0); piece(4, 4, 1);
    }
    SBAR();
    rd(aB, wB, sb, 1);
    SBAR();
    mm2(aA, wA, 0); mm2(aA, wA, 1);
    if (more) piece(kt + 4, (slot + 4) % 5, 2);
    SBAR();
    mm2(aA, wA, 2); mm2(aA, wA, 3);
    if (more) piece(kt + 4, (slot + 4) % 5, 3);
    SBAR();
  };
  WAIT_VL_BAR(12); stage(0, 0, true, true);
  for (int k5 = 1; k5 < 11; k5 += 5) {
    WAIT_VL_BAR(12); stage(k5, 1, true, false);
    WAIT_VL_BAR(12); stage(k5 + 1, 2, true, false);
    WAIT_VL_BAR(12); stage(k5 + 2, 3, true, false);
    WAIT_VL_BAR(12); stage(k5 + 3, 4, true, false);
    WAIT_VL_BAR(12); stage(k5 + 4, 0, true, false);
  }
  WAIT_VL_BAR(12); stage(11, 1, true, false);
  WAIT_VL_BAR(12); stage(12, 2, false, false);
  WAIT_VL_BAR(8); stage(13, 3, false, false);
  WAIT_VL_BAR(4); stage(14, 4, false, false);
  WAIT_VL_BAR(0); stage(15, 0, false, false);
  mm2(aB, wB, 0); mm2(aB, wB, 1); mm2(aB, wB, 2); mm2(aB, wB, 3);
  {
    const __amdgpu_buffer_rsrc_t rQ = mk_rsrc(wq, DM * NH * HD * 2);
    const int u = (blockIdx.x >> 3) * 8 + wid;
    if (u < 80) warm8k(rQ, (unsigned)((lane >> 3) * 8192 + (lane & 7) * 128), (unsigned)(u * 8 * 8192 + (blockIdx.x & 7) * 1024), lds0 + 32768);
  }
  const int token = blockIdx.x * 8 + wid;
  const bool has1 = lane < 16;
  const u32x4 v0 = *(const u32x4*)(x + (size_t)token * DM + lane * 8);
  u32x4 v1 = {0u, 0u, 0u, 0u};
  if (has1) v1 = *(const u32x4*)(x + (size_t)token * DM + 512 + lane * 8);
  SBAR();
#pragma unroll
  for (int i = 0; i < 4; ++i) {
    const int kb32 = tb * 8 + wm * 4 + i;
#pragma unroll
    for (int n = 0; n < 2; ++n) {
#pragma unroll
      for (int sg = 0; sg < 2; ++sg) {
        const u32x4 w = pack8(acc[i][n], 8 * sg);
        if (ISV) {
          const int db = 2 * (wn - 2) + n;
          *(u32x4*)(VF + ((size_t)((((head * 64 + kb32) * 4 + db) * 2 + sg) * 64 + lane)) * 8) = w;
        } else {
          const int db = 2 * wn + n;
          *(u32x4*)(KF + ((size_t)(((head * 64 + kb32) * 8 + 2 * db + sg) * 64 + lane)) * 8) = w;
        }
      }
    }
  }
  {
    float f0[8], f1[8];
#pragma unroll
    for (int j = 0; j < 4; ++j) {
      f0[2 * j] = bflo(v0[j]); f0[2 * j + 1] = bfhi(v0[j]);
      f1[2 * j] = bflo(v1[j]); f1[2 * j + 1] = bfhi(v1[j]);
    }
    float s = 0.f;
#pragma unroll
    for (int j = 0; j < 8; ++j) s += f0[j] + f1[j];
#pragma unroll
    for (int o = 1; o < 64; o <<= 1) s += __shfl_xor(s, o);
    const float mean = s * (1.0f / DM);
    float sq = 0.f;
#pragma unroll
    for (int j = 0; j < 8; ++j) {
      const float d0 = f0[j] - mean; sq += d0 * d0;
      const float d1 = has1 ? f1[j] - mean : 0.f; sq += d1 * d1;
    }
#pragma unroll
    for (int o = 1; o < 64; o <<= 1) sq += __shfl_xor(sq, o);
    const float rstd = 1.0f / sqrtf(sq * (1.0f / DM) + LN_EPS);
    const int tb32 = token >> 5, rr = token & 31;
    u32x4 o0, o1;
#pragma unroll
    for (int j = 0; j < 4; ++j) {
      o0[j] = cvtpk((f0[2 * j] - mean) * rstd, (f0[2 * j + 1] - mean) * rstd);
      o1[j] = cvtpk((f1[2 * j] - mean) * rstd, (f1[2 * j + 1] - mean) * rstd);
    }
    *(u32x4*)(XN + ((size_t)((tb32 * 40 + (lane >> 1)) * 64 + (lane & 1) * 32 + rr)) * 8) = o0;
    if (has1) *(u32x4*)(XN + ((size_t)((tb32 * 40 + 32 + (lane >> 1)) * 64 + (lane & 1) * 32 + rr)) * 8) = o1;
  }
}

__global__ __launch_bounds__(512, 2) void k1_kvproj(const u16* __restrict__ x, const u16* __restrict__ ckv,
                                                    const u16* __restrict__ wuk, const u16* __restrict__ wuv,
                                                    u16* __restrict__ KF, u16* __restrict__ VF, u16* __restrict__ XN,
                                                    const u16* __restrict__ wq) {
  extern __shared__ __attribute__((aligned(16))) char smem[];
  LAS char* lds = (LAS char*)smem;
  const unsigned lds0 = (unsigned)(uintptr_t)lds;
  const int tid = threadIdx.x, lane = tid & 63;
  const int wid = RFL(tid >> 6);
  const int b = blockIdx.x, xcd = b & 7, idx = b >> 3;
  const int head = xcd * 4 + (idx & 3), tb = idx >> 2;
  const u16* wsrc = (wid & 1) ? wuv : wuk;
  if ((wid & 3) >= 2) k1_body<true>(lds, lds0, wid, lane, head, tb, ckv, wsrc, x, KF, VF, XN, wq);
  else k1_body<false>(lds, lds0, wid, lane, head, tb, ckv, wsrc, x, KF, VF, XN, wq);
}

constexpr int K2_LDS = 163840;
constexpr unsigned L_KB1 = 0, L_VB0 = 16384, L_VA1 = 32768, L_KA0 = 49152, L_KB0 = 65536, L_KA1 = 81920, L_VA0 = 98304,
                   L_VB1 = 114688, L_STASH = 131072;
constexpr int NSTEP = 17;
#define SPRIO 2
#define MPRIO 0
#define TRAILPRIO SPRIO

template <int OFF>
DI void dma1o(__amdgpu_buffer_rsrc_t rsrc, unsigned voff, unsigned soff, unsigned ldsaddr) {
  asm volatile("s_mov_b32 m0, %0\n\ts_nop 0\n\tbuffer_load_dwordx4 %1, %2, %3 offen offset:%4 lds"
               ::"s"(ldsaddr), "v"(voff), "s"(rsrc), "s"(soff), "i"(OFF) : "memory", "m0");
}
DI float fmul_s(float a, float b) { float r; asm("v_mul_f32 %0, %1, %2" : "=v"(r) : "v"(a), "v"(b)); return r; }
#define ACLOB "a8","a9","a10","a11","a12","a13","a14","a15","a16","a17","a18","a19","a20","a21","a22","a23","a24","a25","a26","a27","a28","a29","a30","a31","a32","a33","a34","a35","a36","a37","a38","a39","a40","a41","a42","a43","a44","a45","a46","a47","a48","a49","a50","a51","a52","a53","a54","a55","a56","a57","a58","a59","a60","a61","a62","a63","a64","a65","a66","a67","a68","a69","a70","a71","a72","a73","a74","a75","a76","a77","a78","a79","a80","a81","a82","a83","a84","a85","a86","a87","a88","a89","a90","a91","a92","a93","a94","a95","a96","a97","a98","a99","a100","a101","a102","a103","a104","a105","a106","a107","a108","a109","a110","a111","a112","a113","a114","a115","a116","a117","a118","a119","a120","a121","a122","a123","a124","a125","a126","a127"
constexpr int A_O = 8, A_Q = 72, A_R = 104, NRING = 6;
template <int SLOT, int OFF> DI void ring_read(unsigned addr) {
  asm volatile("ds_read_b128 a[%c1:%c2], %0 offset:%c3" ::"v"(addr), "i"(A_R + 4 * SLOT), "i"(A_R + 4 * SLOT + 3), "i"(OFF) : "memory");
}
template <int S, int OFF> DI void q_read(unsigned addr) {
  asm volatile("ds_read_b128 a[%c1:%c2], %0 offset:%c3" ::"v"(addr), "i"(A_Q + 4 * S), "i"(A_Q + 4 * S + 3), "i"(OFF) : "memory");
}
constexpr int V_NEGM = 64, V_S0 = 80, V_S1 = 96, V_PF = 112;
template <int W, int SLOT, int S, int DST> DI void qk_head() {
  asm volatile("s_waitcnt lgkmcnt(%c0)\n\tv_mfma_f32_32x32x16_bf16 v[%c1:%c2], a[%c3:%c4], a[%c5:%c6], v[%c7:%c8]"
               ::"i"(W), "i"(DST), "i"(DST + 15), "i"(A_R + 4 * SLOT), "i"(A_R + 4 * SLOT + 3), "i"(A_Q + 4 * S), "i"(A_Q + 4 * S + 3),
                 "i"(V_NEGM), "i"(V_NEGM + 15));
}
template <int W, int SLOT, int S, int DST> DI void qk_mfma() {
  asm volatile("s_waitcnt lgkmcnt(%c0)\n\tv_mfma_f32_32x32x16_bf16 v[%c1:%c2], a[%c3:%c4], a[%c5:%c6], v[%c1:%c2]"
               ::"i"(W), "i"(DST), "i"(DST + 15), "i"(A_R + 4 * SLOT), "i"(A_R + 4 * SLOT + 3), "i"(A_Q + 4 * S), "i"(A_Q + 4 * S + 3));
}
template <int W, int SLOT, int D, int P> DI void pv_mfma() {
  asm volatile("s_waitcnt lgkmcnt(%c0)\n\tv_mfma_f32_32x32x16_bf16 a[%c1:%c2], a[%c3:%c4], v[%c5:%c6], a[%c1:%c2]"
               ::"i"(W), "i"(A_O + 16 * D), "i"(A_O + 16 * D + 15), "i"(A_R + 4 * SLOT), "i"(A_R + 4 * SLOT + 3), "i"(V_PF + 4 * P),
                 "i"(V_PF + 4 * P + 3));
}
template <int W, int SLOT, int D, int P> DI void pv_zero() {
  asm volatile("s_waitcnt lgkmcnt(%c0)\n\tv_mfma_f32_32x32x16_bf16 a[%c1:%c2], a[%c3:%c4], v[%c5:%c6], 0"
               ::"i"(W), "i"(A_O + 16 * D), "i"(A_O + 16 * D + 15), "i"(A_R + 4 * SLOT), "i"(A_R + 4 * SLOT + 3), "i"(V_PF + 4 * P),
                 "i"(V_PF + 4 * P + 3));
}
template <int S, int P> DI void exps8(float& ps0, float& ps1, float& ps2, float& ps3) {
  float t0, t1, t2, t3, t4, t5, t6, t7;
  asm volatile("v_exp_f32 %4, v[%c12]\n\tv_exp_f32 %5, v[%c12+1]\n\tv_exp_f32 %6, v[%c12+2]\n\tv_exp_f32 %7, v[%c12+3]\n\t"
               "v_exp_f32 %8, v[%c12+4]\n\tv_exp_f32 %9, v[%c12+5]\n\tv_exp_f32 %10, v[%c12+6]\n\tv_exp_f32 %11, v[%c12+7]\n\t"
               "v_add_f32 %0, %0, %4\n\tv_add_f32 %1, %1, %5\n\tv_cvt_pk_bf16_f32 v[%c13], %4, %5\n\t"
               "v_add_f32 %2, %2, %6\n\tv_add_f32 %3, %3, %7\n\tv_cvt_pk_bf16_f32 v[%c13+1], %6, %7\n\t"
               "v_add_f32 %0, %0, %8\n\tv_add_f32 %1, %1, %9\n\tv_cvt_pk_bf16_f32 v[%c13+2], %8, %9\n\t"
               "v_add_f32 %2, %2, %10\n\tv_add_f32 %3, %3, %11\n\tv_cvt_pk_bf16_f32 v[%c13+3], %10, %11"
               : "+v"(ps0), "+v"(ps1), "+v"(ps2), "+v"(ps3), "=&v"(t0), "=&v"(t1), "=&v"(t2), "=&v"(t3), "=&v"(t4), "=&v"(t5),
                 "=&v"(t6), "=&v"(t7)
               : "i"(S), "i"(V_PF + 4 * P));
}
DI float exps32() {
  float ps0 = 0.f, ps1 = 0.f, ps2 = 0.f, ps3 = 0.f, r;
  exps8<V_S0, 0>(ps0, ps1, ps2, ps3); exps8<V_S0 + 8, 1>(ps0, ps1, ps2, ps3);
  exps8<V_S1, 2>(ps0, ps1, ps2, ps3); exps8<V_S1 + 8, 3>(ps0, ps1, ps2, ps3);
  asm volatile("v_add_f32 %1, %1, %2\n\tv_add_f32 %3, %3, %4\n\tv_add_f32 %0, %1, %3" : "=v"(r), "+v"(ps0), "+v"(ps1), "+v"(ps2), "+v"(ps3));
  return r;
}
DI float rowmax32() {
  float a, b;
  asm volatile("v_max3_f32 %0, v80, v81, v82\n\tv_max3_f32 %1, v83, v84, v85\n\tv_max3_f32 %0, %0, v86, v87\n\tv_max3_f32 %1, %1, v88, v89\n\t"
               "v_max3_f32 %0, %0, v90, v91\n\tv_max3_f32 %1, %1, v92, v93\n\tv_max3_f32 %0, %0, v94, v95\n\tv_max3_f32 %1, %1, v96, v97\n\t"
               "v_max3_f32 %0, %0, v98, v99\n\tv_max3_f32 %1, %1, v100, v101\n\tv_max3_f32 %0, %0, v102, v103\n\tv_max3_f32 %1, %1, v104, v105\n\t"
               "v_max3_f32 %0, %0, v106, v107\n\tv_max3_f32 %1, %1, v108, v109\n\tv_max3_f32 %0, %0, v110, v111\n\tv_max_f32 %0, %0, %1"
               : "=&v"(a), "=&v"(b));
  return a;
}
template <int R> DI void vreg_add(float t) { asm volatile("v_add_f32 v[%c1], v[%c1], %0" ::"v"(t), "i"(R)); }
template <int S, int... E> DI void mask_all_(float ninf, std::integer_sequence<int, E...>) { (vreg_add<S + E>(ninf), ...); }
template <int S, int... E> DI void mask_tri_(int dq, float ninf, std::integer_sequence<int, E...>) {
  (vreg_add<S + E>(((E & 3) + 8 * (E >> 2) > dq) ? ninf : 0.f), ...);
}
template <int I> DI float acc_rd() { float r; asm volatile("v_accvgpr_read_b32 %0, a[%c1]" : "=v"(r) : "i"(I)); return r; }
template <int I> DI void acc_wr(float v) { asm volatile("v_accvgpr_write_b32 a[%c1], %0" ::"v"(v), "i"(I)); }
template <int... E> DI void o_scale_(float f, std::integer_sequence<int, E...>) { (acc_wr<A_O + E>(fmul_s(acc_rd<A_O + E>(), f)), ...); }
template <int R> DI u32x4 acc_scale8(float f) {
  u32x4 o; float t0, t1, t2, t3, t4, t5, t6, t7;
  asm volatile("v_accvgpr_read_b32 %4, a[%c13]\n\tv_accvgpr_read_b32 %5, a[%c13+1]\n\tv_accvgpr_read_b32 %6, a[%c13+2]\n\t"
               "v_accvgpr_read_b32 %7, a[%c13+3]\n\tv_accvgpr_read_b32 %8, a[%c13+4]\n\tv_accvgpr_read_b32 %9, a[%c13+5]\n\t"
               "v_accvgpr_read_b32 %10, a[%c13+6]\n\tv_accvgpr_read_b32 %11, a[%c13+7]\n\t"
               "v_mul_f32 %4, %4, %12\n\tv_mul_f32 %5, %5, %12\n\tv_mul_f32 %6, %6, %12\n\tv_mul_f32 %7, %7, %12\n\t"
               "v_mul_f32 %8, %8, %12\n\tv_mul_f32 %9, %9, %12\n\tv_mul_f32 %10, %10, %12\n\tv_mul_f32 %11, %11, %12\n\t"
               "v_cvt_pk_bf16_f32 %0, %4, %5\n\tv_cvt_pk_bf16_f32 %1, %6, %7\n\tv_cvt_pk_bf16_f32 %2, %8, %9\n\tv_cvt_pk_bf16_f32 %3, %10, %11"
               : "=&v"(o[0]), "=&v"(o[1]), "=&v"(o[2]), "=&v"(o[3]), "=&v"(t0), "=&v"(t1), "=&v"(t2), "=&v"(t3), "=&v"(t4),
                 "=&v"(t5), "=&v"(t6), "=&v"(t7)
               : "v"(f), "i"(R));
  return o;
}
template <int R, int OFF> DI void acc_to_lds(unsigned addr) {
  asm volatile("ds_write_b128 %0, a[%c1:%c2] offset:%c3" ::"v"(addr), "i"(R), "i"(R + 3), "i"(OFF) : "memory");
}
template <int R> DI u32x4 acc_combine8f(const f32x4& w0, const f32x4& w1, float cw, float ca) {
  u32x4 o; float t0, t1, t2, t3, t4, t5, t6, t7;
  asm volatile("v_accvgpr_read_b32 %4, a[%c22]\n\tv_accvgpr_read_b32 %5, a[%c22+1]\n\tv_accvgpr_read_b32 %6, a[%c22+2]\n\t"
               "v_accvgpr_read_b32 %7, a[%c22+3]\n\tv_accvgpr_read_b32 %8, a[%c22+4]\n\tv_accvgpr_read_b32 %9, a[%c22+5]\n\t"
               "v_accvgpr_read_b32 %10, a[%c22+6]\n\tv_accvgpr_read_b32 %11, a[%c22+7]\n\t"
               "v_mul_f32 %4, %4, %21\n\tv_mul_f32 %5, %5, %21\n\tv_mul_f32 %6, %6, %21\n\tv_mul_f32 %7, %7, %21\n\t"
               "v_mul_f32 %8, %8, %21\n\tv_mul_f32 %9, %9, %21\n\tv_mul_f32 %10, %10, %21\n\tv_mul_f32 %11, %11, %21\n\t"
               "v_fma_f32 %4, %12, %20, %4\n\tv_fma_f32 %5, %13, %20, %5\n\tv_fma_f32 %6, %14, %20, %6\n\tv_fma_f32 %7, %15, %20, %7\n\t"
               "v_fma_f32 %8, %16, %20, %8\n\tv_fma_f32 %9, %17, %20, %9\n\tv_fma_f32 %10, %18, %20, %10\n\tv_fma_f32 %11, %19, %20, %11\n\t"
               "v_cvt_pk_bf16_f32 %0, %4, %5\n\tv_cvt_pk_bf16_f32 %1, %6, %7\n\tv_cvt_pk_bf16_f32 %2, %8, %9\n\tv_cvt_pk_bf16_f32 %3, %10, %11"
               : "=&v"(o[0]), "=&v"(o[1]), "=&v"(o[2]), "=&v"(o[3]), "=&v"(t0), "=&v"(t1), "=&v"(t2), "=&v"(t3), "=&v"(t4),
                 "=&v"(t5), "=&v"(t6), "=&v"(t7)
               : "v"(w0[0]), "v"(w0[1]), "v"(w0[2]), "v"(w0[3]), "v"(w1[0]), "v"(w1[1]), "v"(w1[2]), "v"(w1[3]), "v"(cw), "v"(ca),
                 "i"(R));
  return o;
}
DI void scores_add(float aone, float v) {
  asm volatile("s_nop 1\n\tv_mfma_f32_32x32x2_f32 v[80:95], %0, %1, v[80:95]\n\tv_mfma_f32_32x32x2_f32 v[96:111], %0, %1, v[96:111]" ::"v"(aone), "v"(v));
}
DI void negm_set(float aone, float v) { asm volatile("s_nop 1\n\tv_mfma_f32_32x32x2_f32 v[64:79], %0, %1, 0" ::"v"(aone), "v"(v)); }
#define ACC_FENCE() asm volatile("s_nop 15\n\ts_nop 3" ::: "memory")
#define BAR_A(WAITS) asm volatile("s_waitcnt " WAITS "\n\ts_barrier" ::: "memory", "v127", ACLOB)

template <int J> DI void frag_read(unsigned ka, unsigned va) {
  if constexpr (J < 16) ring_read<J % NRING, (J & 1) * 8192 + (J >> 1) * 1024>(ka);
  else { constexpr int i = J - 16; ring_read<J % NRING, (i >> 3) * 8192 + ((i & 3) * 2 + ((i >> 2) & 1)) * 1024>(va); }
}
#define PDX 4
constexpr int PD = PDX;

__global__ __launch_bounds__(512, 2) __attribute__((amdgpu_num_vgpr(64))) void k2_attn(const u16* __restrict__ XN, const u16* __restrict__ wq,
                                                  const u16* __restrict__ KF, const u16* __restrict__ VF,
                                                  u16* __restrict__ CF, float* __restrict__ LAM,
                                                  const u16* __restrict__ wo) {
  extern __shared__ __attribute__((aligned(16))) char smem[];
  LAS char* lds = (LAS char*)smem;
  const unsigned lds0 = (unsigned)(uintptr_t)lds;
  const int tid = threadIdx.x, lane = tid & 63;
  const int wid = RFL(tid >> 6);
  const int b = blockIdx.x, xcd = b & 7, idx = b >> 3;
  const int head = xcd * 4 + (idx >> 3), px = idx & 7;
  const int qbH = 15 - px, qbL = px;
  const int rr = wid & 3, c = wid >> 2;
  const int h = lane >> 5;
  const int g1 = (lane >> 4) & 1, q4 = (lane & 15) >> 2, p4 = lane & 3;
  const __amdgpu_buffer_rsrc_t rX = mk_rsrc(XN, SEQ * DM * 2);
  const __amdgpu_buffer_rsrc_t rW = mk_rsrc(wq, DM * NH * HD * 2);
  const __amdgpu_buffer_rsrc_t rK = mk_rsrc(KF, NH * SEQ * HD * 2);
  const __amdgpu_buffer_rsrc_t rV = mk_rsrc(VF, NH * SEQ * HD * 2);
  const unsigned vlin = lane * 16;
  const unsigned voffW = ((lane >> 2) & 7) * 8192 + (lane >> 5) * 64 + (lane & 3) * 16;
  const int p1 = 2 * (px + 1);
  auto kvoff = [&](int sg) -> unsigned {
    const int hidx = sg < p1 ? sg : 2 * sg - p1 + c;
    return (unsigned)((head * 64 + 2 * hidx) * 8192);
  };
  const int tbx = (wid < 4) ? (4 * qbH + wid) : (4 * qbL + wid - 4);
  auto q_load = [&](unsigned addr) {
    q_read<0, 0>(addr); q_read<1, 1024>(addr); q_read<2, 2048>(addr); q_read<3, 3072>(addr);
    q_read<4, 4096>(addr); q_read<5, 5120>(addr); q_read<6, 6144>(addr); q_read<7, 7168>(addr);
  };
  {
    auto q_piece = [&](int kt, int i) {
      const unsigned sb = lds0 + (unsigned)(kt % 3) * 49152;
      if (i < 2) dma1(rW, voffW, (unsigned)((kt * 64 + wid * 8) * 8192 + head * 256 + i * 128), sb + (wid * 2 + i) * 1024);
      else dma1(rX, vlin, (unsigned)((tbx * 40 + 4 * kt + (i - 2)) * 1024), sb + 16384 + wid * 4096 + (i - 2) * 1024);
    };
    auto kv_piece = [&](int i) {
      const unsigned part = wid * 2048 + i * 1024;
      dma1(rK, vlin, (unsigned)(head * 64 * 8192) + part, lds0 + L_KA0 + part);
    };
#pragma unroll
    for (int i = 0; i < 6; ++i) q_piece(0, i);
#pragma unroll
    for (int i = 0; i < 6; ++i) q_piece(1, i);
    f32x16 qa[2][2];
#pragma unroll
    for (int i = 0; i < 2; ++i)
#pragma unroll
      for (int n = 0; n < 2; ++n) qa[i][n] = f32x16{};
    const unsigned wbase = h * 2048 + c * 1024 + q4 * 64 + g1 * 32 + p4 * 8;
    const unsigned xbase = 16384 + rr * 4096 + lane * 16;
    bf16x8 wA[2], xA[2], wB[2], xB[2];
    auto rdq = [&](bf16x8 (&w)[2], bf16x8 (&x)[2], LAS const char* sb, int s) {
      w[0] = lds_tr2(sb + wbase + s * 4096, sb + wbase + s * 4096 + 256);
      x[0] = lds_b128(sb + xbase + s * 1024);
      w[1] = lds_tr2(sb + wbase + s * 4096 + 512, sb + wbase + s * 4096 + 768);
      x[1] = lds_b128(sb + xbase + 16384 + s * 1024);
    };
    auto mmq = [&](const bf16x8 (&w)[2], const bf16x8 (&x)[2]) {
      qa[0][0] = MFMA32(w[0], x[0], qa[0][0]);
      qa[1][0] = MFMA32(w[0], x[1], qa[1][0]);
      qa[0][1] = MFMA32(w[1], x[0], qa[0][1]);
      qa[1][1] = MFMA32(w[1], x[1], qa[1][1]);
    };
    for (int kt = 0; kt < 10; ++kt) {
      if (kt < 9) { WAIT_VL_BAR(6); } else { WAIT_VL_BAR(2); }
      LAS const char* sb = lds + (unsigned)(kt % 3) * 49152;
      auto piece = [&](int i) {
        if (kt < 8) q_piece(kt + 2, i);
        else if (kt == 8 && i < 2) kv_piece(i);
      };
      rdq(wA, xA, sb, 0);
      SBAR();
      if (kt > 0) mmq(wB, xB);
      piece(0); SBAR();
      rdq(wB, xB, sb, 1);
      SBAR();
      mmq(wA, xA);
      piece(1); piece(2); SBAR();
      rdq(wA, xA, sb, 2);
      SBAR();
      mmq(wB, xB);
      piece(3); piece(4); SBAR();
      rdq(wB, xB, sb, 3);
      SBAR();
      mmq(wA, xA);
      piece(5); SBAR();
    }
    mmq(wB, xB);
    WAIT_L_BAR();
#pragma unroll
    for (int i = 0; i < 2; ++i) {
      LAS char* dst = lds + (i == 0 ? L_STASH : 0u) + rr * 8192 + lane * 16;
#pragma unroll
      for (int n = 0; n < 2; ++n) {
        const f32x16 t = qa[i][n] * QSCALE;
#pragma unroll
        for (int sg = 0; sg < 2; ++sg) *(LAS u32x4*)(dst + (4 * c + 2 * n + sg) * 1024) = pack8(t, 8 * sg);
      }
    }
    WAIT_L_BAR();
    q_load(lds0 + (c ? 0u : L_STASH) + rr * 8192 + lane * 16);
    BAR_A("vmcnt(0) lgkmcnt(0)");
  }
  negm_set(0.f, 0.f);
  float mref = 0.f, lsum = 0.f, mfin = 0.f, lfin = 0.f;
  const unsigned kown0 = c ? L_KB0 : L_KA0, kown1 = c ? L_KB1 : L_KA1;
  const unsigned vown0 = c ? L_VB0 : L_VA0, vown1 = c ? L_VB1 : L_VA1;
  const unsigned lrd = lds0 + lane * 16;
  float* lamp = LAM + (size_t)b * 256 + rr * 64;
  f32x2 mlA = {0.f, 0.f};

  auto refix = [&](const bool first) __attribute__((always_inline)) {
    const float rm = swap_max(rowmax32());
    const float dl = first ? fmaxf(rm, NEG_BIG) : fmaxf(rm, 0.f);
    const float f = first ? 0.f : __builtin_amdgcn_exp2f(-dl);
    mref = first ? dl : mref + dl;
    lsum = first ? 0.f : lsum * f;
    int ln = lane;
    asm volatile("" : "+v"(ln));
    const float aone = ln < 32 ? 1.f : 0.f;
    scores_add(aone, -dl);
    negm_set(aone, -mref);
    ACC_FENCE();
    if (!first) { o_scale_(f, std::make_integer_sequence<int, 64>{}); ACC_FENCE(); }
  };
#define TRAIL() do { pv_mfma<0, 28 % NRING, 0, 3>(); pv_mfma<0, 29 % NRING, 1, 3>(); pv_mfma<0, 30 % NRING, 2, 3>(); pv_mfma<0, 31 % NRING, 3, 3>(); SBAR(); } while (0)
  auto splain = [&]() __attribute__((always_inline)) {
    __builtin_amdgcn_s_setprio(TRAILPRIO);
    TRAIL();
    if (TRAILPRIO != SPRIO) __builtin_amdgcn_s_setprio(SPRIO);
    float pst = exps32();
    if (__builtin_expect(__any(pst > P_LIMIT), 0)) { refix(false); pst = exps32(); }
    asm volatile("v_add_f32 %0, %0, %1" : "+v"(lsum) : "v"(pst));
    __builtin_amdgcn_s_setprio(MPRIO);
  };
  auto sgen = [&](const int tau) __attribute__((always_inline)) {
    __builtin_amdgcn_s_setprio(SPRIO);
    if (tau > 0) TRAIL();
    const bool lastL = c == 1 && tau == p1 - 1;
    const bool diag = tau == NSTEP - 1 || (c == 1 && (tau == p1 - 2 || tau == p1 - 1));
    const bool first = tau == 0 || (c == 1 && tau == p1);
    int ln = lane;
    asm volatile("" : "+v"(ln));
    if (diag) {
      const int hp = tau == NSTEP - 1 ? c : tau - (p1 - 2);
      float ninf = -INFINITY;
      asm volatile("" : "+v"(ninf));
      const int dq = (ln & 31) - 4 * (ln >> 5);
      if (2 * hp > rr) mask_all_<V_S0>(ninf, std::make_integer_sequence<int, 16>{});
      else if (2 * hp == rr) mask_tri_<V_S0>(dq, ninf, std::make_integer_sequence<int, 16>{});
      if (2 * hp + 1 > rr) mask_all_<V_S1>(ninf, std::make_integer_sequence<int, 16>{});
      else if (2 * hp + 1 == rr) mask_tri_<V_S1>(dq, ninf, std::make_integer_sequence<int, 16>{});
    }
    if (first && tau > 0) {
      ACC_FENCE();
      const float inv = 1.0f / lfin;
      u16* dstp = CF + ((size_t)(((4 * qbL + rr) * 32 + head) * 8) * 64 + ln) * 8;
      *(u32x4*)(dstp) = acc_scale8<A_O + 0>(inv);          *(u32x4*)(dstp + 512) = acc_scale8<A_O + 8>(inv);
      *(u32x4*)(dstp + 1024) = acc_scale8<A_O + 16>(inv);  *(u32x4*)(dstp + 1536) = acc_scale8<A_O + 24>(inv);
      *(u32x4*)(dstp + 2048) = acc_scale8<A_O + 32>(inv);  *(u32x4*)(dstp + 2560) = acc_scale8<A_O + 40>(inv);
      *(u32x4*)(dstp + 3072) = acc_scale8<A_O + 48>(inv);  *(u32x4*)(dstp + 3584) = acc_scale8<A_O + 56>(inv);
    }
    if (first) refix(true);
    float pst = exps32();
    if (!first && __any(pst > P_LIMIT)) { refix(false); pst = exps32(); }
    asm volatile("v_add_f32 %0, %0, %1" : "+v"(lsum) : "v"(pst));
    if (lastL || tau == NSTEP - 1) {
      mfin = mref; lfin = swap_add(lsum);
      if (lastL) {
        negm_set(0.f, 0.f);
        q_load(lds0 + L_STASH + rr * 8192 + ln * 16);
      } else {
        f32x2* lp = (f32x2*)lamp + (ln & 31);
        if (c == 0) { if (ln < 32) *lp = (f32x2){mfin, lfin}; }
        else mlA = *lp;
      }
    }
    __builtin_amdgcn_s_setprio(0);
  };
#define KVDMA(i) do { if ((i) == 0) dma1o<0>(rK, vlin, gk, lk); else if ((i) == 1) dma1o<1024>(rK, vlin, gk, lk); \
    else if ((i) == 2) dma1o<2048>(rK, vlin, gk, lk); else if ((i) == 3) dma1o<3072>(rK, vlin, gk, lk); \
    else if ((i) == 4) dma1o<0>(rV, vlin, gv, lv); else if ((i) == 5) dma1o<1024>(rV, vlin, gv, lv); \
    else if ((i) == 6) dma1o<2048>(rV, vlin, gv, lv); else dma1o<3072>(rV, vlin, gv, lv); } while (0)
#define MSTEP(J, FIRST, LAST, DMA, ZO) do { \
    constexpr int out_ = ((J) + PD - 1 < (LAST) ? (J) + PD - 1 : (LAST)) - (J);     \
    if constexpr ((J) < 2) qk_head<out_, (J) % NRING, 0, ((J) & 1) ? V_S1 : V_S0>(); \
    else if constexpr ((J) < 16) qk_mfma<out_, (J) % NRING, ((J) >> 1), ((J) & 1) ? V_S1 : V_S0>(); \
    else if constexpr ((J) < 20 && (ZO)) { if (zero_o) pv_zero<out_, (J) % NRING, (J) & 3, 0>(); else pv_mfma<out_, (J) % NRING, (J) & 3, 0>(); } \
    else pv_mfma<out_, (J) % NRING, (J) & 3, (((J) - 16) >> 2)>(); \
    if constexpr ((J) + PD <= (LAST)) frag_read<(J) + PD>(ka, va); \
    if constexpr ((DMA) && ((J) & 1) && (J) < 16) KVDMA((J) >> 1); \
    SBAR(); } while (0)
#define MSTEP4(J, FIRST, LAST, DMA, ZO) MSTEP(J, FIRST, LAST, DMA, ZO); MSTEP((J) + 1, FIRST, LAST, DMA, ZO); MSTEP((J) + 2, FIRST, LAST, DMA, ZO); MSTEP((J) + 3, FIRST, LAST, DMA, ZO)
#define KSLOT(t) (((t) < p1) ? (((t) & 1) ? L_KA1 : L_KA0) : (((t) & 1) ? kown1 : kown0))
#define VSLOT(t) (((t) < p1) ? (((t) & 1) ? L_VA1 : L_VA0) : (((t) & 1) ? vown1 : vown0))
#define STAGE_ADDR(t) \
      const unsigned gk = ((t) + 1 < NSTEP && (c == 0 || (t) + 1 >= p1)) ? kvoff((t) + 1) + rr * 4096 : 0x7ff00000u; \
      const unsigned gv = (c == 0 || (t) >= p1) ? kvoff(t) + rr * 4096 : 0x7ff00000u; \
      const unsigned lk = lds0 + ((((t) + 1) & 1) ? kown1 : kown0) + rr * 4096; \
      const unsigned lv = lds0 + (((t) & 1) ? vown1 : vown0) + rr * 4096;

  if (c == 1) asm volatile("s_barrier" ::: "memory");
  {
    const unsigned ka = lrd + L_KA0, va = 0;
    STAGE_ADDR(0)
    constexpr bool zero_o = false;
    frag_read<0>(ka, va); frag_read<1>(ka, va); frag_read<2>(ka, va); frag_read<3>(ka, va);
    SBAR();
    MSTEP4(0, 0, 15, true, false); MSTEP4(4, 0, 15, true, false); MSTEP4(8, 0, 15, true, false); MSTEP4(12, 0, 15, true, false);
    BAR_A("lgkmcnt(0)");
    ACC_FENCE();
    sgen(0);
    BAR_A("vmcnt(0) lgkmcnt(0)");
  }
#define MPHASE(tau, DMA, ZO) do { \
      const bool zero_o = (tau) == 1 || (c == 1 && (tau) == p1 + 1);     \
      (void)zero_o; \
      const unsigned ka = lrd + KSLOT(tau); \
      const unsigned va = lrd + VSLOT((tau) - 1); \
      STAGE_ADDR(tau) \
      (void)gk; (void)gv; (void)lk; (void)lv; \
      frag_read<0>(ka, va); frag_read<1>(ka, va); frag_read<2>(ka, va); frag_read<3>(ka, va); \
      SBAR(); \
      MSTEP4(0, 0, 31, DMA, ZO); MSTEP4(4, 0, 31, DMA, ZO); MSTEP4(8, 0, 31, DMA, ZO); MSTEP4(12, 0, 31, DMA, ZO); \
      MSTEP4(16, 0, 31, DMA, ZO); MSTEP4(20, 0, 31, DMA, ZO); MSTEP4(24, 0, 31, DMA, ZO); \
      BAR_A("lgkmcnt(0)"); } while (0)
  int tau = 1;
  while (tau < NSTEP) {
    const bool special = tau == 1 || tau == NSTEP - 1 || (c == 1 && tau >= p1 - 2 && tau <= p1 + 1);
    if (special) {
      MPHASE(tau, true, true);
      sgen(tau);
      BAR_A("vmcnt(0) lgkmcnt(0)");
      ++tau;
    } else if (c == 1 && tau < p1 - 2) {
      do {
        MPHASE(tau, false, false);
        splain();
        BAR_A("lgkmcnt(0)");
        ++tau;
      } while (tau < p1 - 2);
    } else {
      do {
        MPHASE(tau, true, false);
        splain();
        BAR_A("vmcnt(0) lgkmcnt(0)");
        ++tau;
      } while (tau < NSTEP - 1);
    }
  }
#undef MPHASE
#undef TRAIL
  {
    const unsigned v128 = lane * 128;
    const unsigned dead = lds0 + vown1 + rr * 256;
    const int u = idx * 8 + wid;
    if (u < 160) warm8k(mk_rsrc(wo, NH * HD * DM * 2), v128, (unsigned)((xcd >> 1) * 1024 * 1280 + u * 8192), dead);
  }
  {
    const unsigned ka = 0, va = lrd + VSLOT(NSTEP - 1);
    const unsigned gk = 0, gv = 0, lk = 0, lv = 0;
    (void)gk; (void)gv; (void)lk; (void)lv; (void)ka;
    const bool zero_o = c == 1 && p1 == NSTEP - 1;
    frag_read<16>(ka, va); frag_read<17>(ka, va); frag_read<18>(ka, va); frag_read<19>(ka, va);
    SBAR();
    MSTEP4(16, 16, 31, false, true); MSTEP4(20, 16, 31, false, true); MSTEP4(24, 16, 31, false, true); MSTEP4(28, 16, 31, false, true);
    BAR_A("lgkmcnt(0)");
    int ln = lane;
    asm volatile("" : "+v"(ln));
    const unsigned mine = lds0 + (c ? L_KB1 : L_KA0) + ((rr & 2) ? (c ? L_KB0 - L_KB1 : L_KA1 - L_KA0) : 0u) + (rr & 1) * 8192 + ln * 16;
    const unsigned theirs = (c ? L_KA0 : L_KB1) + ((rr & 2) ? (c ? L_KA1 - L_KA0 : L_KB0 - L_KB1) : 0u) + (rr & 1) * 8192 + ln * 16;
    ACC_FENCE();
    if (c == 0) {
      acc_to_lds<A_O + 32, 0>(mine); acc_to_lds<A_O + 36, 1024>(mine); acc_to_lds<A_O + 40, 2048>(mine); acc_to_lds<A_O + 44, 3072>(mine);
      acc_to_lds<A_O + 48, 4096>(mine); acc_to_lds<A_O + 52, 5120>(mine); acc_to_lds<A_O + 56, 6144>(mine); acc_to_lds<A_O + 60, 7168>(mine);
      BAR_A("lgkmcnt(0)");
    } else {
      acc_to_lds<A_O + 0, 0>(mine); acc_to_lds<A_O + 4, 1024>(mine); acc_to_lds<A_O + 8, 2048>(mine); acc_to_lds<A_O + 12, 3072>(mine);
      acc_to_lds<A_O + 16, 4096>(mine); acc_to_lds<A_O + 20, 5120>(mine); acc_to_lds<A_O + 24, 6144>(mine); acc_to_lds<A_O + 28, 7168>(mine);
      if (ln < 32) *(LAS f32x2*)(lds + L_STASH + rr * 256 + ln * 8) = (f32x2){mfin, lfin};
    }
    BAR_A("lgkmcnt(0)");
    {
      const f32x2 mo = c ? mlA : *(LAS const f32x2*)(lds + L_STASH + rr * 256 + (ln & 31) * 8);
      const float m = fmaxf(mo[0], mfin);
      const float eo = __builtin_amdgcn_exp2f(mo[0] - m), em = __builtin_amdgcn_exp2f(mfin - m);
      const float inv = 1.0f / (mo[1] * eo + lfin * em);
      const float co = eo * inv, cm = em * inv;
      f32x4 w[8];
#pragma unroll
      for (int i = 0; i < 8; ++i) w[i] = *(LAS const f32x4*)(lds + theirs + i * 1024);
      u16* dstp = CF + ((size_t)(((4 * qbH + rr) * 32 + head) * 8 + 4 * c) * 64 + ln) * 8;
      if (c == 0) {
        *(u32x4*)(dstp) = acc_combine8f<A_O + 0>(w[0], w[1], co, cm);         *(u32x4*)(dstp + 512) = acc_combine8f<A_O + 8>(w[2], w[3], co, cm);
        *(u32x4*)(dstp + 1024) = acc_combine8f<A_O + 16>(w[4], w[5], co, cm); *(u32x4*)(dstp + 1536) = acc_combine8f<A_O + 24>(w[6], w[7], co, cm);
      } else {
        *(u32x4*)(dstp) = acc_combine8f<A_O + 32>(w[0], w[1], co, cm);        *(u32x4*)(dstp + 512) = acc_combine8f<A_O + 40>(w[2], w[3], co, cm);
        *(u32x4*)(dstp + 1024) = acc_combine8f<A_O + 48>(w[4], w[5], co, cm); *(u32x4*)(dstp + 1536) = acc_combine8f<A_O + 56>(w[6], w[7], co, cm);
      }
    }
  }
#undef KSLOT
#undef VSLOT
#undef STAGE_ADDR
#undef KVDMA
#undef MSTEP
#undef MSTEP4
}

constexpr int K3_STAGE = 37888, K3_NST = 4, K3_LDS = K3_STAGE * K3_NST;

__global__ __launch_bounds__(512, 2) void k3_oproj(const u16* __restrict__ CF, const u16* __restrict__ wo,
                                                   u16* __restrict__ PT) {
  extern __shared__ __attribute__((aligned(16))) char smem[];
  LAS char* lds = (LAS char*)smem;
  const unsigned lds0 = (unsigned)(uintptr_t)lds;
  const int tid = threadIdx.x, lane = tid & 63;
  const int wid = RFL(tid >> 6);
  const int b = blockIdx.x, xcd = b & 7, idx = b >> 3;
  const int ks = xcd >> 1, panel = (xcd & 1) * 8 + (idx >> 2), nt = idx & 3;
  const int tbi = wid & 3, kp = wid >> 2;
  const int h = lane >> 5, g1 = (lane >> 4) & 1, q4 = (lane & 15) >> 2, p4 = lane & 3;
  const __amdgpu_buffer_rsrc_t rC = mk_rsrc(CF, NH * SEQ * HD * 2);
  const __amdgpu_buffer_rsrc_t rW = mk_rsrc(wo, NH * HD * DM * 2);
  const unsigned vlin = lane * 16;
  unsigned voffW[3];
#pragma unroll
  for (int i = 0; i < 3; ++i) {
    const int pc = wid + 8 * i;
    const int u = 2 * pc + (lane >> 5);
    const int kb = u / 5, nb = u % 5;
    voffW[i] = (unsigned)((kb * 8 + ((lane >> 2) & 7)) * 1280 + (nb * 32 + (lane & 3) * 8) * 2);
  }
  const bool lo = wid < 4;
  const unsigned sW0 = (unsigned)(ks * 1024 * 1280 + nt * 320);
  const unsigned sC0 = (unsigned)(((panel * 4 + tbi) * 256 + ks * 64) * 1024);
  const __amdgpu_buffer_rsrc_t r2 = lo ? rW : rC;
  const unsigned v2 = lo ? voffW[2] : vlin;
  const unsigned s2 = lo ? sW0 : sC0, st2 = lo ? 81920u : 4096u, l2 = lo ? (wid + 16) * 1024 : 20480 + tbi * 4096;
  const unsigned s3 = sC0 + (lo ? 3072u : 1024u), l3 = 20480 + tbi * 4096 + (lo ? 3072 : 1024);
  const unsigned s4 = lo ? 0x7ff00000u : sC0 + 2048u, l4 = lo ? 36864 : 20480 + tbi * 4096 + 2048;
  auto piece = [&](int j, int slot, int i) __attribute__((always_inline)) {
    const unsigned sb = lds0 + slot * K3_STAGE;
    if (i == 0) dma1(rW, voffW[0], sW0 + j * 81920, sb + wid * 1024);
    else if (i == 1) dma1(rW, voffW[1], sW0 + j * 81920, sb + (wid + 8) * 1024);
    else if (i == 2) dma1(r2, v2, s2 + j * st2, sb + l2);
    else if (i == 3) dma1(rC, vlin, s3 + j * 4096, sb + l3);
    else dma1(rC, vlin, s4 + j * 4096, sb + l4);
  };
  f32x16 acc[5];
#pragma unroll
  for (int n = 0; n < 5; ++n) acc[n] = f32x16{};
  const unsigned wbase = h * 256 + q4 * 64 + g1 * 32 + p4 * 8 + kp * 4 * 2560;
  const unsigned cbase = 20480 + tbi * 4096 + lane * 16 + kp * 2048;
  auto rd = [&](bf16x8& cf, bf16x8 (&w)[5], LAS const char* sb, int si) {
    cf = lds_b128(sb + cbase + si * 1024);
#pragma unroll
    for (int n = 0; n < 5; ++n)
      w[n] = lds_tr2(sb + wbase + (2 * si) * 2560 + n * 512, sb + wbase + (2 * si + 1) * 2560 + n * 512);
  };
  bf16x8 cA, cB, wA[5], wB[5];
#pragma unroll
  for (int j = 0; j < 3; ++j)
#pragma unroll
    for (int i = 0; i < 5; ++i) piece(j, j, i);
  auto stage = [&](const int j, const int slot, const bool more, const bool first) __attribute__((always_inline)) {
    LAS const char* sb = lds + slot * K3_STAGE;
    rd(cA, wA, sb, 0);
    SBAR();
    if (!first) {
      acc[0] = MFMA32(wB[0], cB, acc[0]); acc[1] = MFMA32(wB[1], cB, acc[1]);
      if (more) piece(j + 3, (slot + 3) & 3, 0);
      SBAR();
      acc[2] = MFMA32(wB[2], cB, acc[2]); acc[3] = MFMA32(wB[3], cB, acc[3]);
      if (more) piece(j + 3, (slot + 3) & 3, 1);
      SBAR();
      acc[4] = MFMA32(wB[4], cB, acc[4]);
    } else {
      piece(3, 3, 0); piece(3, 3, 1);
    }
    SBAR();
    rd(cB, wB, sb, 1);
    SBAR();
    acc[0] = MFMA32(wA[0], cA, acc[0]);
    if (more) piece(j + 3, (slot + 3) & 3, 2);
    SBAR();
    acc[1] = MFMA32(wA[1], cA, acc[1]); acc[2] = MFMA32(wA[2], cA, acc[2]);
    if (more) piece(j + 3, (slot + 3) & 3, 3);
    SBAR();
    acc[3] = MFMA32(wA[3], cA, acc[3]); acc[4] = MFMA32(wA[4], cA, acc[4]);
    if (more) piece(j + 3, (slot + 3) & 3, 4);
    SBAR();
  };
  WAIT_VL_BAR(10); stage(0, 0, true, true);
  for (int j4 = 1; j4 < 13; j4 += 4) {
    WAIT_VL_BAR(10); stage(j4, 1, true, false);
    WAIT_VL_BAR(10); stage(j4 + 1, 2, true, false);
    WAIT_VL_BAR(10); stage(j4 + 2, 3, true, false);
    WAIT_VL_BAR(10); stage(j4 + 3, 0, true, false);
  }
  WAIT_VL_BAR(10); stage(13, 1, false, false);
  WAIT_VL_BAR(5); stage(14, 2, false, false);
  WAIT_VL_BAR(0); stage(15, 3, false, false);
#pragma unroll
  for (int n = 0; n < 5; ++n) acc[n] = MFMA32(wB[n], cB, acc[n]);
  WAIT_L_BAR();
  {
    LAS char* mine = lds + (tbi * 2 + kp) * 12288 + lane * 16;
    LAS const char* theirs = lds + (tbi * 2 + (kp ^ 1)) * 12288 + lane * 16;
    auto put = [&](const f32x16& a, int slot) {
#pragma unroll
      for (int gq = 0; gq < 4; ++gq)
        *(LAS f32x4*)(mine + (slot * 4 + gq) * 1024) = (f32x4){a[4 * gq], a[4 * gq + 1], a[4 * gq + 2], a[4 * gq + 3]};
    };
    auto fin = [&](const f32x16& a, int slot, int n) {
      f32x16 t;
#pragma unroll
      for (int gq = 0; gq < 4; ++gq) {
        const f32x4 v = *(LAS const f32x4*)(theirs + (slot * 4 + gq) * 1024);
#pragma unroll
        for (int e = 0; e < 4; ++e) t[4 * gq + e] = a[4 * gq + e] + v[e];
      }
      u16* dst = PT + ((size_t)((((ks * 64 + panel * 4 + tbi) * 20 + nt * 5 + n) * 2) * 64 + lane)) * 8;
      *(u32x4*)dst = pack8(t, 0);
      *(u32x4*)(dst + 512) = pack8(t, 8);
    };
    if (kp == 0) { put(acc[3], 0); put(acc[4], 1); } else { put(acc[0], 0); put(acc[1], 1); put(acc[2], 2); }
    WAIT_L_BAR();
    if (kp == 0) { fin(acc[0], 0, 0); fin(acc[1], 1, 1); fin(acc[2], 2, 2); } else { fin(acc[3], 0, 3); fin(acc[4], 1, 4); }
  }
}

__global__ __launch_bounds__(320) void k4_ln(const u16* __restrict__ x, const u16* __restrict__ PT,
                                             float* __restrict__ H, u16* __restrict__ HN, const u16* __restrict__ w1) {
  __shared__ __attribute__((aligned(16))) u16 tile[8 * 648];
  __shared__ float red[2][5][8];
  __shared__ __attribute__((aligned(16))) char dead[5 * 256];
  const int tid = threadIdx.x, lane = tid & 63, wid = RFL(tid >> 6);
  const int b = blockIdx.x;
  const int r8 = lane & 7, hh = (lane >> 3) & 1, sg = (lane >> 4) & 1, ml = lane >> 5;
  const int token = b * 8 + r8, tb32 = token >> 5, rr = token & 31;
  u32x2 xv[2][2];
  u32x4 pv[2][4];
#pragma unroll
  for (int i = 0; i < 2; ++i) {
    const int mb = wid * 4 + 2 * i + ml;
    const int m0 = mb * 32 + sg * 16 + hh * 4;
    xv[i][0] = *(const u32x2*)(x + (size_t)token * DM + m0);
    xv[i][1] = *(const u32x2*)(x + (size_t)token * DM + m0 + 8);
#pragma unroll
    for (int ks = 0; ks < 4; ++ks)
      pv[i][ks] = *(const u32x4*)(PT + ((size_t)((((ks * 64 + tb32) * 20 + mb) * 2 + sg) * 64 + hh * 32 + rr)) * 8);
  }
  SBAR();
  float v[2][8];
  float s = 0.f;
#pragma unroll
  for (int i = 0; i < 2; ++i) {
    const int m0 = (wid * 4 + 2 * i + ml) * 32 + sg * 16 + hh * 4;
#pragma unroll
    for (int q = 0; q < 4; ++q) {
      const unsigned xw = xv[i][q >> 1][q & 1];
      v[i][2 * q] = bflo(xw) + ((bflo(pv[i][0][q]) + bflo(pv[i][1][q])) + (bflo(pv[i][2][q]) + bflo(pv[i][3][q])));
      v[i][2 * q + 1] = bfhi(xw) + ((bfhi(pv[i][0][q]) + bfhi(pv[i][1][q])) + (bfhi(pv[i][2][q]) + bfhi(pv[i][3][q])));
    }
    *(f32x4*)(H + (size_t)token * DM + m0) = (f32x4){v[i][0], v[i][1], v[i][2], v[i][3]};
    *(f32x4*)(H + (size_t)token * DM + m0 + 8) = (f32x4){v[i][4], v[i][5], v[i][6], v[i][7]};
#pragma unroll
    for (int e = 0; e < 8; ++e) s += v[i][e];
  }
  {
    const int u = (b >> 3) * 5 + wid;
    const int a4 = (b & 7) >> 1;
    const unsigned dd = (unsigned)(uintptr_t)(LAS char*)dead + wid * 256;
    const __amdgpu_buffer_rsrc_t r1 = mk_rsrc(w1, SEQ * FF * 2);
#pragma unroll
    for (int i = 0; i < 2; ++i) {
      const int line = (u + 160 * i) * 64 + lane;
      const unsigned off = line < 14336 ? (unsigned)((line / 7) * (FF * 2) + a4 * 896 + (line % 7) * 128) : 0x7ff00000u;
      warm8k(r1, off, 0u, dd);
    }
  }
  s += __shfl_xor(s, 8); s += __shfl_xor(s, 16); s += __shfl_xor(s, 32);
  if (lane < 8) red[0][wid][lane] = s;
  WAIT_L_BAR();
  const float mean = ((red[0][0][r8] + red[0][1][r8]) + (red[0][2][r8] + red[0][3][r8]) + red[0][4][r8]) * (1.0f / DM);
  float sq = 0.f;
#pragma unroll
  for (int i = 0; i < 2; ++i)
#pragma unroll
    for (int e = 0; e < 8; ++e) { v[i][e] -= mean; sq += v[i][e] * v[i][e]; }
  sq += __shfl_xor(sq, 8); sq += __shfl_xor(sq, 16); sq += __shfl_xor(sq, 32);
  if (lane < 8) red[1][wid][lane] = sq;
  WAIT_L_BAR();
  const float var = ((red[1][0][r8] + red[1][1][r8]) + (red[1][2][r8] + red[1][3][r8]) + red[1][4][r8]) * (1.0f / DM);
  const float r1 = 1.0f / sqrtf(var + LN_EPS);
  const float r2 = 1.0f / sqrtf(var * r1 * r1 + LN_EPS);
  const float sc = r1 * r2;
#pragma unroll
  for (int i = 0; i < 2; ++i) {
    const int m0 = (wid * 4 + 2 * i + ml) * 32 + sg * 16 + hh * 4;
    *(u32x2*)(tile + r8 * 648 + m0) = (u32x2){cvtpk(v[i][0] * sc, v[i][1] * sc), cvtpk(v[i][2] * sc, v[i][3] * sc)};
    *(u32x2*)(tile + r8 * 648 + m0 + 8) = (u32x2){cvtpk(v[i][4] * sc, v[i][5] * sc), cvtpk(v[i][6] * sc, v[i][7] * sc)};
  }
  WAIT_L_BAR();
  const int ks32 = b >> 2, g = b & 3;
#pragma unroll
  for (int i = 0; i < 2; ++i) {
    const int m = tid + 320 * i;
    u16 e[8];
#pragma unroll
    for (int j = 0; j < 8; ++j) e[j] = tile[j * 648 + m];
    u32x4 w = {(unsigned)e[0] | ((unsigned)e[1] << 16), (unsigned)e[2] | ((unsigned)e[3] << 16),
               (unsigned)e[4] | ((unsigned)e[5] << 16), (unsigned)e[6] | ((unsigned)e[7] << 16)};
    *(u32x4*)(HN + ((size_t)(((m >> 4) * 64 + ks32) * 64 + g * 16 + (m & 15))) * 8) = w;
  }
}

constexpr int K56_LDS = 163840;

DI float gelu_tanh(float x) {
  const float u = 0.7978845608028654f * (x + 0.044715f * x * x * x);
  const float e = __builtin_amdgcn_exp2f(u * 2.8853900817779268f);
  const float t = 1.0f - 2.0f * __builtin_amdgcn_rcpf(e + 1.0f);
  return 0.5f * x * (1.0f + t);
}

#define ACLOB56 "a0","a1","a2","a3","a4","a5","a6","a7","a8","a9","a10","a11","a12","a13","a14","a15","a16","a17","a18","a19","a20","a21","a22","a23","a24","a25","a26","a27","a28","a29","a30","a31","a32","a33","a34","a35","a36","a37","a38","a39","a40","a41","a42","a43","a44","a45","a46","a47","a48","a49","a50","a51","a52","a53","a54","a55","a56","a57","a58","a59","a60","a61","a62","a63","a64","a65","a66","a67","a68","a69","a70","a71","a72","a73","a74","a75","a76","a77","a78","a79","a80","a81","a82","a83","a84","a85","a86","a87","a88","a89","a90","a91"
template <int R> DI void m_aload(__amdgpu_buffer_rsrc_t rsrc, unsigned voff, unsigned soff) {
  asm volatile("buffer_load_dwordx4 a[%c3:%c4], %0, %1, %2 offen" ::"v"(voff), "s"(rsrc), "s"(soff), "i"(R), "i"(R + 3) : "memory");
}
template <int R, int OFF> DI void m_trread(unsigned addr) {
  asm volatile("ds_read_b64_tr_b16 a[%c1:%c2], %0 offset:%c3" ::"v"(addr), "i"(R), "i"(R + 1), "i"(OFF) : "memory");
}
template <int RA, int RB> DI void m_mfma(f32x4& d) {
  asm volatile("v_mfma_f32_16x16x32_bf16 %0, a[%c1:%c2], a[%c3:%c4], %0" : "+v"(d) : "i"(RA), "i"(RA + 3), "i"(RB), "i"(RB + 3));
}

template <int MODE>
__global__ __launch_bounds__(512, 2) void k56_mix(const u16* __restrict__ W, const u16* __restrict__ AF,
                                                  const u16* __restrict__ bias, const float* __restrict__ H,
                                                  u16* __restrict__ outp, const u16* __restrict__ wnext) {
  constexpr int LDW = MODE == 0 ? FF : SEQ;
  constexpr int NKS = MODE == 0 ? 64 : 54;
  constexpr int NK = MODE == 0 ? 8 : 7;
  constexpr int KROWS = MODE == 0 ? SEQ : FF;
  extern __shared__ __attribute__((aligned(16))) char smem[];
  LAS char* lds = (LAS char*)smem;
  const unsigned lds0 = (unsigned)(uintptr_t)lds;
  const int tid = threadIdx.x, lane = tid & 63;
  const int wid = RFL(tid >> 6);
  const int b = blockIdx.x, xcd = b & 7, idx = b >> 3;
  int wt, dt;
  if (MODE == 0) {
    wt = (xcd >> 1) * 7 + (idx >> 2); dt = (xcd & 1) * 4 + (idx & 3);
    if (idx >= 28 || wt >= 27) {
      if (idx >= 28) {
        const __amdgpu_buffer_rsrc_t r2 = mk_rsrc(wnext, SEQ * FF * 2);
#pragma unroll
        for (int i = 0; i < 7; ++i) {
          const int line = (((idx - 28) * 8 + wid) * 7 + i) * 64 + lane;
          const unsigned off = line < 13824 ? (unsigned)((line >> 3) * 4096 + (xcd >> 1) * 1024 + (line & 7) * 128) : 0x7ff00000u;
          warm8k(r2, off, 0u, lds0 + wid * 256);
        }
      }
      return;
    }
  } else {
    wt = (xcd >> 1) * 8 + (idx >> 2); dt = (xcd & 1) * 4 + (idx & 3);
  }
  const __amdgpu_buffer_rsrc_t rW = mk_rsrc(W, KROWS * LDW * 2);
  const __amdgpu_buffer_rsrc_t rA = mk_rsrc(AF, 40 * NKS * 1024);
  const unsigned vlin = lane * 16;
  const int prow = (lane >> 2) & 7;
  const unsigned voffW0 = (unsigned)(prow * LDW * 2 + (lane >> 5) * 64 +
                                     (((lane & 3) ^ (MODE == 1 ? (((prow >> 2) & 1) << 1) : 0)) << 4));
  const unsigned voffW1 = MODE == 1 ? voffW0 : (voffW0 ^ 32u);
  const unsigned myl = lds0 + wid * 20480;
  const int g = lane >> 4, q4 = (lane & 15) >> 2, p4 = lane & 3;
  unsigned wb[2][2];
#pragma unroll
  for (int t = 0; t < 2; ++t)
#pragma unroll
    for (int hf = 0; hf < 2; ++hf) {
      const int kb = MODE == 0 ? g : 2 * t + (g >> 1);
      const int row = MODE == 0 ? 4 * t + q4 : 4 * (g & 1) + q4;
      const int sw = MODE == 0 ? (kb & 1) : ((row >> 2) & 1);
      wb[t][hf] = myl + kb * 1024 + row * 64 + ((hf ^ sw) * 32) + p4 * 8;
    }
  f32x4 acc[4][5];
#pragma unroll
  for (int i = 0; i < 4; ++i)
#pragma unroll
    for (int d = 0; d < 5; ++d) acc[i][d] = (f32x4){0.f, 0.f, 0.f, 0.f};
  u32x2 eb0[2], eb1[2];
  f32x4 eh[3]; u16 ebs[3];
#define KSTEP(IT) (wid + 8 * (IT))
#define WDMA(IT, KB) dma1(rW, ((KB) & 1) ? voffW1 : voffW0, (unsigned)((KSTEP(IT) * 32 + (KB) * 8) * LDW * 2 + wt * 128), myl + ((IT) % 5) * 4096 + (KB) * 1024)
#define ALOAD(IT, D) m_aload<((IT) % 3) * 20 + 4 * (D)>(rA, vlin, (unsigned)(((dt * 5 + (D)) * NKS + KSTEP(IT)) * 1024))
#define WDMA4(IT) do { WDMA(IT, 0); WDMA(IT, 1); WDMA(IT, 2); WDMA(IT, 3); } while (0)
#define ALOAD5(IT) do { ALOAD(IT, 0); ALOAD(IT, 1); ALOAD(IT, 2); ALOAD(IT, 3); ALOAD(IT, 4); } while (0)
#define WREAD1(IT, I) do { m_trread<60 + 16 * ((IT) & 1) + 4 * (I), ((IT) % 5) * 4096 + ((I) >> 1) * 512>(wb[0][(I) & 1]); \
                           m_trread<60 + 16 * ((IT) & 1) + 4 * (I) + 2, ((IT) % 5) * 4096 + ((I) >> 1) * 512>(wb[1][(I) & 1]); } while (0)
#define WREAD(IT) do { WREAD1(IT, 0); WREAD1(IT, 1); WREAD1(IT, 2); WREAD1(IT, 3); } while (0)
#define MM(IT, I, D) do { if constexpr (MODE == 0) m_mfma<60 + 16 * ((IT) & 1) + 4 * (I), ((IT) % 3) * 20 + 4 * (D)>(acc[I][D]); \
                          else m_mfma<((IT) % 3) * 20 + 4 * (D), 60 + 16 * ((IT) & 1) + 4 * (I)>(acc[I][D]); } while (0)
#define MMROW(IT, I) do { MM(IT, I, 0); MM(IT, I, 1); MM(IT, I, 2); MM(IT, I, 3); MM(IT, I, 4); } while (0)
#define NV(IT) (((IT) + 4 < NK ? 4 : 0) + ((IT) + 2 < NK ? 5 : 0))
#define ALLOW(IT) ((IT) == 0 ? (5 + (NK > 2 ? 4 : 0) + (NK > 3 ? 4 : 0)) : (IT) == 1 ? ((NK > 3 ? 4 : 0) + NV(0)) : NV((IT) - 1))
#define STEP(IT) do { if constexpr ((IT) < NK) { \
    asm volatile("s_waitcnt vmcnt(%c0) lgkmcnt(0)" ::"i"(ALLOW(IT)) : "memory", ACLOB56); \
    if constexpr ((IT) + 1 < NK) WREAD((IT) + 1); \
    SBAR(); \
    MMROW(IT, 0); if constexpr ((IT) + 4 < NK) { WDMA((IT) + 4, 0); WDMA((IT) + 4, 1); } SBAR(); \
    MMROW(IT, 1); if constexpr ((IT) + 4 < NK) { WDMA((IT) + 4, 2); WDMA((IT) + 4, 3); } SBAR(); \
    MMROW(IT, 2); if constexpr ((IT) + 2 < NK) { ALOAD((IT) + 2, 0); ALOAD((IT) + 2, 1); ALOAD((IT) + 2, 2); } SBAR(); \
    MMROW(IT, 3); if constexpr ((IT) + 2 < NK) { ALOAD((IT) + 2, 3); ALOAD((IT) + 2, 4); } SBAR(); \
    } } while (0)
  asm volatile("" ::: "memory", ACLOB56);
  WDMA4(0); ALOAD5(0); asm volatile("s_barrier" ::: "memory");
  WDMA4(1); ALOAD5(1); asm volatile("s_barrier" ::: "memory");
  WDMA4(2); WDMA4(3);
  asm volatile("s_waitcnt vmcnt(22)" ::: "memory");
  WREAD(0);
  STEP(0); STEP(1); STEP(2); STEP(3); STEP(4);
  if (MODE == 0) {
#pragma unroll
    for (int i = 0; i < 2; ++i) {
      const int u = wid + 8 * i < 10 ? wid + 8 * i : wid;
      const int f0 = wt * 64 + (u / 5) * 32 + 4 * g;
      eb0[i] = *(const u32x2*)(bias + f0); eb1[i] = *(const u32x2*)(bias + f0 + 16);
    }
  } else {
#pragma unroll
    for (int i = 0; i < 3; ++i) {
      const int u = wid + 8 * i < 20 ? wid + 8 * i : wid;
      const int s = wt * 64 + (u / 5) * 16 + (lane & 15), d0 = dt * 80 + (u % 5) * 16 + 4 * g;
      ebs[i] = bias[s];
      eh[i] = *(const f32x4*)(H + (size_t)s * DM + d0);
    }
  }
  SBAR();
  STEP(5); STEP(6); STEP(7);
  asm volatile("s_nop 15" ::: "memory");
#undef KSTEP
#undef WDMA
#undef ALOAD
#undef WDMA4
#undef ALOAD5
#undef WREAD1
#undef WREAD
#undef MM
#undef MMROW
#undef NV
#undef ALLOW
#undef STEP
  WAIT_L0();
  {
    LAS char* dst = lds + wid * 20480 + lane * 16;
#pragma unroll
    for (int i = 0; i < 4; ++i)
#pragma unroll
      for (int d = 0; d < 5; ++d) *(LAS f32x4*)(dst + (i * 5 + d) * 1024) = acc[i][d];
  }
  WAIT_L_BAR();
  if (MODE == 0) {
#pragma unroll
    for (int i = 0; i < 2; ++i) {
      const int u = wid + 8 * i;
      if (u < 10) {
        const int a = u / 5, d = u % 5;
        f32x4 t0 = {0.f, 0.f, 0.f, 0.f}, t1 = {0.f, 0.f, 0.f, 0.f};
#pragma unroll
        for (int w = 0; w < 8; ++w) {
          t0 += *(LAS const f32x4*)(lds + w * 20480 + ((2 * a) * 5 + d) * 1024 + lane * 16);
          t1 += *(LAS const f32x4*)(lds + w * 20480 + ((2 * a + 1) * 5 + d) * 1024 + lane * 16);
        }
        const u32x2 b0 = eb0[i], b1 = eb1[i];
        t0 += (f32x4){bflo(b0[0]), bfhi(b0[0]), bflo(b0[1]), bfhi(b0[1])};
        t1 += (f32x4){bflo(b1[0]), bfhi(b1[0]), bflo(b1[1]), bfhi(b1[1])};
        u32x4 w = {cvtpk(gelu_tanh(t0[0]), gelu_tanh(t0[1])), cvtpk(gelu_tanh(t0[2]), gelu_tanh(t0[3])),
                   cvtpk(gelu_tanh(t1[0]), gelu_tanh(t1[1])), cvtpk(gelu_tanh(t1[2]), gelu_tanh(t1[3]))};
        *(u32x4*)(outp + ((size_t)(((dt * 5 + d) * 54 + wt * 2 + a) * 64 + lane)) * 8) = w;
      }
    }
  } else {
#pragma unroll
    for (int i = 0; i < 3; ++i) {
      const int u = wid + 8 * i;
      if (u < 20) {
        f32x4 t0 = {0.f, 0.f, 0.f, 0.f};
#pragma unroll
        for (int w = 0; w < 8; ++w) t0 += *(LAS const f32x4*)(lds + w * 20480 + u * 1024 + lane * 16);
        const int s = wt * 64 + (u / 5) * 16 + (lane & 15);
        const int d0 = dt * 80 + (u % 5) * 16 + 4 * g;
        const float bb = bf2f(ebs[i]);
        const f32x4 hv = eh[i];
        u32x2 w = {cvtpk(t0[0] + bb + hv[0], t0[1] + bb + hv[1]), cvtpk(t0[2] + bb + hv[2], t0[3] + bb + hv[3])};
        *(u32x2*)(outp + (size_t)s * DM + d0) = w;
      }
    }
  }
}

}

extern "C" void kernel_launch(void* const* d_in, const int* in_sizes, int n_in, void* d_out, int out_size, void* d_ws,
                              size_t ws_size, hipStream_t stream) {
  (void)in_sizes; (void)n_in; (void)out_size; (void)ws_size;
  const u16* x = (const u16*)d_in[0];
  const u16* ckv = (const u16*)d_in[1];
  const u16* wq = (const u16*)d_in[2];
  const u16* wuk = (const u16*)d_in[3];
  const u16* wuv = (const u16*)d_in[4];
  const u16* wo = (const u16*)d_in[5];
  const u16* w1 = (const u16*)d_in[7];
  const u16* b1 = (const u16*)d_in[8];
  const u16* w2 = (const u16*)d_in[9];
  const u16* b2 = (const u16*)d_in[10];
  char* ws = (char*)d_ws;
  u16* XN = (u16*)(ws + WS_XN);
  u16* KF = (u16*)(ws + WS_KF);
  u16* VF = (u16*)(ws + WS_VF);
  u16* CF = (u16*)(ws + WS_CF);
  u16* PT = (u16*)(ws + WS_PT);
  float* H = (float*)(ws + WS_H);
  u16* HN = (u16*)(ws + WS_HN);
  u16* UF = (u16*)(ws + WS_UF);
  (void)hipFuncSetAttribute((const void*)k1_kvproj, hipFuncAttributeMaxDynamicSharedMemorySize, K1_LDS);
  (void)hipFuncSetAttribute((const void*)k2_attn, hipFuncAttributeMaxDynamicSharedMemorySize, K2_LDS);
  (void)hipFuncSetAttribute((const void*)k3_oproj, hipFuncAttributeMaxDynamicSharedMemorySize, K3_LDS);
  (void)hipFuncSetAttribute((const void*)k56_mix<0>, hipFuncAttributeMaxDynamicSharedMemorySize, K56_LDS);
  (void)hipFuncSetAttribute((const void*)k56_mix<1>, hipFuncAttributeMaxDynamicSharedMemorySize, K56_LDS);
  k1_kvproj<<<256, 512, K1_LDS, stream>>>(x, ckv, wuk, wuv, KF, VF, XN, wq);
  k2_attn<<<256, 512, K2_LDS, stream>>>(XN, wq, KF, VF, CF, (float*)(ws + WS_LAM), wo);
  k3_oproj<<<256, 512, K3_LDS, stream>>>(CF, wo, PT);
  k4_ln<<<256, 320, 0, stream>>>(x, PT, H, HN, w1);
  k56_mix<0><<<256, 512, K56_LDS, stream>>>(w1, HN, b1, nullptr, UF, w2);
  k56_mix<1><<<256, 512, K56_LDS, stream>>>(w2, UF, b2, H, (u16*)d_out, nullptr);
}
```

```cpp
#include <hip/hip_runtime.h>
#include <stdint.h>
#include <utility>

namespace {

typedef unsigned short u16;
typedef __bf16 bf16x8 __attribute__((ext_vector_type(8)));
typedef __bf16 bf16x2 __attribute__((ext_vector_type(2)));
typedef short s16x4 __attribute__((ext_vector_type(4)));
typedef short s16x8 __attribute__((ext_vector_type(8)));
typedef float f32x2 __attribute__((ext_vector_type(2)));
typedef float f32x4 __attribute__((ext_vector_type(4)));
typedef float f32x16 __attribute__((ext_vector_type(16)));
typedef unsigned u32x2 __attribute__((ext_vector_type(2)));
typedef unsigned u32x4 __attribute__((ext_vector_type(4)));
#define LAS __attribute__((address_space(3)))
#define DI __device__ __forceinline__
#define SBAR() __builtin_amdgcn_sched_barrier(0)
#define RFL(x) __builtin_amdgcn_readfirstlane(x)

constexpr int SEQ = 2048, DM = 640, DC = 512, NH = 32, HD = 128, FF = 1728;
constexpr float LN_EPS = 1e-5f;
constexpr float QSCALE = 0.08838834764831845f * 1.4426950408889634f;
constexpr float P_LIMIT = 1073741824.0f;
constexpr float NEG_BIG = -1e30f;

constexpr size_t WS_XN = 0;
constexpr size_t WS_KF = WS_XN + (size_t)SEQ * DM * 2;
constexpr size_t WS_VF = WS_KF + (size_t)NH * SEQ * HD * 2;
constexpr size_t WS_CF = WS_VF + (size_t)NH * SEQ * HD * 2;
constexpr size_t WS_PT = WS_CF + (size_t)NH * SEQ * HD * 2;
constexpr size_t WS_H = WS_PT + (size_t)4 * SEQ * DM * 2;
constexpr size_t WS_HN = WS_H + (size_t)SEQ * DM * 4;
constexpr size_t WS_UF = WS_HN + (size_t)SEQ * DM * 2;
constexpr size_t WS_LAM = WS_UF + (size_t)DM * FF * 2;

DI __amdgpu_buffer_rsrc_t mk_rsrc(const void* p, unsigned bytes) {
  return __builtin_amdgcn_make_buffer_rsrc((void*)p, 0, bytes, 0x00020000);
}
DI void dma1(__amdgpu_buffer_rsrc_t rsrc, unsigned voff, unsigned soff, unsigned ldsaddr) {
  asm volatile("s_mov_b32 m0, %0\n\ts_nop 0\n\tbuffer_load_dwordx4 %1, %2, %3 offen lds"
               ::"s"(ldsaddr), "v"(voff), "s"(rsrc), "s"(soff) : "memory", "m0");
}
DI void warm8k(__amdgpu_buffer_rsrc_t rsrc, unsigned voff128, unsigned soff, unsigned ldsaddr) {
  asm volatile("s_mov_b32 m0, %0\n\ts_nop 0\n\tbuffer_load_dword %1, %2, %3 offen lds"
               ::"s"(ldsaddr), "v"(voff128), "s"(rsrc), "s"(soff) : "memory", "m0");
}
#define WAIT_VL_BAR(N) asm volatile("s_waitcnt vmcnt(" #N ") lgkmcnt(0)\n\ts_barrier" ::: "memory")
#define WAIT_L_BAR() asm volatile("s_waitcnt lgkmcnt(0)\n\ts_barrier" ::: "memory")
#define WAIT_L0() asm volatile("s_waitcnt lgkmcnt(0)" ::: "memory")

DI bf16x8 lds_b128(LAS const char* p) { return *(LAS const bf16x8*)p; }
DI bf16x8 lds_tr2(LAS const char* p0, LAS const char* p1) {
  s16x4 lo = __builtin_amdgcn_ds_read_tr16_b64_v4i16((LAS s16x4*)p0);
  s16x4 hi = __builtin_amdgcn_ds_read_tr16_b64_v4i16((LAS s16x4*)p1);
  s16x8 w = __builtin_shufflevector(lo, hi, 0, 1, 2, 3, 4, 5, 6, 7);
  return __builtin_bit_cast(bf16x8, w);
}
DI unsigned cvtpk(float lo, float hi) {
  f32x2 v = {lo, hi};
  bf16x2 b = __builtin_convertvector(v, bf16x2);
  return __builtin_bit_cast(unsigned, b);
}
DI u32x4 pack8(const f32x16& a, int o) {
  u32x4 w = {cvtpk(a[o], a[o + 1]), cvtpk(a[o + 2], a[o + 3]), cvtpk(a[o + 4], a[o + 5]), cvtpk(a[o + 6], a[o + 7])};
  return w;
}
DI float bf2f(unsigned short b) { return __uint_as_float(((unsigned)b) << 16); }
DI float bflo(unsigned w) { return __uint_as_float(w << 16); }
DI float bfhi(unsigned w) { return __uint_as_float(w & 0xffff0000u); }
#define MFMA32(a, b, c) __builtin_amdgcn_mfma_f32_32x32x16_bf16((a), (b), (c), 0, 0, 0)
#define MFMA16(a, b, c) __builtin_amdgcn_mfma_f32_16x16x32_bf16((a), (b), (c), 0, 0, 0)
DI float swap_max(float v) {
  auto rr = __builtin_amdgcn_permlane32_swap(__float_as_uint(v), __float_as_uint(v), false, false);
  return fmaxf(__uint_as_float(rr[0]), __uint_as_float(rr[1]));
}
DI float swap_add(float v) {
  auto rr = __builtin_amdgcn_permlane32_swap(__float_as_uint(v), __float_as_uint(v), false, false);
  return __uint_as_float(rr[0]) + __uint_as_float(rr[1]);
}

constexpr int K1_LDS = 4 * 32768;

template <bool ISV>
DI void k1_body(LAS char* lds, unsigned lds0, int wid, int lane, int head, int tb, const u16* __restrict__ ckv,
                const u16* __restrict__ wsrc, const u16* __restrict__ x, u16* __restrict__ KF, u16* __restrict__ VF,
                u16* __restrict__ XN, const u16* __restrict__ wq) {
  const int wm = wid >> 2, wn = wid & 3;
  const int r = lane & 31, h = lane >> 5;
  const int g1 = (lane >> 4) & 1, q = (lane & 15) >> 2, p = lane & 3;
  const __amdgpu_buffer_rsrc_t rA = mk_rsrc(ckv, SEQ * DC * 2);
  const __amdgpu_buffer_rsrc_t rWm = mk_rsrc(wsrc, DC * NH * HD * 2);
  const unsigned voffA = (lane >> 2) * 1024 + (((lane & 3) ^ ((lane >> 4) & 3)) << 4);
  const unsigned voffW = ((lane >> 2) & 7) * 8192 + (lane >> 5) * 64 + (lane & 3) * 16;
  auto piece = [&](int kt, int slot, int i) __attribute__((always_inline)) {
    const unsigned sb = lds0 + slot * 32768;
    if (i < 2) dma1(rA, voffA, (unsigned)((tb * 256 + (wid * 2 + i) * 16) * 1024 + kt * 64), sb + (wid * 2 + i) * 1024);
    else dma1(rWm, voffW, (unsigned)((kt * 32 + (wid >> 1) * 8) * 8192 + head * 256 + (i - 2) * 128),
              sb + 16384 + ((wid >> 1) * 4 + (wid & 1) * 2 + (i - 2)) * 1024);
  };
  unsigned abase[2];
#pragma unroll
  for (int s = 0; s < 2; ++s) abase[s] = wm * 8192 + r * 64 + (((2 * s + h) ^ ((r >> 2) & 3)) << 4);
  const unsigned wbase = 16384 + h * 4096 + wn * 1024 + q * 64 + g1 * 32 + p * 8;

  f32x16 acc[4][2];
#pragma unroll
  for (int i = 0; i < 4; ++i)
#pragma unroll
    for (int n = 0; n < 2; ++n) acc[i][n] = f32x16{};
  auto rd = [&](bf16x8 (&a)[4], bf16x8 (&w)[2], LAS const char* sb, int s) {
#pragma unroll
    for (int n = 0; n < 2; ++n) w[n] = lds_tr2(sb + wbase + s * 8192 + n * 512, sb + wbase + s * 8192 + n * 512 + 256);
#pragma unroll
    for (int i = 0; i < 4; ++i) a[i] = lds_b128(sb + abase[s] + i * 2048);
  };
  auto mm2 = [&](const bf16x8 (&a)[4], const bf16x8 (&w)[2], int i) {
#pragma unroll
    for (int n = 0; n < 2; ++n) {
      if (ISV) acc[i][n] = MFMA32(a[i], w[n], acc[i][n]);
      else acc[i][n] = MFMA32(w[n], a[i], acc[i][n]);
    }
  };
  bf16x8 aA[4], wA[2], aB[4], wB[2];
#pragma unroll
  for (int kt = 0; kt < 3; ++kt)
#pragma unroll
    for (int i = 0; i < 4; ++i) piece(kt, kt, i);
  auto stage = [&](const int kt, const int slot, const bool more, const bool first) __attribute__((always_inline)) {
    LAS const char* sb = lds + slot * 32768;
    rd(aA, wA, sb, 0);
    SBAR();
    if (!first) {
      mm2(aB, wB, 0); mm2(aB, wB, 1);
      if (more) piece(kt + 3, (slot + 3) & 3, 0);
      SBAR();
      mm2(aB, wB, 2); mm2(aB, wB, 3);
      if (more) piece(kt + 3, (slot + 3) & 3, 1);
    } else {
      piece(3, 3, 0); piece(3, 3, 1);
    }
    SBAR();
    rd(aB, wB, sb, 1);
    SBAR();
    mm2(aA, wA, 0); mm2(aA, wA, 1);
    if (more) piece(kt + 3, (slot + 3) & 3, 2);
    SBAR();
    mm2(aA, wA, 2); mm2(aA, wA, 3);
    if (more) piece(kt + 3, (slot + 3) & 3, 3);
    SBAR();
  };
  WAIT_VL_BAR(8); stage(0, 0, true, true);
  for (int k4 = 1; k4 < 13; k4 += 4) {
    WAIT_VL_BAR(8); stage(k4, 1, true, false);
    WAIT_VL_BAR(8); stage(k4 + 1, 2, true, false);
    WAIT_VL_BAR(8); stage(k4 + 2, 3, true, false);
    WAIT_VL_BAR(8); stage(k4 + 3, 0, true, false);
  }
  WAIT_VL_BAR(8); stage(13, 1, false, false);
  WAIT_VL_BAR(4); stage(14, 2, false, false);
  WAIT_VL_BAR(0); stage(15, 3, false, false);
  mm2(aB, wB, 0); mm2(aB, wB, 1); mm2(aB, wB, 2); mm2(aB, wB, 3);
  {
    const __amdgpu_buffer_rsrc_t rQ = mk_rsrc(wq, DM * NH * HD * 2);
    const int u = (blockIdx.x >> 3) * 8 + wid;
    if (u < 80) warm8k(rQ, (unsigned)((lane >> 3) * 8192 + (lane & 7) * 128), (unsigned)(u * 8 * 8192 + (blockIdx.x & 7) * 1024), lds0);
  }
  const int token = blockIdx.x * 8 + wid;
  const bool has1 = lane < 16;
  const u32x4 v0 = *(const u32x4*)(x + (size_t)token * DM + lane * 8);
  u32x4 v1 = {0u, 0u, 0u, 0u};
  if (has1) v1 = *(const u32x4*)(x + (size_t)token * DM + 512 + lane * 8);
  SBAR();
#pragma unroll
  for (int i = 0; i < 4; ++i) {
    const int kb32 = tb * 8 + wm * 4 + i;
#pragma unroll
    for (int n = 0; n < 2; ++n) {
#pragma unroll
      for (int sg = 0; sg < 2; ++sg) {
        const u32x4 w = pack8(acc[i][n], 8 * sg);
        if (ISV) {
          const int db = 2 * (wn - 2) + n;
          *(u32x4*)(VF + ((size_t)((((head * 64 + kb32) * 4 + db) * 2 + sg) * 64 + lane)) * 8) = w;
        } else {
          const int db = 2 * wn + n;
          *(u32x4*)(KF + ((size_t)(((head * 64 + kb32) * 8 + 2 * db + sg) * 64 + lane)) * 8) = w;
        }
      }
    }
  }
  {
    float f0[8], f1[8];
#pragma unroll
    for (int j = 0; j < 4; ++j) {
      f0[2 * j] = bflo(v0[j]); f0[2 * j + 1] = bfhi(v0[j]);
      f1[2 * j] = bflo(v1[j]); f1[2 * j + 1] = bfhi(v1[j]);
    }
    float s = 0.f;
#pragma unroll
    for (int j = 0; j < 8; ++j) s += f0[j] + f1[j];
#pragma unroll
    for (int o = 1; o < 64; o <<= 1) s += __shfl_xor(s, o);
    const float mean = s * (1.0f / DM);
    float sq = 0.f;
#pragma unroll
    for (int j = 0; j < 8; ++j) {
      const float d0 = f0[j] - mean; sq += d0 * d0;
      const float d1 = has1 ? f1[j] - mean : 0.f; sq += d1 * d1;
    }
#pragma unroll
    for (int o = 1; o < 64; o <<= 1) sq += __shfl_xor(sq, o);
    const float rstd = 1.0f / sqrtf(sq * (1.0f / DM) + LN_EPS);
    const int tb32 = token >> 5, rr = token & 31;
    u32x4 o0, o1;
#pragma unroll
    for (int j = 0; j < 4; ++j) {
      o0[j] = cvtpk((f0[2 * j] - mean) * rstd, (f0[2 * j + 1] - mean) * rstd);
      o1[j] = cvtpk((f1[2 * j] - mean) * rstd, (f1[2 * j + 1] - mean) * rstd);
    }
    *(u32x4*)(XN + ((size_t)((tb32 * 40 + (lane >> 1)) * 64 + (lane & 1) * 32 + rr)) * 8) = o0;
    if (has1) *(u32x4*)(XN + ((size_t)((tb32 * 40 + 32 + (lane >> 1)) * 64 + (lane & 1) * 32 + rr)) * 8) = o1;
  }
}

__global__ __launch_bounds__(512, 2) void k1_kvproj(const u16* __restrict__ x, const u16* __restrict__ ckv,
                                                    const u16* __restrict__ wuk, const u16* __restrict__ wuv,
                                                    u16* __restrict__ KF, u16* __restrict__ VF, u16* __restrict__ XN,
                                                    const u16* __restrict__ wq) {
  extern __shared__ __attribute__((aligned(16))) char smem[];
  LAS char* lds = (LAS char*)smem;
  const unsigned lds0 = (unsigned)(uintptr_t)lds;
  const int tid = threadIdx.x, lane = tid & 63;
  const int wid = RFL(tid >> 6);
  const int b = blockIdx.x, xcd = b & 7, idx = b >> 3;
  const int head = xcd * 4 + (idx & 3), tb = idx >> 2;
  const u16* wsrc = (wid & 1) ? wuv : wuk;
  if ((wid & 3) >= 2) k1_body<true>(lds, lds0, wid, lane, head, tb, ckv, wsrc, x, KF, VF, XN, wq);
  else k1_body<false>(lds, lds0, wid, lane, head, tb, ckv, wsrc, x, KF, VF, XN, wq);
}

constexpr int K2_LDS = 163840;
constexpr unsigned L_KB1 = 0, L_VB0 = 16384, L_VA1 = 32768, L_KA0 = 49152, L_KB0 = 65536, L_KA1 = 81920, L_VA0 = 98304,
                   L_VB1 = 114688, L_STASH = 131072;
constexpr int NSTEP = 17;
#define SPRIO 2
#define MPRIO 0
#define TRAILPRIO SPRIO

template <int OFF>
DI void dma1o(__amdgpu_buffer_rsrc_t rsrc, unsigned voff, unsigned soff, unsigned ldsaddr) {
  asm volatile("s_mov_b32 m0, %0\n\ts_nop 0\n\tbuffer_load_dwordx4 %1, %2, %3 offen offset:%4 lds"
               ::"s"(ldsaddr), "v"(voff), "s"(rsrc), "s"(soff), "i"(OFF) : "memory", "m0");
}
DI float fmul_s(float a, float b) { float r; asm("v_mul_f32 %0, %1, %2" : "=v"(r) : "v"(a), "v"(b)); return r; }
#define ACLOB "a8","a9","a10","a11","a12","a13","a14","a15","a16","a17","a18","a19","a20","a21","a22","a23","a24","a25","a26","a27","a28","a29","a30","a31","a32","a33","a34","a35","a36","a37","a38","a39","a40","a41","a42","a43","a44","a45","a46","a47","a48","a49","a50","a51","a52","a53","a54","a55","a56","a57","a58","a59","a60","a61","a62","a63","a64","a65","a66","a67","a68","a69","a70","a71","a72","a73","a74","a75","a76","a77","a78","a79","a80","a81","a82","a83","a84","a85","a86","a87","a88","a89","a90","a91","a92","a93","a94","a95","a96","a97","a98","a99","a100","a101","a102","a103","a104","a105","a106","a107","a108","a109","a110","a111","a112","a113","a114","a115","a116","a117","a118","a119","a120","a121","a122","a123","a124","a125","a126","a127"
constexpr int A_O = 8, A_Q = 72, A_R = 104, NRING = 6;
template <int SLOT, int OFF> DI void ring_read(unsigned addr) {
  asm volatile("ds_read_b128 a[%c1:%c2], %0 offset:%c3" ::"v"(addr), "i"(A_R + 4 * SLOT), "i"(A_R + 4 * SLOT + 3), "i"(OFF) : "memory");
}
template <int S, int OFF> DI void q_read(unsigned addr) {
  asm volatile("ds_read_b128 a[%c1:%c2], %0 offset:%c3" ::"v"(addr), "i"(A_Q + 4 * S), "i"(A_Q + 4 * S + 3), "i"(OFF) : "memory");
}
constexpr int V_NEGM = 64, V_S0 = 80, V_S1 = 96, V_PF = 112;
template <int W, int SLOT, int S, int DST> DI void qk_head() {
  asm volatile("s_waitcnt lgkmcnt(%c0)\n\tv_mfma_f32_32x32x16_bf16 v[%c1:%c2], a[%c3:%c4], a[%c5:%c6], v[%c7:%c8]"
               ::"i"(W), "i"(DST), "i"(DST + 15), "i"(A_R + 4 * SLOT), "i"(A_R + 4 * SLOT + 3), "i"(A_Q + 4 * S), "i"(A_Q + 4 * S + 3),
                 "i"(V_NEGM), "i"(V_NEGM + 15));
}
template <int W, int SLOT, int S, int DST> DI void qk_mfma() {
  asm volatile("s_waitcnt lgkmcnt(%c0)\n\tv_mfma_f32_32x32x16_bf16 v[%c1:%c2], a[%c3:%c4], a[%c5:%c6], v[%c1:%c2]"
               ::"i"(W), "i"(DST), "i"(DST + 15), "i"(A_R + 4 * SLOT), "i"(A_R + 4 * SLOT + 3), "i"(A_Q + 4 * S), "i"(A_Q + 4 * S + 3));
}
template <int W, int SLOT, int D, int P> DI void pv_mfma() {
  asm volatile("s_waitcnt lgkmcnt(%c0)\n\tv_mfma_f32_32x32x16_bf16 a[%c1:%c2], a[%c3:%c4], v[%c5:%c6], a[%c1:%c2]"
               ::"i"(W), "i"(A_O + 16 * D), "i"(A_O + 16 * D + 15), "i"(A_R + 4 * SLOT), "i"(A_R + 4 * SLOT + 3), "i"(V_PF + 4 * P),
                 "i"(V_PF + 4 * P + 3));
}
template <int W, int SLOT, int D, int P> DI void pv_zero() {
  asm volatile("s_waitcnt lgkmcnt(%c0)\n\tv_mfma_f32_32x32x16_bf16 a[%c1:%c2], a[%c3:%c4], v[%c5:%c6], 0"
               ::"i"(W), "i"(A_O + 16 * D), "i"(A_O + 16 * D + 15), "i"(A_R + 4 * SLOT), "i"(A_R + 4 * SLOT + 3), "i"(V_PF + 4 * P),
                 "i"(V_PF + 4 * P + 3));
}
template <int S, int P> DI void exps8(float& ps0, float& ps1, float& ps2, float& ps3) {
  float t0, t1, t2, t3, t4, t5, t6, t7;
  asm volatile("v_exp_f32 %4, v[%c12]\n\tv_exp_f32 %5, v[%c12+1]\n\tv_exp_f32 %6, v[%c12+2]\n\tv_exp_f32 %7, v[%c12+3]\n\t"
               "v_exp_f32 %8, v[%c12+4]\n\tv_exp_f32 %9, v[%c12+5]\n\tv_exp_f32 %10, v[%c12+6]\n\tv_exp_f32 %11, v[%c12+7]\n\t"
               "v_add_f32 %0, %0, %4\n\tv_add_f32 %1, %1, %5\n\tv_cvt_pk_bf16_f32 v[%c13], %4, %5\n\t"
               "v_add_f32 %2, %2, %6\n\tv_add_f32 %3, %3, %7\n\tv_cvt_pk_bf16_f32 v[%c13+1], %6, %7\n\t"
               "v_add_f32 %0, %0, %8\n\tv_add_f32 %1, %1, %9\n\tv_cvt_pk_bf16_f32 v[%c13+2], %8, %9\n\t"
               "v_add_f32 %2, %2, %10\n\tv_add_f32 %3, %3, %11\n\tv_cvt_pk_bf16_f32 v[%c13+3], %10, %11"
               : "+v"(ps0), "+v"(ps1), "+v"(ps2), "+v"(ps3), "=&v"(t0), "=&v"(t1), "=&v"(t2), "=&v"(t3), "=&v"(t4), "=&v"(t5),
                 "=&v"(t6), "=&v"(t7)
               : "i"(S), "i"(V_PF + 4 * P));
}
DI float exps32() {
  float ps0 = 0.f, ps1 = 0.f, ps2 = 0.f, ps3 = 0.f, r;
  exps8<V_S0, 0>(ps0, ps1, ps2, ps3); exps8<V_S0 + 8, 1>(ps0, ps1, ps2, ps3);
  exps8<V_S1, 2>(ps0, ps1, ps2, ps3); exps8<V_S1 + 8, 3>(ps0, ps1, ps2, ps3);
  asm volatile("v_add_f32 %1, %1, %2\n\tv_add_f32 %3, %3, %4\n\tv_add_f32 %0, %1, %3" : "=v"(r), "+v"(ps0), "+v"(ps1), "+v"(ps2), "+v"(ps3));
  return r;
}
DI float rowmax32() {
  float a, b;
  asm volatile("v_max3_f32 %0, v80, v81, v82\n\tv_max3_f32 %1, v83, v84, v85\n\tv_max3_f32 %0, %0, v86, v87\n\tv_max3_f32 %1, %1, v88, v89\n\t"
               "v_max3_f32 %0, %0, v90, v91\n\tv_max3_f32 %1, %1, v92, v93\n\tv_max3_f32 %0, %0, v94, v95\n\tv_max3_f32 %1, %1, v96, v97\n\t"
               "v_max3_f32 %0, %0, v98, v99\n\tv_max3_f32 %1, %1, v100, v101\n\tv_max3_f32 %0, %0, v102, v103\n\tv_max3_f32 %1, %1, v104, v105\n\t"
               "v_max3_f32 %0, %0, v106, v107\n\tv_max3_f32 %1, %1, v108, v109\n\tv_max3_f32 %0, %0, v110, v111\n\tv_max_f32 %0, %0, %1"
               : "=&v"(a), "=&v"(b));
  return a;
}
template <int R> DI void vreg_add(float t) { asm volatile("v_add_f32 v[%c1], v[%c1], %0" ::"v"(t), "i"(R)); }
template <int S, int... E> DI void mask_all_(float ninf, std::integer_sequence<int, E...>) { (vreg_add<S + E>(ninf), ...); }
template <int S, int... E> DI void mask_tri_(int dq, float ninf, std::integer_sequence<int, E...>) {
  (vreg_add<S + E>(((E & 3) + 8 * (E >> 2) > dq) ? ninf : 0.f), ...);
}
template <int I> DI float acc_rd() { float r; asm volatile("v_accvgpr_read_b32 %0, a[%c1]" : "=v"(r) : "i"(I)); return r; }
template <int I> DI void acc_wr(float v) { asm volatile("v_accvgpr_write_b32 a[%c1], %0" ::"v"(v), "i"(I)); }
template <int... E> DI void o_scale_(float f, std::integer_sequence<int, E...>) { (acc_wr<A_O + E>(fmul_s(acc_rd<A_O + E>(), f)), ...); }
template <int R> DI u32x4 acc_scale8(float f) {
  u32x4 o; float t0, t1, t2, t3, t4, t5, t6, t7;
  asm volatile("v_accvgpr_read_b32 %4, a[%c13]\n\tv_accvgpr_read_b32 %5, a[%c13+1]\n\tv_accvgpr_read_b32 %6, a[%c13+2]\n\t"
               "v_accvgpr_read_b32 %7, a[%c13+3]\n\tv_accvgpr_read_b32 %8, a[%c13+4]\n\tv_accvgpr_read_b32 %9, a[%c13+5]\n\t"
               "v_accvgpr_read_b32 %10, a[%c13+6]\n\tv_accvgpr_read_b32 %11, a[%c13+7]\n\t"
               "v_mul_f32 %4, %4, %12\n\tv_mul_f32 %5, %5, %12\n\tv_mul_f32 %6, %6, %12\n\tv_mul_f32 %7, %7, %12\n\t"
               "v_mul_f32 %8, %8, %12\n\tv_mul_f32 %9, %9, %12\n\tv_mul_f32 %10, %10, %12\n\tv_mul_f32 %11, %11, %12\n\t"
               "v_cvt_pk_bf16_f32 %0, %4, %5\n\tv_cvt_pk_bf16_f32 %1, %6, %7\n\tv_cvt_pk_bf16_f32 %2, %8, %9\n\tv_cvt_pk_bf16_f32 %3, %10, %11"
               : "=&v"(o[0]), "=&v"(o[1]), "=&v"(o[2]), "=&v"(o[3]), "=&v"(t0), "=&v"(t1), "=&v"(t2), "=&v"(t3), "=&v"(t4),
                 "=&v"(t5), "=&v"(t6), "=&v"(t7)
               : "v"(f), "i"(R));
  return o;
}
template <int R, int OFF> DI void acc_to_lds(unsigned addr) {
  asm volatile("ds_write_b128 %0, a[%c1:%c2] offset:%c3" ::"v"(addr), "i"(R), "i"(R + 3), "i"(OFF) : "memory");
}
template <int R> DI u32x4 acc_combine8f(const f32x4& w0, const f32x4& w1, float cw, float ca) {
  u32x4 o; float t0, t1, t2, t3, t4, t5, t6, t7;
  asm volatile("v_accvgpr_read_b32 %4, a[%c22]\n\tv_accvgpr_read_b32 %5, a[%c22+1]\n\tv_accvgpr_read_b32 %6, a[%c22+2]\n\t"
               "v_accvgpr_read_b32 %7, a[%c22+3]\n\tv_accvgpr_read_b32 %8, a[%c22+4]\n\tv_accvgpr_read_b32 %9, a[%c22+5]\n\t"
               "v_accvgpr_read_b32 %10, a[%c22+6]\n\tv_accvgpr_read_b32 %11, a[%c22+7]\n\t"
               "v_mul_f32 %4, %4, %21\n\tv_mul_f32 %5, %5, %21\n\tv_mul_f32 %6, %6, %21\n\tv_mul_f32 %7, %7, %21\n\t"
               "v_mul_f32 %8, %8, %21\n\tv_mul_f32 %9, %9, %21\n\tv_mul_f32 %10, %10, %21\n\tv_mul_f32 %11, %11, %21\n\t"
               "v_fma_f32 %4, %12, %20, %4\n\tv_fma_f32 %5, %13, %20, %5\n\tv_fma_f32 %6, %14, %20, %6\n\tv_fma_f32 %7, %15, %20, %7\n\t"
               "v_fma_f32 %8, %16, %20, %8\n\tv_fma_f32 %9, %17, %20, %9\n\tv_fma_f32 %10, %18, %20, %10\n\tv_fma_f32 %11, %19, %20, %11\n\t"
               "v_cvt_pk_bf16_f32 %0, %4, %5\n\tv_cvt_pk_bf16_f32 %1, %6, %7\n\tv_cvt_pk_bf16_f32 %2, %8, %9\n\tv_cvt_pk_bf16_f32 %3, %10, %11"
               : "=&v"(o[0]), "=&v"(o[1]), "=&v"(o[2]), "=&v"(o[3]), "=&v"(t0), "=&v"(t1), "=&v"(t2), "=&v"(t3), "=&v"(t4),
                 "=&v"(t5), "=&v"(t6), "=&v"(t7)
               : "v"(w0[0]), "v"(w0[1]), "v"(w0[2]), "v"(w0[3]), "v"(w1[0]), "v"(w1[1]), "v"(w1[2]), "v"(w1[3]), "v"(cw), "v"(ca),
                 "i"(R));
  return o;
}
DI void scores_add(float aone, float v) {
  asm volatile("s_nop 1\n\tv_mfma_f32_32x32x2_f32 v[80:95], %0, %1, v[80:95]\n\tv_mfma_f32_32x32x2_f32 v[96:111], %0, %1, v[96:111]" ::"v"(aone), "v"(v));
}
DI void negm_set(float aone, float v) { asm volatile("s_nop 1\n\tv_mfma_f32_32x32x2_f32 v[64:79], %0, %1, 0" ::"v"(aone), "v"(v)); }
#define ACC_FENCE() asm volatile("s_nop 15\n\ts_nop 3" ::: "memory")
#define BAR_A(WAITS) asm volatile("s_waitcnt " WAITS "\n\ts_barrier" ::: "memory", "v127", ACLOB)

template <int J> DI void frag_read(unsigned ka, unsigned va) {
  if constexpr (J < 16) ring_read<J % NRING, (J & 1) * 8192 + (J >> 1) * 1024>(ka);
  else { constexpr int i = J - 16; ring_read<J % NRING, (i >> 3) * 8192 + ((i & 3) * 2 + ((i >> 2) & 1)) * 1024>(va); }
}
#define PDX 4
constexpr int PD = PDX;

__global__ __launch_bounds__(512, 2) __attribute__((amdgpu_num_vgpr(64))) void k2_attn(const u16* __restrict__ XN, const u16* __restrict__ wq,
                                                  const u16* __restrict__ KF, const u16* __restrict__ VF,
                                                  u16* __restrict__ CF, float* __restrict__ LAM,
                                                  const u16* __restrict__ wo) {
  extern __shared__ __attribute__((aligned(16))) char smem[];
  LAS char* lds = (LAS char*)smem;
  const unsigned lds0 = (unsigned)(uintptr_t)lds;
  const int tid = threadIdx.x, lane = tid & 63;
  const int wid = RFL(tid >> 6);
  const int b = blockIdx.x, xcd = b & 7, idx = b >> 3;
  const int head = xcd * 4 + (idx >> 3), px = idx & 7;
  const int qbH = 15 - px, qbL = px;
  const int rr = wid & 3, c = wid >> 2;
  const int h = lane >> 5;
  const int g1 = (lane >> 4) & 1, q4 = (lane & 15) >> 2, p4 = lane & 3;
  const __amdgpu_buffer_rsrc_t rX = mk_rsrc(XN, SEQ * DM * 2);
  const __amdgpu_buffer_rsrc_t rW = mk_rsrc(wq, DM * NH * HD * 2);
  const __amdgpu_buffer_rsrc_t rK = mk_rsrc(KF, NH * SEQ * HD * 2);
  const __amdgpu_buffer_rsrc_t rV = mk_rsrc(VF, NH * SEQ * HD * 2);
  const unsigned vlin = lane * 16;
  const unsigned voffW = ((lane >> 2) & 7) * 8192 + (lane >> 5) * 64 + (lane & 3) * 16;
  const int p1 = 2 * (px + 1);
  auto kvoff = [&](int sg) -> unsigned {
    const int hidx = sg < p1 ? sg : 2 * sg - p1 + c;
    return (unsigned)((head * 64 + 2 * hidx) * 8192);
  };
  const int tbx = (wid < 4) ? (4 * qbH + wid) : (4 * qbL + wid - 4);
  auto q_load = [&](unsigned addr) {
    q_read<0, 0>(addr); q_read<1, 1024>(addr); q_read<2, 2048>(addr); q_read<3, 3072>(addr);
    q_read<4, 4096>(addr); q_read<5, 5120>(addr); q_read<6, 6144>(addr); q_read<7, 7168>(addr);
  };
  {
    auto q_piece = [&](int kt, int i) {
      const unsigned sb = lds0 + (unsigned)(kt % 3) * 49152;
      if (i < 2) dma1(rW, voffW, (unsigned)((kt * 64 + wid * 8) * 8192 + head * 256 + i * 128), sb + (wid * 2 + i) * 1024);
      else dma1(rX, vlin, (unsigned)((tbx * 40 + 4 * kt + (i - 2)) * 1024), sb + 16384 + wid * 4096 + (i - 2) * 1024);
    };
    auto kv_piece = [&](int i) {
      const unsigned part = wid * 2048 + i * 1024;
      dma1(rK, vlin, (unsigned)(head * 64 * 8192) + part, lds0 + L_KA0 + part);
    };
#pragma unroll
    for (int i = 0; i < 6; ++i) q_piece(0, i);
#pragma unroll
    for (int i = 0; i < 6; ++i) q_piece(1, i);
    f32x16 qa[2][2];
#pragma unroll
    for (int i = 0; i < 2; ++i)
#pragma unroll
      for (int n = 0; n < 2; ++n) qa[i][n] = f32x16{};
    const unsigned wbase = h * 2048 + c * 1024 + q4 * 64 + g1 * 32 + p4 * 8;
    const unsigned xbase = 16384 + rr * 4096 + lane * 16;
    bf16x8 wA[2], xA[2], wB[2], xB[2];
    auto rdq = [&](bf16x8 (&w)[2], bf16x8 (&x)[2], LAS const char* sb, int s) {
      w[0] = lds_tr2(sb + wbase + s * 4096, sb + wbase + s * 4096 + 256);
      x[0] = lds_b128(sb + xbase + s * 1024);
      w[1] = lds_tr2(sb + wbase + s * 4096 + 512, sb + wbase + s * 4096 + 768);
      x[1] = lds_b128(sb + xbase + 16384 + s * 1024);
    };
    auto mmq = [&](const bf16x8 (&w)[2], const bf16x8 (&x)[2]) {
      qa[0][0] = MFMA32(w[0], x[0], qa[0][0]);
      qa[1][0] = MFMA32(w[0], x[1], qa[1][0]);
      qa[0][1] = MFMA32(w[1], x[0], qa[0][1]);
      qa[1][1] = MFMA32(w[1], x[1], qa[1][1]);
    };
    for (int kt = 0; kt < 10; ++kt) {
      if (kt < 9) { WAIT_VL_BAR(6); } else { WAIT_VL_BAR(2); }
      LAS const char* sb = lds + (unsigned)(kt % 3) * 49152;
      auto piece = [&](int i) {
        if (kt < 8) q_piece(kt + 2, i);
        else if (kt == 8 && i < 2) kv_piece(i);
      };
      rdq(wA, xA, sb, 0);
      SBAR();
      if (kt > 0) mmq(wB, xB);
      piece(0); SBAR();
      rdq(wB, xB, sb, 1);
      SBAR();
      mmq(wA, xA);
      piece(1); piece(2); SBAR();
      rdq(wA, xA, sb, 2);
      SBAR();
      mmq(wB, xB);
      piece(3); piece(4); SBAR();
      rdq(wB, xB, sb, 3);
      SBAR();
      mmq(wA, xA);
      piece(5); SBAR();
    }
    mmq(wB, xB);
    WAIT_L_BAR();
#pragma unroll
    for (int i = 0; i < 2; ++i) {
      LAS char* dst = lds + (i == 0 ? L_STASH : 0u) + rr * 8192 + lane * 16;
#pragma unroll
      for (int n = 0; n < 2; ++n) {
        const f32x16 t = qa[i][n] * QSCALE;
#pragma unroll
        for (int sg = 0; sg < 2; ++sg) *(LAS u32x4*)(dst + (4 * c + 2 * n + sg) * 1024) = pack8(t, 8 * sg);
      }
    }
    WAIT_L_BAR();
    q_load(lds0 + (c ? 0u : L_STASH) + rr * 8192 + lane * 16);
    BAR_A("vmcnt(0) lgkmcnt(0)");
  }
  negm_set(0.f, 0.f);
  float mref = 0.f, lsum = 0.f, mfin = 0.f, lfin = 0.f;
  const unsigned kown0 = c ? L_KB0 : L_KA0, kown1 = c ? L_KB1 : L_KA1;
  const unsigned vown0 = c ? L_VB0 : L_VA0, vown1 = c ? L_VB1 : L_VA1;
  const unsigned lrd = lds0 + lane * 16;
  float* lamp = LAM + (size_t)b * 256 + rr * 64;
  f32x2 mlA = {0.f, 0.f};

  auto refix = [&](const bool first) __attribute__((always_inline)) {
    const float rm = swap_max(rowmax32());
    const float dl = first ? fmaxf(rm, NEG_BIG) : fmaxf(rm, 0.f);
    const float f = first ? 0.f : __builtin_amdgcn_exp2f(-dl);
    mref = first ? dl : mref + dl;
    lsum = first ? 0.f : lsum * f;
    int ln = lane;
    asm volatile("" : "+v"(ln));
    const float aone = ln < 32 ? 1.f : 0.f;
    scores_add(aone, -dl);
    negm_set(aone, -mref);
    ACC_FENCE();
    if (!first) { o_scale_(f, std::make_integer_sequence<int, 64>{}); ACC_FENCE(); }
  };
#define TRAIL() do { pv_mfma<0, 28 % NRING, 0, 3>(); pv_mfma<0, 29 % NRING, 1, 3>(); pv_mfma<0, 30 % NRING, 2, 3>(); pv_mfma<0, 31 % NRING, 3, 3>(); SBAR(); } while (0)
  auto splain = [&]() __attribute__((always_inline)) {
    __builtin_amdgcn_s_setprio(TRAILPRIO);
    TRAIL();
    if (TRAILPRIO != SPRIO) __builtin_amdgcn_s_setprio(SPRIO);
    float pst = exps32();
    if (__builtin_expect(__any(pst > P_LIMIT), 0)) { refix(false); pst = exps32(); }
    asm volatile("v_add_f32 %0, %0, %1" : "+v"(lsum) : "v"(pst));
    __builtin_amdgcn_s_setprio(MPRIO);
  };
  auto sgen = [&](const int tau) __attribute__((always_inline)) {
    __builtin_amdgcn_s_setprio(SPRIO);
    if (tau > 0) TRAIL();
    const bool lastL = c == 1 && tau == p1 - 1;
    const bool diag = tau == NSTEP - 1 || (c == 1 && (tau == p1 - 2 || tau == p1 - 1));
    const bool first = tau == 0 || (c == 1 && tau == p1);
    int ln = lane;
    asm volatile("" : "+v"(ln));
    if (diag) {
      const int hp = tau == NSTEP - 1 ? c : tau - (p1 - 2);
      float ninf = -INFINITY;
      asm volatile("" : "+v"(ninf));
      const int dq = (ln & 31) - 4 * (ln >> 5);
      if (2 * hp > rr) mask_all_<V_S0>(ninf, std::make_integer_sequence<int, 16>{});
      else if (2 * hp == rr) mask_tri_<V_S0>(dq, ninf, std::make_integer_sequence<int, 16>{});
      if (2 * hp + 1 > rr) mask_all_<V_S1>(ninf, std::make_integer_sequence<int, 16>{});
      else if (2 * hp + 1 == rr) mask_tri_<V_S1>(dq, ninf, std::make_integer_sequence<int, 16>{});
    }
    if (first && tau > 0) {
      ACC_FENCE();
      const float inv = 1.0f / lfin;
      u16* dstp = CF + ((size_t)(((4 * qbL + rr) * 32 + head) * 8) * 64 + ln) * 8;
      *(u32x4*)(dstp) = acc_scale8<A_O + 0>(inv);          *(u32x4*)(dstp + 512) = acc_scale8<A_O + 8>(inv);
      *(u32x4*)(dstp + 1024) = acc_scale8<A_O + 16>(inv);  *(u32x4*)(dstp + 1536) = acc_scale8<A_O + 24>(inv);
      *(u32x4*)(dstp + 2048) = acc_scale8<A_O + 32>(inv);  *(u32x4*)(dstp + 2560) = acc_scale8<A_O + 40>(inv);
      *(u32x4*)(dstp + 3072) = acc_scale8<A_O + 48>(inv);  *(u32x4*)(dstp + 3584) = acc_scale8<A_O + 56>(inv);
    }
    if (first) refix(true);
    float pst = exps32();
    if (!first && __any(pst > P_LIMIT)) { refix(false); pst = exps32(); }
    asm volatile("v_add_f32 %0, %0, %1" : "+v"(lsum) : "v"(pst));
    if (lastL || tau == NSTEP - 1) {
      mfin = mref; lfin = swap_add(lsum);
      if (lastL) {
        negm_set(0.f, 0.f);
        q_load(lds0 + L_STASH + rr * 8192 + ln * 16);
      } else {
        f32x2* lp = (f32x2*)lamp + (ln & 31);
        if (c == 0) { if (ln < 32) *lp = (f32x2){mfin, lfin}; }
        else mlA = *lp;
      }
    }
    __builtin_amdgcn_s_setprio(0);
  };
#define KVDMA(i) do { if ((i) == 0) dma1o<0>(rK, vlin, gk, lk); else if ((i) == 1) dma1o<1024>(rK, vlin, gk, lk); \
    else if ((i) == 2) dma1o<2048>(rK, vlin, gk, lk); else if ((i) == 3) dma1o<3072>(rK, vlin, gk, lk); \
    else if ((i) == 4) dma1o<0>(rV, vlin, gv, lv); else if ((i) == 5) dma1o<1024>(rV, vlin, gv, lv); \
    else if ((i) == 6) dma1o<2048>(rV, vlin, gv, lv); else dma1o<3072>(rV, vlin, gv, lv); } while (0)
#define MSTEP(J, FIRST, LAST, DMA, ZO) do { \
    constexpr int out_ = ((J) + PD - 1 < (LAST) ? (J) + PD - 1 : (LAST)) - (J);     \
    if constexpr ((J) < 2) qk_head<out_, (J) % NRING, 0, ((J) & 1) ? V_S1 : V_S0>(); \
    else if constexpr ((J) < 16) qk_mfma<out_, (J) % NRING, ((J) >> 1), ((J) & 1) ? V_S1 : V_S0>(); \
    else if constexpr ((J) < 20 && (ZO)) { if (zero_o) pv_zero<out_, (J) % NRING, (J) & 3, 0>(); else pv_mfma<out_, (J) % NRING, (J) & 3, 0>(); } \
    else pv_mfma<out_, (J) % NRING, (J) & 3, (((J) - 16) >> 2)>(); \
    if constexpr ((J) + PD <= (LAST)) frag_read<(J) + PD>(ka, va); \
    if constexpr ((DMA) && ((J) & 1) && (J) < 16) KVDMA((J) >> 1); \
    SBAR(); } while (0)
#define MSTEP4(J, FIRST, LAST, DMA, ZO) MSTEP(J, FIRST, LAST, DMA, ZO); MSTEP((J) + 1, FIRST, LAST, DMA, ZO); MSTEP((J) + 2, FIRST, LAST, DMA, ZO); MSTEP((J) + 3, FIRST, LAST, DMA, ZO)
#define KSLOT(t) (((t) < p1) ? (((t) & 1) ? L_KA1 : L_KA0) : (((t) & 1) ? kown1 : kown0))
#define VSLOT(t) (((t) < p1) ? (((t) & 1) ? L_VA1 : L_VA0) : (((t) & 1) ? vown1 : vown0))
#define STAGE_ADDR(t) \
      const unsigned gk = ((t) + 1 < NSTEP && (c == 0 || (t) + 1 >= p1)) ? kvoff((t) + 1) + rr * 4096 : 0x7ff00000u; \
      const unsigned gv = (c == 0 || (t) >= p1) ? kvoff(t) + rr * 4096 : 0x7ff00000u; \
      const unsigned lk = lds0 + ((((t) + 1) & 1) ? kown1 : kown0) + rr * 4096; \
      const unsigned lv = lds0 + (((t) & 1) ? vown1 : vown0) + rr * 4096;

  if (c == 1) asm volatile("s_barrier" ::: "memory");
  {
    const unsigned ka = lrd + L_KA0, va = 0;
    STAGE_ADDR(0)
    constexpr bool zero_o = false;
    frag_read<0>(ka, va); frag_read<1>(ka, va); frag_read<2>(ka, va); frag_read<3>(ka, va);
    SBAR();
    MSTEP4(0, 0, 15, true, false); MSTEP4(4, 0, 15, true, false); MSTEP4(8, 0, 15, true, false); MSTEP4(12, 0, 15, true, false);
    BAR_A("lgkmcnt(0)");
    ACC_FENCE();
    sgen(0);
    BAR_A("vmcnt(0) lgkmcnt(0)");
  }
#define MPHASE(tau, DMA, ZO) do { \
      const bool zero_o = (tau) == 1 || (c == 1 && (tau) == p1 + 1);     \
      (void)zero_o; \
      const unsigned ka = lrd + KSLOT(tau); \
      const unsigned va = lrd + VSLOT((tau) - 1); \
      STAGE_ADDR(tau) \
      (void)gk; (void)gv; (void)lk; (void)lv; \
      frag_read<0>(ka, va); frag_read<1>(ka, va); frag_read<2>(ka, va); frag_read<3>(ka, va); \
      SBAR(); \
      MSTEP4(0, 0, 31, DMA, ZO); MSTEP4(4, 0, 31, DMA, ZO); MSTEP4(8, 0, 31, DMA, ZO); MSTEP4(12, 0, 31, DMA, ZO); \
      MSTEP4(16, 0, 31, DMA, ZO); MSTEP4(20, 0, 31, DMA, ZO); MSTEP4(24, 0, 31, DMA, ZO); \
      BAR_A("lgkmcnt(0)"); } while (0)
  int tau = 1;
  while (tau < NSTEP) {
    const bool special = tau == 1 || tau == NSTEP - 1 || (c == 1 && tau >= p1 - 2 && tau <= p1 + 1);
    if (special) {
      MPHASE(tau, true, true);
      sgen(tau);
      BAR_A("vmcnt(0) lgkmcnt(0)");
      ++tau;
    } else if (c == 1 && tau < p1 - 2) {
      do {
        MPHASE(tau, false, false);
        splain();
        BAR_A("lgkmcnt(0)");
        ++tau;
      } while (tau < p1 - 2);
    } else {
      do {
        MPHASE(tau, true, false);
        splain();
        BAR_A("vmcnt(0) lgkmcnt(0)");
        ++tau;
      } while (tau < NSTEP - 1);
    }
  }
#undef MPHASE
#undef TRAIL
  {
    const unsigned v128 = lane * 128;
    const unsigned dead = lds0 + vown1 + rr * 256;
    const int u = idx * 8 + wid;
    if (u < 160) warm8k(mk_rsrc(wo, NH * HD * DM * 2), v128, (unsigned)((xcd >> 1) * 1024 * 1280 + u * 8192), dead);
  }
  {
    const unsigned ka = 0, va = lrd + VSLOT(NSTEP - 1);
    const unsigned gk = 0, gv = 0, lk = 0, lv = 0;
    (void)gk; (void)gv; (void)lk; (void)lv; (void)ka;
    const bool zero_o = c == 1 && p1 == NSTEP - 1;
    frag_read<16>(ka, va); frag_read<17>(ka, va); frag_read<18>(ka, va); frag_read<19>(ka, va);
    SBAR();
    MSTEP4(16, 16, 31, false, true); MSTEP4(20, 16, 31, false, true); MSTEP4(24, 16, 31, false, true); MSTEP4(28, 16, 31, false, true);
    BAR_A("lgkmcnt(0)");
    int ln = lane;
    asm volatile("" : "+v"(ln));
    const unsigned mine = lds0 + (c ? L_KB1 : L_KA0) + ((rr & 2) ? (c ? L_KB0 - L_KB1 : L_KA1 - L_KA0) : 0u) + (rr & 1) * 8192 + ln * 16;
    const unsigned theirs = (c ? L_KA0 : L_KB1) + ((rr & 2) ? (c ? L_KA1 - L_KA0 : L_KB0 - L_KB1) : 0u) + (rr & 1) * 8192 + ln * 16;
    ACC_FENCE();
    if (c == 0) {
      acc_to_lds<A_O + 32, 0>(mine); acc_to_lds<A_O + 36, 1024>(mine); acc_to_lds<A_O + 40, 2048>(mine); acc_to_lds<A_O + 44, 3072>(mine);
      acc_to_lds<A_O + 48, 4096>(mine); acc_to_lds<A_O + 52, 5120>(mine); acc_to_lds<A_O + 56, 6144>(mine); acc_to_lds<A_O + 60, 7168>(mine);
      BAR_A("lgkmcnt(0)");
    } else {
      acc_to_lds<A_O + 0, 0>(mine); acc_to_lds<A_O + 4, 1024>(mine); acc_to_lds<A_O + 8, 2048>(mine); acc_to_lds<A_O + 12, 3072>(mine);
      acc_to_lds<A_O + 16, 4096>(mine); acc_to_lds<A_O + 20, 5120>(mine); acc_to_lds<A_O + 24, 6144>(mine); acc_to_lds<A_O + 28, 7168>(mine);
      if (ln < 32) *(LAS f32x2*)(lds + L_STASH + rr * 256 + ln * 8) = (f32x2){mfin, lfin};
    }
    BAR_A("lgkmcnt(0)");
    {
      const f32x2 mo = c ? mlA : *(LAS const f32x2*)(lds + L_STASH + rr * 256 + (ln & 31) * 8);
      const float m = fmaxf(mo[0], mfin);
      const float eo = __builtin_amdgcn_exp2f(mo[0] - m), em = __builtin_amdgcn_exp2f(mfin - m);
      const float inv = 1.0f / (mo[1] * eo + lfin * em);
      const float co = eo * inv, cm = em * inv;
      f32x4 w[8];
#pragma unroll
      for (int i = 0; i < 8; ++i) w[i] = *(LAS const f32x4*)(lds + theirs + i * 1024);
      u16* dstp = CF + ((size_t)(((4 * qbH + rr) * 32 + head) * 8 + 4 * c) * 64 + ln) * 8;
      if (c == 0) {
        *(u32x4*)(dstp) = acc_combine8f<A_O + 0>(w[0], w[1], co, cm);         *(u32x4*)(dstp + 512) = acc_combine8f<A_O + 8>(w[2], w[3], co, cm);
        *(u32x4*)(dstp + 1024) = acc_combine8f<A_O + 16>(w[4], w[5], co, cm); *(u32x4*)(dstp + 1536) = acc_combine8f<A_O + 24>(w[6], w[7], co, cm);
      } else {
        *(u32x4*)(dstp) = acc_combine8f<A_O + 32>(w[0], w[1], co, cm);        *(u32x4*)(dstp + 512) = acc_combine8f<A_O + 40>(w[2], w[3], co, cm);
        *(u32x4*)(dstp + 1024) = acc_combine8f<A_O + 48>(w[4], w[5], co, cm); *(u32x4*)(dstp + 1536) = acc_combine8f<A_O + 56>(w[6], w[7], co, cm);
      }
    }
  }
#undef KSLOT
#undef VSLOT
#undef STAGE_ADDR
#undef KVDMA
#undef MSTEP
#undef MSTEP4
}

constexpr int K3_STAGE = 37888, K3_NST = 4, K3_LDS = K3_STAGE * K3_NST;

__global__ __launch_bounds__(512, 2) void k3_oproj(const u16* __restrict__ CF, const u16* __restrict__ wo,
                                                   u16* __restrict__ PT) {
  extern __shared__ __attribute__((aligned(16))) char smem[];
  LAS char* lds = (LAS char*)smem;
  const unsigned lds0 = (unsigned)(uintptr_t)lds;
  const int tid = threadIdx.x, lane = tid & 63;
  const int wid = RFL(tid >> 6);
  const int b = blockIdx.x, xcd = b & 7, idx = b >> 3;
  const int ks = xcd >> 1, panel = (xcd & 1) * 8 + (idx >> 2), nt = idx & 3;
  const int tbi = wid & 3, kp = wid >> 2;
  const int h = lane >> 5, g1 = (lane >> 4) & 1, q4 = (lane & 15) >> 2, p4 = lane & 3;
  const __amdgpu_buffer_rsrc_t rC = mk_rsrc(CF, NH * SEQ * HD * 2);
  const __amdgpu_buffer_rsrc_t rW = mk_rsrc(wo, NH * HD * DM * 2);
  const unsigned vlin = lane * 16;
  unsigned voffW[3];
#pragma unroll
  for (int i = 0; i < 3; ++i) {
    const int pc = wid + 8 * i;
    const int u = 2 * pc + (lane >> 5);
    const int kb = u / 5, nb = u % 5;
    voffW[i] = (unsigned)((kb * 8 + ((lane >> 2) & 7)) * 1280 + (nb * 32 + (lane & 3) * 8) * 2);
  }
  const bool lo = wid < 4;
  const unsigned sW0 = (unsigned)(ks * 1024 * 1280 + nt * 320);
  const unsigned sC0 = (unsigned)(((panel * 4 + tbi) * 256 + ks * 64) * 1024);
  const __amdgpu_buffer_rsrc_t r2 = lo ? rW : rC;
  const unsigned v2 = lo ? voffW[2] : vlin;
  const unsigned s2 = lo ? sW0 : sC0, st2 = lo ? 81920u : 4096u, l2 = lo ? (wid + 16) * 1024 : 20480 + tbi * 4096;
  const unsigned s3 = sC0 + (lo ? 3072u : 1024u), l3 = 20480 + tbi * 4096 + (lo ? 3072 : 1024);
  const unsigned s4 = lo ? 0x7ff00000u : sC0 + 2048u, l4 = lo ? 36864 : 20480 + tbi * 4096 + 2048;
  auto piece = [&](int j, int slot, int i) __attribute__((always_inline)) {
    const unsigned sb = lds0 + slot * K3_STAGE;
    if (i == 0) dma1(rW, voffW[0], sW0 + j * 81920, sb + wid * 1024);
    else if (i == 1) dma1(rW, voffW[1], sW0 + j * 81920, sb + (wid + 8) * 1024);
    else if (i == 2) dma1(r2, v2, s2 + j * st2, sb + l2);
    else if (i == 3) dma1(rC, vlin, s3 + j * 4096, sb + l3);
    else dma1(rC, vlin, s4 + j * 4096, sb + l4);
  };
  f32x16 acc[5];
#pragma unroll
  for (int n = 0; n < 5; ++n) acc[n] = f32x16{};
  const unsigned wbase = h * 256 + q4 * 64 + g1 * 32 + p4 * 8 + kp * 4 * 2560;
  const unsigned cbase = 20480 + tbi * 4096 + lane * 16 + kp * 2048;
  auto rd = [&](bf16x8& cf, bf16x8 (&w)[5], LAS const char* sb, int si) {
    cf = lds_b128(sb + cbase + si * 1024);
#pragma unroll
    for (int n = 0; n < 5; ++n)
      w[n] = lds_tr2(sb + wbase + (2 * si) * 2560 + n * 512, sb + wbase + (2 * si + 1) * 2560 + n * 512);
  };
  bf16x8 cA, cB, wA[5], wB[5];
#pragma unroll
  for (int j = 0; j < 3; ++j)
#pragma unroll
    for (int i = 0; i < 5; ++i) piece(j, j, i);
  auto stage = [&](const int j, const int slot, const bool more, const bool first) __attribute__((always_inline)) {
    LAS const char* sb = lds + slot * K3_STAGE;
    rd(cA, wA, sb, 0);
    SBAR();
    if (!first) {
      acc[0] = MFMA32(wB[0], cB, acc[0]); acc[1] = MFMA32(wB[1], cB, acc[1]);
      if (more) piece(j + 3, (slot + 3) & 3, 0);
      SBAR();
      acc[2] = MFMA32(wB[2], cB, acc[2]); acc[3] = MFMA32(wB[3], cB, acc[3]);
      if (more) piece(j + 3, (slot + 3) & 3, 1);
      SBAR();
      acc[4] = MFMA32(wB[4], cB, acc[4]);
    } else {
      piece(3, 3, 0); piece(3, 3, 1);
    }
    SBAR();
    rd(cB, wB, sb, 1);
    SBAR();
    acc[0] = MFMA32(wA[0], cA, acc[0]);
    if (more) piece(j + 3, (slot + 3) & 3, 2);
    SBAR();
    acc[1] = MFMA32(wA[1], cA, acc[1]); acc[2] = MFMA32(wA[2], cA, acc[2]);
    if (more) piece(j + 3, (slot + 3) & 3, 3);
    SBAR();
    acc[3] = MFMA32(wA[3], cA, acc[3]); acc[4] = MFMA32(wA[4], cA, acc[4]);
    if (more) piece(j + 3, (slot + 3) & 3, 4);
    SBAR();
  };
  WAIT_VL_BAR(10); stage(0, 0, true, true);
  for (int j4 = 1; j4 < 13; j4 += 4) {
    WAIT_VL_BAR(10); stage(j4, 1, true, false);
    WAIT_VL_BAR(10); stage(j4 + 1, 2, true, false);
    WAIT_VL_BAR(10); stage(j4 + 2, 3, true, false);
    WAIT_VL_BAR(10); stage(j4 + 3, 0, true, false);
  }
  WAIT_VL_BAR(10); stage(13, 1, false, false);
  WAIT_VL_BAR(5); stage(14, 2, false, false);
  WAIT_VL_BAR(0); stage(15, 3, false, false);
#pragma unroll
  for (int n = 0; n < 5; ++n) acc[n] = MFMA32(wB[n], cB, acc[n]);
  WAIT_L_BAR();
  {
    LAS char* mine = lds + (tbi * 2 + kp) * 12288 + lane * 16;
    LAS const char* theirs = lds + (tbi * 2 + (kp ^ 1)) * 12288 + lane * 16;
    auto put = [&](const f32x16& a, int slot) {
#pragma unroll
      for (int gq = 0; gq < 4; ++gq)
        *(LAS f32x4*)(mine + (slot * 4 + gq) * 1024) = (f32x4){a[4 * gq], a[4 * gq + 1], a[4 * gq + 2], a[4 * gq + 3]};
    };
    auto fin = [&](const f32x16& a, int slot, int n) {
      f32x16 t;
#pragma unroll
      for (int gq = 0; gq < 4; ++gq) {
        const f32x4 v = *(LAS const f32x4*)(theirs + (slot * 4 + gq) * 1024);
#pragma unroll
        for (int e = 0; e < 4; ++e) t[4 * gq + e] = a[4 * gq + e] + v[e];
      }
      u16* dst = PT + ((size_t)((((ks * 64 + panel * 4 + tbi) * 20 + nt * 5 + n) * 2) * 64 + lane)) * 8;
      *(u32x4*)dst = pack8(t, 0);
      *(u32x4*)(dst + 512) = pack8(t, 8);
    };
    if (kp == 0) { put(acc[3], 0); put(acc[4], 1); } else { put(acc[0], 0); put(acc[1], 1); put(acc[2], 2); }
    WAIT_L_BAR();
    if (kp == 0) { fin(acc[0], 0, 0); fin(acc[1], 1, 1); fin(acc[2], 2, 2); } else { fin(acc[3], 0, 3); fin(acc[4], 1, 4); }
  }
}

__global__ __launch_bounds__(320) void k4_ln(const u16* __restrict__ x, const u16* __restrict__ PT,
                                             float* __restrict__ H, u16* __restrict__ HN, const u16* __restrict__ w1) {
  __shared__ __attribute__((aligned(16))) u16 tile[8 * 648];
  __shared__ float red[2][5][8];
  __shared__ __attribute__((aligned(16))) char dead[5 * 256];
  const int tid = threadIdx.x, lane = tid & 63, wid = RFL(tid >> 6);
  const int b = blockIdx.x;
  const int r8 = lane & 7, hh = (lane >> 3) & 1, sg = (lane >> 4) & 1, ml = lane >> 5;
  const int token = b * 8 + r8, tb32 = token >> 5, rr = token & 31;
  u32x2 xv[2][2];
  u32x4 pv[2][4];
#pragma unroll
  for (int i = 0; i < 2; ++i) {
    const int mb = wid * 4 + 2 * i + ml;
    const int m0 = mb * 32 + sg * 16 + hh * 4;
    xv[i][0] = *(const u32x2*)(x + (size_t)token * DM + m0);
    xv[i][1] = *(const u32x2*)(x + (size_t)token * DM + m0 + 8);
#pragma unroll
    for (int ks = 0; ks < 4; ++ks)
      pv[i][ks] = *(const u32x4*)(PT + ((size_t)((((ks * 64 + tb32) * 20 + mb) * 2 + sg) * 64 + hh * 32 + rr)) * 8);
  }
  SBAR();
  float v[2][8];
  float s = 0.f;
#pragma unroll
  for (int i = 0; i < 2; ++i) {
    const int m0 = (wid * 4 + 2 * i + ml) * 32 + sg * 16 + hh * 4;
#pragma unroll
    for (int q = 0; q < 4; ++q) {
      const unsigned xw = xv[i][q >> 1][q & 1];
      v[i][2 * q] = bflo(xw) + ((bflo(pv[i][0][q]) + bflo(pv[i][1][q])) + (bflo(pv[i][2][q]) + bflo(pv[i][3][q])));
      v[i][2 * q + 1] = bfhi(xw) + ((bfhi(pv[i][0][q]) + bfhi(pv[i][1][q])) + (bfhi(pv[i][2][q]) + bfhi(pv[i][3][q])));
    }
    *(f32x4*)(H + (size_t)token * DM + m0) = (f32x4){v[i][0], v[i][1], v[i][2], v[i][3]};
    *(f32x4*)(H + (size_t)token * DM + m0 + 8) = (f32x4){v[i][4], v[i][5], v[i][6], v[i][7]};
#pragma unroll
    for (int e = 0; e < 8; ++e) s += v[i][e];
  }
  {
    const int u = (b >> 3) * 5 + wid;
    const int a4 = (b & 7) >> 1;
    const unsigned dd = (unsigned)(uintptr_t)(LAS char*)dead + wid * 256;
    const __amdgpu_buffer_rsrc_t r1 = mk_rsrc(w1, SEQ * FF * 2);
#pragma unroll
    for (int i = 0; i < 2; ++i) {
      const int line = (u + 160 * i) * 64 + lane;
      const unsigned off = line < 14336 ? (unsigned)((line / 7) * (FF * 2) + a4 * 896 + (line % 7) * 128) : 0x7ff00000u;
      warm8k(r1, off, 0u, dd);
    }
  }
  s += __shfl_xor(s, 8); s += __shfl_xor(s, 16); s += __shfl_xor(s, 32);
  if (lane < 8) red[0][wid][lane] = s;
  WAIT_L_BAR();
  const float mean = ((red[0][0][r8] + red[0][1][r8]) + (red[0][2][r8] + red[0][3][r8]) + red[0][4][r8]) * (1.0f / DM);
  float sq = 0.f;
#pragma unroll
  for (int i = 0; i < 2; ++i)
#pragma unroll
    for (int e = 0; e < 8; ++e) { v[i][e] -= mean; sq += v[i][e] * v[i][e]; }
  sq += __shfl_xor(sq, 8); sq += __shfl_xor(sq, 16); sq += __shfl_xor(sq, 32);
  if (lane < 8) red[1][wid][lane] = sq;
  WAIT_L_BAR();
  const float var = ((red[1][0][r8] + red[1][1][r8]) + (red[1][2][r8] + red[1][3][r8]) + red[1][4][r8]) * (1.0f / DM);
  const float r1 = 1.0f / sqrtf(var + LN_EPS);
  const float r2 = 1.0f / sqrtf(var * r1 * r1 + LN_EPS);
  const float sc = r1 * r2;
#pragma unroll
  for (int i = 0; i < 2; ++i) {
    const int m0 = (wid * 4 + 2 * i + ml) * 32 + sg * 16 + hh * 4;
    *(u32x2*)(tile + r8 * 648 + m0) = (u32x2){cvtpk(v[i][0] * sc, v[i][1] * sc), cvtpk(v[i][2] * sc, v[i][3] * sc)};
    *(u32x2*)(tile + r8 * 648 + m0 + 8) = (u32x2){cvtpk(v[i][4] * sc, v[i][5] * sc), cvtpk(v[i][6] * sc, v[i][7] * sc)};
  }
  WAIT_L_BAR();
  const int ks32 = b >> 2, g = b & 3;
#pragma unroll
  for (int i = 0; i < 2; ++i) {
    const int m = tid + 320 * i;
    u16 e[8];
#pragma unroll
    for (int j = 0; j < 8; ++j) e[j] = tile[j * 648 + m];
    u32x4 w = {(unsigned)e[0] | ((unsigned)e[1] << 16), (unsigned)e[2] | ((unsigned)e[3] << 16),
               (unsigned)e[4] | ((unsigned)e[5] << 16), (unsigned)e[6] | ((unsigned)e[7] << 16)};
    *(u32x4*)(HN + ((size_t)(((m >> 4) * 64 + ks32) * 64 + g * 16 + (m & 15))) * 8) = w;
  }
}

constexpr int K56_LDS = 163840;

DI float gelu_tanh(float x) {
  const float u = 0.7978845608028654f * (x + 0.044715f * x * x * x);
  const float e = __builtin_amdgcn_exp2f(u * 2.8853900817779268f);
  const float t = 1.0f - 2.0f * __builtin_amdgcn_rcpf(e + 1.0f);
  return 0.5f * x * (1.0f + t);
}

#define ACLOB56 "a0","a1","a2","a3","a4","a5","a6","a7","a8","a9","a10","a11","a12","a13","a14","a15","a16","a17","a18","a19","a20","a21","a22","a23","a24","a25","a26","a27","a28","a29","a30","a31","a32","a33","a34","a35","a36","a37","a38","a39","a40","a41","a42","a43","a44","a45","a46","a47","a48","a49","a50","a51","a52","a53","a54","a55","a56","a57","a58","a59","a60","a61","a62","a63","a64","a65","a66","a67","a68","a69","a70","a71","a72","a73","a74","a75","a76","a77","a78","a79","a80","a81","a82","a83","a84","a85","a86","a87","a88","a89","a90","a91"
template <int R> DI void m_aload(__amdgpu_buffer_rsrc_t rsrc, unsigned voff, unsigned soff) {
  asm volatile("buffer_load_dwordx4 a[%c3:%c4], %0, %1, %2 offen" ::"v"(voff), "s"(rsrc), "s"(soff), "i"(R), "i"(R + 3) : "memory");
}
template <int R, int OFF> DI void m_trread(unsigned addr) {
  asm volatile("ds_read_b64_tr_b16 a[%c1:%c2], %0 offset:%c3" ::"v"(addr), "i"(R), "i"(R + 1), "i"(OFF) : "memory");
}
template <int RA, int RB> DI void m_mfma(f32x4& d) {
  asm volatile("v_mfma_f32_16x16x32_bf16 %0, a[%c1:%c2], a[%c3:%c4], %0" : "+v"(d) : "i"(RA), "i"(RA + 3), "i"(RB), "i"(RB + 3));
}

template <int MODE>
__global__ __launch_bounds__(512, 2) void k56_mix(const u16* __restrict__ W, const u16* __restrict__ AF,
                                                  const u16* __restrict__ bias, const float* __restrict__ H,
                                                  u16* __restrict__ outp, const u16* __restrict__ wnext) {
  constexpr int LDW = MODE == 0 ? FF : SEQ;
  constexpr int NKS = MODE == 0 ? 64 : 54;
  constexpr int NK = MODE == 0 ? 8 : 7;
  constexpr int KROWS = MODE == 0 ? SEQ : FF;
  extern __shared__ __attribute__((aligned(16))) char smem[];
  LAS char* lds = (LAS char*)smem;
  const unsigned lds0 = (unsigned)(uintptr_t)lds;
  const int tid = threadIdx.x, lane = tid & 63;
  const int wid = RFL(tid >> 6);
  const int b = blockIdx.x, xcd = b & 7, idx = b >> 3;
  int wt, dt;
  if (MODE == 0) {
    wt = (xcd >> 1) * 7 + (idx >> 2); dt = (xcd & 1) * 4 + (idx & 3);
    if (idx >= 28 || wt >= 27) {
      if (idx >= 28) {
        const __amdgpu_buffer_rsrc_t r2 = mk_rsrc(wnext, SEQ * FF * 2);
#pragma unroll
        for (int i = 0; i < 7; ++i) {
          const int line = (((idx - 28) * 8 + wid) * 7 + i) * 64 + lane;
          const unsigned off = line < 13824 ? (unsigned)((line >> 3) * 4096 + (xcd >> 1) * 1024 + (line & 7) * 128) : 0x7ff00000u;
          warm8k(r2, off, 0u, lds0 + wid * 256);
        }
      }
      return;
    }
  } else {
    wt = (xcd >> 1) * 8 + (idx >> 2); dt = (xcd & 1) * 4 + (idx & 3);
  }
  const __amdgpu_buffer_rsrc_t rW = mk_rsrc(W, KROWS * LDW * 2);
  const __amdgpu_buffer_rsrc_t rA = mk_rsrc(AF, 40 * NKS * 1024);
  const unsigned vlin = lane * 16;
  const int prow = (lane >> 2) & 7;
  const unsigned voffW0 = (unsigned)(prow * LDW * 2 + (lane >> 5) * 64 +
                                     (((lane & 3) ^ (MODE == 1 ? (((prow >> 2) & 1) << 1) : 0)) << 4));
  const unsigned voffW1 = MODE == 1 ? voffW0 : (voffW0 ^ 32u);
  const unsigned myl = lds0 + wid * 20480;
  const int g = lane >> 4, q4 = (lane & 15) >> 2, p4 = lane & 3;
  unsigned wb[2][2];
#pragma unroll
  for (int t = 0; t < 2; ++t)
#pragma unroll
    for (int hf = 0; hf < 2; ++hf) {
      const int kb = MODE == 0 ? g : 2 * t + (g >> 1);
      const int row = MODE == 0 ? 4 * t + q4 : 4 * (g & 1) + q4;
      const int sw = MODE == 0 ? (kb & 1) : ((row >> 2) & 1);
      wb[t][hf] = myl + kb * 1024 + row * 64 + ((hf ^ sw) * 32) + p4 * 8;
    }
  f32x4 acc[4][5];
#pragma unroll
  for (int i = 0; i < 4; ++i)
#pragma unroll
    for (int d = 0; d < 5; ++d) acc[i][d] = (f32x4){0.f, 0.f, 0.f, 0.f};
  u32x2 eb0[2], eb1[2];
  f32x4 eh[3]; u16 ebs[3];
#define KSTEP(IT) (wid + 8 * (IT))
#define WDMA(IT, KB) dma1(rW, ((KB) & 1) ? voffW1 : voffW0, (unsigned)((KSTEP(IT) * 32 + (KB) * 8) * LDW * 2 + wt * 128), myl + ((IT) % 5) * 4096 + (KB) * 1024)
#define ALOAD(IT, D) m_aload<((IT) % 3) * 20 + 4 * (D)>(rA, vlin, (unsigned)(((dt * 5 + (D)) * NKS + KSTEP(IT)) * 1024))
#define WDMA4(IT) do { WDMA(IT, 0); WDMA(IT, 1); WDMA(IT, 2); WDMA(IT, 3); } while (0)
#define ALOAD5(IT) do { ALOAD(IT, 0); ALOAD(IT, 1); ALOAD(IT, 2); ALOAD(IT, 3); ALOAD(IT, 4); } while (0)
#define WREAD1(IT, I) do { m_trread<60 + 16 * ((IT) & 1) + 4 * (I), ((IT) % 5) * 4096 + ((I) >> 1) * 512>(wb[0][(I) & 1]); \
                           m_trread<60 + 16 * ((IT) & 1) + 4 * (I) + 2, ((IT) % 5) * 4096 + ((I) >> 1) * 512>(wb[1][(I) & 1]); } while (0)
#define WREAD(IT) do { WREAD1(IT, 0); WREAD1(IT, 1); WREAD1(IT, 2); WREAD1(IT, 3); } while (0)
#define MM(IT, I, D) do { if constexpr (MODE == 0) m_mfma<60 + 16 * ((IT) & 1) + 4 * (I), ((IT) % 3) * 20 + 4 * (D)>(acc[I][D]); \
                          else m_mfma<((IT) % 3) * 20 + 4 * (D), 60 + 16 * ((IT) & 1) + 4 * (I)>(acc[I][D]); } while (0)
#define MMROW(IT, I) do { MM(IT, I, 0); MM(IT, I, 1); MM(IT, I, 2); MM(IT, I, 3); MM(IT, I, 4); } while (0)
#define NV(IT) (((IT) + 4 < NK ? 4 : 0) + ((IT) + 2 < NK ? 5 : 0))
#define ALLOW(IT) ((IT) == 0 ? (5 + (NK > 2 ? 4 : 0) + (NK > 3 ? 4 : 0)) : (IT) == 1 ? ((NK > 3 ? 4 : 0) + NV(0)) : NV((IT) - 1))
#define STEP(IT) do { if constexpr ((IT) < NK) { \
    asm volatile("s_waitcnt vmcnt(%c0) lgkmcnt(0)" ::"i"(ALLOW(IT)) : "memory", ACLOB56); \
    if constexpr ((IT) + 1 < NK) WREAD((IT) + 1); \
    SBAR(); \
    MMROW(IT, 0); if constexpr ((IT) + 4 < NK) { WDMA((IT) + 4, 0); WDMA((IT) + 4, 1); } SBAR(); \
    MMROW(IT, 1); if constexpr ((IT) + 4 < NK) { WDMA((IT) + 4, 2); WDMA((IT) + 4, 3); } SBAR(); \
    MMROW(IT, 2); if constexpr ((IT) + 2 < NK) { ALOAD((IT) + 2, 0); ALOAD((IT) + 2, 1); ALOAD((IT) + 2, 2); } SBAR(); \
    MMROW(IT, 3); if constexpr ((IT) + 2 < NK) { ALOAD((IT) + 2, 3); ALOAD((IT) + 2, 4); } SBAR(); \
    } } while (0)
  asm volatile("" ::: "memory", ACLOB56);
  WDMA4(0); ALOAD5(0); asm volatile("s_barrier" ::: "memory");
  WDMA4(1); ALOAD5(1); asm volatile("s_barrier" ::: "memory");
  WDMA4(2); WDMA4(3);
  asm volatile("s_waitcnt vmcnt(22)" ::: "memory");
  WREAD(0);
  STEP(0); STEP(1); STEP(2); STEP(3); STEP(4);
  if (MODE == 0) {
#pragma unroll
    for (int i = 0; i < 2; ++i) {
      const int u = wid + 8 * i < 10 ? wid + 8 * i : wid;
      const int f0 = wt * 64 + (u / 5) * 32 + 4 * g;
      eb0[i] = *(const u32x2*)(bias + f0); eb1[i] = *(const u32x2*)(bias + f0 + 16);
    }
  } else {
#pragma unroll
    for (int i = 0; i < 3; ++i) {
      const int u = wid + 8 * i < 20 ? wid + 8 * i : wid;
      const int s = wt * 64 + (u / 5) * 16 + (lane & 15), d0 = dt * 80 + (u % 5) * 16 + 4 * g;
      ebs[i] = bias[s];
      eh[i] = *(const f32x4*)(H + (size_t)s * DM + d0);
    }
  }
  SBAR();
  STEP(5); STEP(6); STEP(7);
  asm volatile("s_nop 15" ::: "memory");
#undef KSTEP
#undef WDMA
#undef ALOAD
#undef WDMA4
#undef ALOAD5
#undef WREAD1
#undef WREAD
#undef MM
#undef MMROW
#undef NV
#undef ALLOW
#undef STEP
  WAIT_L0();
  {
    LAS char* dst = lds + wid * 20480 + lane * 16;
#pragma unroll
    for (int i = 0; i < 4; ++i)
#pragma unroll
      for (int d = 0; d < 5; ++d) *(LAS f32x4*)(dst + (i * 5 + d) * 1024) = acc[i][d];
  }
  WAIT_L_BAR();
  if (MODE == 0) {
#pragma unroll
    for (int i = 0; i < 2; ++i) {
      const int u = wid + 8 * i;
      if (u < 10) {
        const int a = u / 5, d = u % 5;
        f32x4 t0 = {0.f, 0.f, 0.f, 0.f}, t1 = {0.f, 0.f, 0.f, 0.f};
#pragma unroll
        for (int w = 0; w < 8; ++w) {
          t0 += *(LAS const f32x4*)(lds + w * 20480 + ((2 * a) * 5 + d) * 1024 + lane * 16);
          t1 += *(LAS const f32x4*)(lds + w * 20480 + ((2 * a + 1) * 5 + d) * 1024 + lane * 16);
        }
        const u32x2 b0 = eb0[i], b1 = eb1[i];
        t0 += (f32x4){bflo(b0[0]), bfhi(b0[0]), bflo(b0[1]), bfhi(b0[1])};
        t1 += (f32x4){bflo(b1[0]), bfhi(b1[0]), bflo(b1[1]), bfhi(b1[1])};
        u32x4 w = {cvtpk(gelu_tanh(t0[0]), gelu_tanh(t0[1])), cvtpk(gelu_tanh(t0[2]), gelu_tanh(t0[3])),
                   cvtpk(gelu_tanh(t1[0]), gelu_tanh(t1[1])), cvtpk(gelu_tanh(t1[2]), gelu_tanh(t1[3]))};
        *(u32x4*)(outp + ((size_t)(((dt * 5 + d) * 54 + wt * 2 + a) * 64 + lane)) * 8) = w;
      }
    }
  } else {
#pragma unroll
    for (int i = 0; i < 3; ++i) {
      const int u = wid + 8 * i;
      if (u < 20) {
        f32x4 t0 = {0.f, 0.f, 0.f, 0.f};
#pragma unroll
        for (int w = 0; w < 8; ++w) t0 += *(LAS const f32x4*)(lds + w * 20480 + u * 1024 + lane * 16);
        const int s = wt * 64 + (u / 5) * 16 + (lane & 15);
        const int d0 = dt * 80 + (u % 5) * 16 + 4 * g;
        const float bb = bf2f(ebs[i]);
        const f32x4 hv = eh[i];
        u32x2 w = {cvtpk(t0[0] + bb + hv[0], t0[1] + bb + hv[1]), cvtpk(t0[2] + bb + hv[2], t0[3] + bb + hv[3])};
        *(u32x2*)(outp + (size_t)s * DM + d0) = w;
      }
    }
  }
}

}

extern "C" void kernel_launch(void* const* d_in, const int* in_sizes, int n_in, void* d_out, int out_size, void* d_ws,
                              size_t ws_size, hipStream_t stream) {
  (void)in_sizes; (void)n_in; (void)out_size; (void)ws_size;
  const u16* x = (const u16*)d_in[0];
  const u16* ckv = (const u16*)d_in[1];
  const u16* wq = (const u16*)d_in[2];
  const u16* wuk = (const u16*)d_in[3];
  const u16* wuv = (const u16*)d_in[4];
  const u16* wo = (const u16*)d_in[5];
  const u16* w1 = (const u16*)d_in[7];
  const u16* b1 = (const u16*)d_in[8];
  const u16* w2 = (const u16*)d_in[9];
  const u16* b2 = (const u16*)d_in[10];
  char* ws = (char*)d_ws;
  u16* XN = (u16*)(ws + WS_XN);
  u16* KF = (u16*)(ws + WS_KF);
  u16* VF = (u16*)(ws + WS_VF);
  u16* CF = (u16*)(ws + WS_CF);
  u16* PT = (u16*)(ws + WS_PT);
  float* H = (float*)(ws + WS_H);
  u16* HN = (u16*)(ws + WS_HN);
  u16* UF = (u16*)(ws + WS_UF);
  (void)hipFuncSetAttribute((const void*)k1_kvproj, hipFuncAttributeMaxDynamicSharedMemorySize, K1_LDS);
  (void)hipFuncSetAttribute((const void*)k2_attn, hipFuncAttributeMaxDynamicSharedMemorySize, K2_LDS);
  (void)hipFuncSetAttribute((const void*)k3_oproj, hipFuncAttributeMaxDynamicSharedMemorySize, K3_LDS);
  (void)hipFuncSetAttribute((const void*)k56_mix<0>, hipFuncAttributeMaxDynamicSharedMemorySize, K56_LDS);
  (void)hipFuncSetAttribute((const void*)k56_mix<1>, hipFuncAttributeMaxDynamicSharedMemorySize, K56_LDS);
  k1_kvproj<<<256, 512, K1_LDS, stream>>>(x, ckv, wuk, wuv, KF, VF, XN, wq);
  k2_attn<<<256, 512, K2_LDS, stream>>>(XN, wq, KF, VF, CF, (float*)(ws + WS_LAM), wo);
  k3_oproj<<<256, 512, K3_LDS, stream>>>(CF, wo, PT);
  k4_ln<<<256, 320, 0, stream>>>(x, PT, H, HN, w1);
  k56_mix<0><<<256, 512, K56_LDS, stream>>>(w1, HN, b1, nullptr, UF, w2);
  k56_mix<1><<<256, 512, K56_LDS, stream>>>(w2, UF, b2, H, (u16*)d_out, nullptr);
}
```
